```python
import torch

def _softmax(x, axis=-1):
    return torch.softmax(x, dim=axis)
def _gelu(x):
    return torch.nn.functional.gelu(x, approximate="tanh")
def _silu(x):
    return torch.nn.functional.silu(x)
def _sigmoid(x):
    return torch.sigmoid(x)
def _layernorm(x, axis=-1, eps=1e-5):
    m = x.mean(dim=axis, keepdim=True)
    v = x.var(dim=axis, keepdim=True, unbiased=False)
    return (x - m) / torch.sqrt(v + eps)
def _scatter_add(tgt, idx, src, axis=0):
    return tgt.clone().index_add_(axis, idx, src)
def _segment_sum(src, seg, n):
    out = torch.zeros((n,) + src.shape[1:], dtype=src.dtype)
    return out.index_add_(0, seg, src)
def _pad(x, pw):
    flat = tuple(v for pair in reversed(pw) for v in pair)
    return torch.nn.functional.pad(x, flat)

kernel_name = 'recurrent_scan__sliding_block_cached__dense_activation_silu__prompt'

def setup_inputs():

    _w = (lambda a: (lambda t: t.to(torch.bfloat16) if t.is_floating_point() else t)(torch.as_tensor(a)))
    def _A_setup():
        B, Sq, D = 1, 2048, 576
        Hq, Hkv, Sk, Dh = 32, 16, 2048, 128
        assert Hq % Hkv == 0, (Hq, Hkv)
        return {
            "x": _w((lambda *s: torch.randn(*s, dtype=torch.bfloat16) * 0.05)(B, Sq, D)),
            "k_cache": _w((lambda *s: torch.randn(*s, dtype=torch.bfloat16) * 1.6)(B, Hkv, Sk, Dh)),
            "v_cache": _w((lambda *s: torch.randn(*s, dtype=torch.bfloat16) * 0.8)(B, Hkv, Sk, Dh)),
            "wq": _w((lambda *s: torch.randn(*s, dtype=torch.bfloat16) * 1.6)(D, Hq * Dh) * (D ** -0.5)),
            "wo": _w((lambda *s: torch.randn(*s, dtype=torch.bfloat16) * 0.8)(Hq * Dh, D) * ((Hq * Dh) ** -0.5)),
        }
    _A_in = _A_setup()

    def _B_setup():
        B, S, D, F = 1, 2048, 576, 4608
        return {
            "x": _w((lambda *s: torch.randn(*s, dtype=torch.bfloat16) * 0.05)(B, S, D)),
            "w1": _w((lambda *s: torch.randn(*s, dtype=torch.bfloat16) * 1.6)(D, F) * (D ** -0.5)),
            "w2": _w((lambda *s: torch.randn(*s, dtype=torch.bfloat16) * 0.2)(F, D) * (F ** -0.5)),
        }
    _B_in = _B_setup()

    return {
        "A_x": _A_in["x"],
        "A_k_cache": _A_in["k_cache"],
        "A_v_cache": _A_in["v_cache"],
        "A_wq": _A_in["wq"],
        "A_wo": _A_in["wo"],
        "B_x": _B_in["x"],
        "B_w1": _B_in["w1"],
        "B_w2": _B_in["w2"],
    }

def reference(A_x, A_k_cache, A_v_cache, A_wq, A_wo, B_x, B_w1, B_w2):
    def _A_ref(x, k_cache, v_cache, wq, wo):
        B, Sq, D = x.shape
        Hq, Dh = 32, 128
        Hkv, Sk = k_cache.shape[1], k_cache.shape[2]
        W = min(4096, max(256, Sk // 4))
        rep = Hq // Hkv
        q = (lambda x, *d: x.permute(*d))((x @ wq).reshape(B, Sq, Hq, Dh), 0, 2, 1, 3)
        k = (lambda x, n, axis: torch.repeat_interleave(x, n, dim=axis))(k_cache[:, :, Sk - W:, :], rep, axis=1)
        v = (lambda x, n, axis: torch.repeat_interleave(x, n, dim=axis))(v_cache[:, :, Sk - W:, :], rep, axis=1)
        scores = torch.einsum("bhqd,bhkd->bhqk", q, k) * (Dh ** -0.5)
        p = _softmax(scores, axis=-1)
        o = torch.einsum("bhqk,bhkd->bhqd", p, v)
        return (lambda x, *d: x.permute(*d))(o, 0, 2, 1, 3).reshape(B, Sq, Hq * Dh) @ wo

    def _B_ref(x, w1, w2):
        h = torch.einsum("bsd,df->bsf", x, w1)
        h = _silu(h)
        out = torch.einsum("bsf,fd->bsd", h, w2)
        return out

    x = A_x
    xn = _layernorm(x, axis=-1)
    s = _A_ref(xn, A_k_cache, A_v_cache, A_wq, A_wo)
    g = _silu(_B_ref(xn, B_w1, B_w2))
    return x + s * g
```

```cpp
#include <hip/hip_runtime.h>
#include <hip/hip_bf16.h>
#include <stdint.h>
typedef __bf16 bf16x8 __attribute__((ext_vector_type(8)));
typedef float f32x4 __attribute__((ext_vector_type(4)));
typedef unsigned short u16;
constexpr int S=2048, D=576, HQ=32, HKV=16, SK=2048, DH=128, W=512, F=4608;
__device__ inline float bf2f(u16 v){ return __uint_as_float(((unsigned)v)<<16); }
__device__ inline u16 f2bf(float f){ __bf16 b=(__bf16)f; return __builtin_bit_cast(u16,b); }
__device__ inline float rbf(float f){ return bf2f(f2bf(f)); }

__global__ void ln_kernel(const u16* x, u16* xn){
  int row=blockIdx.x; int t=threadIdx.x;
  float v[9]; float s=0;
  for(int i=0;i<9;i++){ v[i]=bf2f(x[row*D+i*64+t]); s+=v[i]; }
  for(int o=32;o;o>>=1) s+=__shfl_xor(s,o);
  float m=s/D; float q=0;
  for(int i=0;i<9;i++){ float d=v[i]-m; q+=d*d; }
  for(int o=32;o;o>>=1) q+=__shfl_xor(q,o);
  float mb=rbf(m), vb=rbf(q/D); float den=rbf(sqrtf(rbf(vb+1e-5f)));
  for(int i=0;i<9;i++) xn[row*D+i*64+t]=f2bf(rbf(v[i]-mb)/den);
}
template<int MODE>
__global__ void gemm_kernel(const u16* A,const u16* B,u16* C,int M,int N,int K){
  int lane=threadIdx.x&63, w=threadIdx.x>>6;
  int m0=(blockIdx.y*4+w)*16, n0=blockIdx.x*64;
  f32x4 acc[4]={};
  for(int k0=0;k0<K;k0+=32){
    bf16x8 a=*(const bf16x8*)(A+(size_t)(m0+(lane&15))*K+k0+8*(lane>>4));
    for(int j=0;j<4;j++){
      bf16x8 b;
      for(int e=0;e<8;e++) b[e]=__builtin_bit_cast(__bf16,B[(size_t)(k0+8*(lane>>4)+e)*N+n0+j*16+(lane&15)]);
      acc[j]=__builtin_amdgcn_mfma_f32_16x16x32_bf16(a,b,acc[j],0,0,0);
    }
  }
  for(int j=0;j<4;j++) for(int r=0;r<4;r++){
    float v=acc[j][r];
    if(MODE==1){ v=rbf(v); v=v/(1.f+__expf(-v)); }
    C[(size_t)(m0+(lane>>4)*4+r)*N+n0+j*16+(lane&15)]=f2bf(v);
  }
}
__global__ __launch_bounds__(256) void attn_kernel(const u16* Q,const u16* Kc,const u16* Vc,u16* O){
  __shared__ float sc[16][W+4];
  __shared__ u16 pb[16][W+8];
  int h=blockIdx.x, q0=blockIdx.y*16, lane=threadIdx.x&63, w=threadIdx.x>>6; int hk=h/2;
  const u16* Kp=Kc+((size_t)hk*SK+(SK-W))*DH; const u16* Vp=Vc+((size_t)hk*SK+(SK-W))*DH;
  bf16x8 a[4];
  for(int s=0;s<4;s++) a[s]=*(const bf16x8*)(Q+(size_t)(q0+(lane&15))*(HQ*DH)+h*DH+s*32+8*(lane>>4));
  for(int kb=w;kb<W/16;kb+=4){
    f32x4 acc={};
    for(int s=0;s<4;s++){ bf16x8 b=*(const bf16x8*)(Kp+(size_t)(kb*16+(lane&15))*DH+s*32+8*(lane>>4));
      acc=__builtin_amdgcn_mfma_f32_16x16x32_bf16(a[s],b,acc,0,0,0);}
    for(int r=0;r<4;r++) sc[(lane>>4)*4+r][kb*16+(lane&15)]=acc[r]*0.08838834764831845f;
  }
  __syncthreads();
  for(int r=w*4;r<w*4+4;r++){
    float m=-1e30f; for(int i=lane;i<W;i+=64) m=fmaxf(m,sc[r][i]);
    for(int o=32;o;o>>=1) m=fmaxf(m,__shfl_xor(m,o));
    float s=0; for(int i=lane;i<W;i+=64){ float e=__expf(sc[r][i]-m); sc[r][i]=e; s+=e;}
    for(int o=32;o;o>>=1) s+=__shfl_xor(s,o);
    for(int i=lane;i<W;i+=64) pb[r][i]=f2bf(sc[r][i]/s);
  }
  __syncthreads();
  for(int db=w;db<8;db+=4){
    f32x4 acc={};
    _Pragma("unroll 1") for(int k0=0;k0<W;k0+=32){
      bf16x8 p=*(const bf16x8*)(&pb[lane&15][k0+8*(lane>>4)]);
      bf16x8 b; for(int e=0;e<8;e++) b[e]=__builtin_bit_cast(__bf16,Vp[(size_t)(k0+8*(lane>>4)+e)*DH+db*16+(lane&15)]);
      acc=__builtin_amdgcn_mfma_f32_16x16x32_bf16(p,b,acc,0,0,0);
    }
    for(int r=0;r<4;r++) O[(size_t)(q0+(lane>>4)*4+r)*(HQ*DH)+h*DH+db*16+(lane&15)]=f2bf(acc[r]);
  }
}
__global__ void final_kernel(const u16* x,const u16* s,const u16* g,u16* out,int n){
  int i=blockIdx.x*256+threadIdx.x; if(i<n) out[i]=f2bf(bf2f(x[i])+rbf(bf2f(s[i])*bf2f(g[i])));
}
extern "C" void kernel_launch(void* const* d_in,const int* in_sizes,int n_in,void* d_out,int out_size,void* d_ws,size_t ws_size,hipStream_t stream){
  const u16* x=(const u16*)d_in[0]; const u16* kc=(const u16*)d_in[1]; const u16* vc=(const u16*)d_in[2];
  const u16* wq=(const u16*)d_in[3]; const u16* wo=(const u16*)d_in[4]; const u16* w1=(const u16*)d_in[6]; const u16* w2=(const u16*)d_in[7];
  u16* ws=(u16*)d_ws; u16* xn=ws; u16* q=xn+S*D; u16* h=q+S*HQ*DH; u16* o=h+S*F; u16* s=o+S*HQ*DH; u16* g=s+S*D;
  ln_kernel<<<S,64,0,stream>>>(x,xn);
  gemm_kernel<0><<<dim3(HQ*DH/64,S/64),256,0,stream>>>(xn,wq,q,S,HQ*DH,D);
  gemm_kernel<1><<<dim3(F/64,S/64),256,0,stream>>>(xn,w1,h,S,F,D);
  attn_kernel<<<dim3(HQ,S/16),256,0,stream>>>(q,kc,vc,o);
  gemm_kernel<0><<<dim3(D/64,S/64),256,0,stream>>>(o,wo,s,S,D,HQ*DH);
  gemm_kernel<1><<<dim3(D/64,S/64),256,0,stream>>>(h,w2,g,S,D,F);
  final_kernel<<<(S*D+255)/256,256,0,stream>>>(x,s,g,(u16*)d_out,S*D);
}
```

```cpp
#include <hip/hip_runtime.h>
#include <stdint.h>

__global__ __launch_bounds__(256, 1) __attribute__((aligned(4096))) void main_kernel(const void* x, const void* kc, const void* vc, const void* wq,
                                                      const void* wo, const void* w1, const void* w2, void* ws, void* out) {
  __shared__ __attribute__((aligned(1024))) char smem[163840];
  const unsigned wv = __builtin_amdgcn_readfirstlane(threadIdx.x >> 6) + (unsigned)(uintptr_t)smem;
  asm volatile(
".macro WL n\ns_waitcnt lgkmcnt(\\n)\n.endm\n.macro LD a,r,o,i\n"
"buffer_load_dwordx4 v[\\a], s[\\r:\\r+3], s[\\o] offen offset:\\i lds\n.endm\n.macro AW d,a\n"
"v_accvgpr_write_b32 a[\\d], v[\\a]\n.endm\n.macro Mvavz d,a,b\n"
"v_mfma_f32_16x16x32_bf16 v[\\d:\\d+3], a[\\a:\\a+3], v[\\b:\\b+3], 0\n.endm\n.macro Mvav d,a,b\n"
"v_mfma_f32_16x16x32_bf16 v[\\d:\\d+3], a[\\a:\\a+3], v[\\b:\\b+3], v[\\d:\\d+3]\n.endm\n.macro Mvvvz d,a,b\n"
"v_mfma_f32_16x16x32_bf16 v[\\d:\\d+3], v[\\a:\\a+3], v[\\b:\\b+3], 0\n.endm\n.macro Mvvv d,a,b\n"
"v_mfma_f32_16x16x32_bf16 v[\\d:\\d+3], v[\\a:\\a+3], v[\\b:\\b+3], v[\\d:\\d+3]\n.endm\n.macro CV d,a,b\n"
"v_cvt_pk_bf16_f32 v[\\d], v[\\a], v[\\b]\n.endm\n.macro Mvvvc d,a,b,c\n"
"v_mfma_f32_16x16x32_bf16 v[\\d:\\d+3], v[\\a:\\a+3], v[\\b:\\b+3], v[\\c:\\c+3]\n.endm\n.macro Mvaa d,a,b\n"
"v_mfma_f32_16x16x32_bf16 v[\\d:\\d+3], a[\\a:\\a+3], a[\\b:\\b+3], v[\\d:\\d+3]\n.endm\n.macro DT f,b,o\n"
"ds_read_b64_tr_b16 a[\\f:\\f+1], v[\\b] offset:\\o\n.endm\n.macro Mvvac d,a,b,c\n"
"v_mfma_f32_16x16x32_bf16 v[\\d:\\d+3], v[\\a:\\a+3], a[\\b:\\b+3], v[\\c:\\c+3]\n.endm\n.macro RC d,a\n"
"v_rcp_f32 v[\\d], v[\\a]\n.endm\n.macro ML d,a,b\nv_mul_f32 v[\\d], v[\\a], v[\\b]\n.endm\n.macro EX d,a\n"
"v_exp_f32 v[\\d], v[\\a]\n.endm\n.macro DK f,b,o\nds_read_b64 a[\\f:\\f+1], v[\\b] offset:\\o\n.endm\n"
".macro Mvaaz d,a,b\nv_mfma_f32_16x16x32_bf16 v[\\d:\\d+3], a[\\a:\\a+3], a[\\b:\\b+3], 0\n.endm\n"
".macro Mvaac d,a,b,c\nv_mfma_f32_16x16x32_bf16 v[\\d:\\d+3], a[\\a:\\a+3], a[\\b:\\b+3], v[\\c:\\c+3]\n.endm\n"
".macro Maavz d,a,b\nv_mfma_f32_16x16x32_bf16 a[\\d:\\d+3], a[\\a:\\a+3], v[\\b:\\b+3], 0\n.endm\n"
".macro Maav d,a,b\nv_mfma_f32_16x16x32_bf16 a[\\d:\\d+3], a[\\a:\\a+3], v[\\b:\\b+3], a[\\d:\\d+3]\n.endm\n"
".macro Mvvaz d,a,b\nv_mfma_f32_16x16x32_bf16 v[\\d:\\d+3], v[\\a:\\a+3], a[\\b:\\b+3], 0\n.endm\n"
".macro Mvva d,a,b\nv_mfma_f32_16x16x32_bf16 v[\\d:\\d+3], v[\\a:\\a+3], a[\\b:\\b+3], v[\\d:\\d+3]\n.endm\n"
"Lstart_%=:\ns_load_dwordx2 s[36:37], %[ka], 0\ns_bfe_u32 s79, %[bid], 0x10003\ns_lshl_b32 s90, s79, 4\n"
"s_add_u32 s90, s90, 32\ns_load_dwordx2 s[52:53], %[ka], 8\ns_load_dwordx2 s[56:57], %[ka], 16\n"
"s_load_dwordx2 s[40:41], %[ka], 24\ns_load_dwordx2 s[44:45], %[ka], 40\ns_load_dwordx2 s[60:61], %[ka], 56\n"
"s_load_dwordx2 s[34:35], %[ka], 64\ns_load_dwordx2 s[48:49], %[ka], s90\ns_and_b32 s90, %[bid], 3\n"
"s_lshl_b32 s90, s90, 2\ns_bfe_u32 s81, %[bid], 0x20003\ns_or_b32 s81, s81, s90\ns_bfe_u32 s90, %[bid], 0x10002\n"
"s_lshl_b32 s90, s90, 3\ns_lshr_b32 s91, %[bid], 5\ns_add_u32 s90, s90, s91\ns_lshl_b32 s90, s90, 2\n"
"s_add_u32 s76, s90, %[wv]\ns_mov_b32 s77, %[wv]\ns_mul_i32 s72, %[wv], 40960\ns_mul_i32 s92, s76, 36864\n"
"s_mov_b32 s38, 36864\ns_mov_b32 s39, 0x20000\ns_mul_i32 s90, s81, 18\nv_mbcnt_lo_u32_b32 v16, -1, 0\n"
"v_mbcnt_hi_u32_b32 v16, -1, v16\nv_min_u32 v25, 17, v16\nv_add_u32 v25, s90, v25\nv_lshlrev_b32 v25, 7, v25\n"
"v_lshrrev_b32 v17, 3, v16\nv_and_b32 v18, 7, v16\nv_lshrrev_b32 v19, 1, v17\nv_xor_b32 v19, v18, v19\n"
"v_mul_u32_u24 v20, 1152, v17\nv_lshl_add_u32 v104, v19, 4, v20\nv_xor_b32 v105, 64, v104\n"
"v_add_u32 v105, 0x2000, v105\ns_getpc_b64 s[30:31]\n"
"Lpc_%=:\ns_sub_u32 s30, s30, Lpc_%=-Lstart_%=\ns_subb_u32 s31, s31, 0\nWL 0\ns_add_u32 s36, s36, s92\n"
"s_addc_u32 s37, s37, 0\ns_mov_b32 m0, s72\ns_mul_i32 s84, s79, 18432\nLD 104,36,84,0\nLD 105,36,84,1024\n"
"s_addk_i32 m0, 2048\ns_addk_i32 s84, 128\nLD 104,36,84,0\nLD 105,36,84,1024\ns_addk_i32 m0, 2048\n"
"s_addk_i32 s84, 128\nLD 104,36,84,0\nLD 105,36,84,1024\ns_addk_i32 m0, 2048\ns_addk_i32 s84, 128\n"
"LD 104,36,84,0\nLD 105,36,84,1024\ns_mul_i32 s87, s77, 1760256\ns_load_dword s83, s[44:45], s87\n"
"s_mul_i32 s87, s77, 1564672\ns_load_dword s83, s[40:41], s87\ns_lshl_b32 s87, s81, 19\n"
"s_add_u32 s87, s87, 393216\ns_load_dword s83, s[52:53], s87\ns_load_dword s83, s[56:57], s87\n"
"s_and_b32 s87, s81, 14\ns_lshl1_add_u32 s87, s87, s77\ns_lshl4_add_u32 s88, s79, 128\ns_mul_i32 s87, s87, s88\n"
"s_mul_i32 s87, s87, 1152\ns_load_dword s83, s[48:49], s87\ns_mov_b32 s87, s92\ns_load_dword s83, s[34:35], s87\n"
"s_mul_i32 s90, s79, Lhub_%=-Lw1_%=\ns_add_u32 s90, s90, Lw1_%=-Lstart_%=\ns_add_u32 s6, s30, s90\n"
"s_addc_u32 s7, s31, 0\ns_lshl_b32 s90, s77, 12\ns_add_u32 s6, s6, s90\ns_addc_u32 s7, s7, 0\n"
"s_load_dword s83, s[6:7], 0x0\ns_load_dword s83, s[6:7], 0x4000\ns_load_dword s83, s[6:7], 0x8000\n"
"s_load_dword s83, s[6:7], 0xc000\nv_lshrrev_b32 v21, 4, v16\nv_and_b32 v22, 15, v16\nv_lshlrev_b32 v13, 4, v16\n"
"v_lshrrev_b32 v19, 1, v22\nv_xor_b32 v19, v19, v21\nv_lshlrev_b32 v20, 7, v22\nv_lshl_add_u32 v19, v19, 4, v20\n"
"v_add_u32 v23, s72, v19\nv_xor_b32 v24, 64, v23\nv_mov_b32 v19, 0x3f803f80\nAW 236,19\nAW 237,19\nAW 238,19\n"
"AW 239,19\ns_add_u32 s34, s34, s92\ns_addc_u32 s35, s35, 0\ns_lshl_b32 s93, s76, 4\ns_add_u32 s93, s93, s81\n"
"s_lshl_b32 s93, s93, 7\ns_lshl_b32 s90, s76, 21\ns_add_u32 s90, s90, 0x1fffff\ns_add_u32 s60, s60, s90\n"
"s_addc_u32 s61, s61, 0\ns_and_b32 s60, s60, 0xffe00000\ns_lshl_b32 s90, s81, 4\ns_add_u32 s98, s60, 1835008\n"
"s_add_u32 s96, s98, 0x2000\ns_add_u32 s94, s96, s90\ns_mov_b32 s99, s61\ns_mov_b32 s95, s61\n"
"s_mov_b32 s97, s61\ns_lshr_b32 s91, s81, 1\ns_mul_i32 s90, s91, 1152\ns_mul_i32 s92, s79, 640\n"
"s_add_u32 s90, s90, s92\ns_add_u32 s44, s44, s90\ns_addc_u32 s45, s45, 0\ns_sub_u32 s46, 5308416, s90\n"
"s_mov_b32 s47, 0x20000\nv_and_b32 v19, 6, v17\nv_xor_b32 v18, v18, v19\ns_and_b32 s90, s77, 1\n"
"s_lshl_b32 s90, s90, 4\ns_lshr_b32 s92, s77, 1\ns_lshl_b32 s92, s92, 2\ns_add_u32 s90, s90, s92\n"
"v_and_b32 v19, 4, v17\nv_add_u32 v19, v19, v17\nv_add_u32 v19, s90, v19\nv_mul_u32_u24 v20, 9216, v19\n"
"v_lshl_add_u32 v245, v18, 4, v20\nv_lshlrev_b32 v20, 13, v19\nv_lshl_add_u32 v244, v18, 4, v20\n"
"s_lshl_b32 s90, s77, 3\nv_add_u32 v19, s90, v17\nv_mul_u32_u24 v20, 1152, v19\nv_lshl_add_u32 v12, v18, 4, v20\n"
"v_lshlrev_b32 v20, 8, v19\nv_lshl_add_u32 v11, v18, 4, v20\ns_lshl_b32 s90, s77, 2\nv_add_u32 v19, s90, v21\n"
"v_xor_b32 v20, v22, v21\nv_lshlrev_b32 v19, 8, v19\nv_lshl_add_u32 v10, v20, 4, v19\ns_lshl_b32 s90, s81, 9\n"
"v_lshrrev_b32 v19, 2, v22\ns_add_u32 s40, s40, s90\ns_addc_u32 s41, s41, 0\nv_mul_u32_u24 v19, 41024, v19\n"
"s_sub_u32 s42, 4718592, s90\nv_and_b32 v20, 3, v22\ns_lshl_b32 s90, s81, 19\nv_lshl_add_u32 v19, v20, 8, v19\n"
"s_mov_b64 exec, 0\ns_swappc_b64 s[74:75], s[6:7]\n"
"Lret0_%=:\ns_mov_b64 exec, -1\ns_add_u32 s6, s6, 0x4000\ns_addc_u32 s7, s7, 0\n"
"global_load_dword v172, v13, s[98:99] sc1\nglobal_load_dword v173, v13, s[96:97] sc1\ns_xor_b32 s96, s94, 16\n"
"s_addk_i32 m0, 2048\ns_addk_i32 s84, 128\nLD 104,36,84,0\nLD 105,36,84,1024\ns_waitcnt vmcnt(10)\n"
"ds_read_b128 v[32:35], v23 offset:0\nds_read_b128 v[40:43], v24 offset:0\ns_addk_i32 m0, 2048\n"
"s_addk_i32 s84, 128\nLD 104,36,84,0\nLD 105,36,84,1024\ns_mov_b64 exec, 0\ns_swappc_b64 s[74:75], s[6:7]\n"
"Lret1_%=:\ns_mov_b64 exec, -1\ns_add_u32 s6, s6, 0x4000\ns_addc_u32 s7, s7, 0\ns_waitcnt vmcnt(10)\n"
"ds_read_b128 v[48:51], v23 offset:2048\nds_read_b128 v[56:59], v24 offset:2048\nWL 2\nMvavz 176,236,32\n"
"s_add_u32 s90, s90, 393216\nv_lshrrev_b32 v9, 1, v21\nMvav 176,236,40\ns_add_u32 s52, s52, s90\n"
"s_addc_u32 s53, s53, 0\nv_xor_b32 v20, v20, v9\nMvvvz 184,32,32\ns_add_u32 s56, s56, s90\n"
"s_addc_u32 s57, s57, 0\nv_lshl_add_u32 v19, v20, 4, v19\nMvvv 184,40,40\ns_mov_b32 s54, 131072\n"
"v_and_b32 v20, 1, v21\ns_addk_i32 m0, 2048\ns_addk_i32 s84, 128\nLD 104,36,84,0\nLD 105,36,84,1024\n"
"s_waitcnt vmcnt(10)\nds_read_b128 v[64:67], v23 offset:4096\nds_read_b128 v[72:75], v24 offset:4096\nWL 2\n"
"Mvav 176,236,48\ns_mov_b32 s58, 131072\nv_lshl_add_u32 v8, v20, 3, v19\nMvav 176,236,56\n"
"s_mul_i32 s50, s79, 73728\nv_xor_b32 v9, 32, v8\nMvvv 184,48,48\ns_add_u32 s50, s50, 589824\n"
"v_lshrrev_b32 v19, 2, v22\nMvvv 184,56,56\ns_mul_i32 s90, s91, s50\nv_lshl_add_u32 v25, v19, 4, v22\n"
"s_addk_i32 m0, 2048\ns_addk_i32 s84, 128\nLD 104,36,84,0\nLD 105,36,84,1024\ns_mov_b64 exec, 0\n"
"s_swappc_b64 s[74:75], s[6:7]\n"
"Lret2_%=:\ns_mov_b64 exec, -1\ns_add_u32 s6, s6, 0x4000\ns_addc_u32 s7, s7, 0\ns_waitcnt vmcnt(10)\n"
"ds_read_b128 v[80:83], v23 offset:6144\nds_read_b128 v[88:91], v24 offset:6144\nWL 2\nMvav 176,236,64\n"
"s_add_u32 s48, s48, s90\ns_addc_u32 s49, s49, 0\nv_lshlrev_b32 v25, 2, v25\nMvav 176,236,72\n"
"s_mul_i32 s90, s91, 49152\nv_cmp_eq_u32 vcc, v19, v21\nMvvv 184,64,64\ns_add_u32 s90, s90, 1179648\n"
"v_and_b32 v20, 1, v22\nMvvv 184,72,72\ns_add_u32 s64, s60, s90\nv_lshlrev_b32 v20, 4, v20\ns_addk_i32 m0, 2048\n"
"s_addk_i32 s84, 128\nLD 104,36,84,0\nLD 105,36,84,1024\ns_waitcnt vmcnt(8)\n"
"ds_read_b128 v[96:99], v23 offset:8192\nds_read_b128 v[128:131], v24 offset:8192\nWL 2\nMvav 176,236,80\n"
"s_mov_b32 s65, s61\nv_mov_b32 v26, 0x3f80\nMvav 176,236,88\ns_mov_b32 s66, 36864\nv_lshlrev_b32 v20, v20, v26\n"
"Mvvv 184,80,80\ns_lshl_b32 s90, s91, 15\nv_cndmask_b32 v20, 0, v20, vcc\nMvvv 184,88,88\n"
"s_add_u32 s90, s90, 1572864\nv_and_b32 v19, 2, v22\ns_addk_i32 m0, 2048\ns_sub_u32 s84, 19456, s84\n"
"LD 104,36,84,0\nLD 105,36,84,1024\ns_cmp_ge_u32 s77, 3\ns_cbranch_scc1 Lret3_%=\ns_mov_b64 exec, 0\n"
"s_swappc_b64 s[74:75], s[6:7]\n"
"Lret3_%=:\ns_mov_b64 exec, -1\ns_add_u32 s6, s6, 0x4000\ns_addc_u32 s7, s7, 0\ns_waitcnt vmcnt(8)\n"
"ds_read_b128 v[132:135], v23 offset:10240\nds_read_b128 v[136:139], v24 offset:10240\nWL 2\nMvav 176,236,96\n"
"s_add_u32 s68, s60, s90\nv_cmp_eq_u32 vcc, 0, v19\nMvav 176,236,128\ns_mov_b32 s69, s61\nv_mov_b32 v26, 0\n"
"Mvvv 184,96,96\ns_movk_i32 s70, 24576\nv_mov_b32 v27, 0\nMvvv 184,128,128\ns_and_b32 s90, s81, 8\n"
"v_mov_b32 v30, 0\ns_addk_i32 m0, 2048\ns_addk_i32 s84, 128\nLD 104,36,84,0\nLD 105,36,84,1024\n"
"s_waitcnt vmcnt(8)\nds_read_b128 v[140:143], v23 offset:12288\nds_read_b128 v[144:147], v24 offset:12288\nWL 2\n"
"Mvav 176,236,132\ns_add_u32 s90, s90, s81\nv_mov_b32 v31, 0\nMvav 176,236,136\ns_lshl_b32 s90, s90, 10\n"
"v_cndmask_b32 v28, 0, v20, vcc\nMvvv 184,132,132\ns_add_u32 s60, s60, s90\nv_sub_u32 v29, v20, v28\n"
"Mvvv 184,136,136\ns_mov_b32 s62, 1147904\nv_bfe_u32 v17, v16, 2, 3\ns_addk_i32 m0, 2048\ns_addk_i32 s84, 128\n"
"LD 104,36,84,0\nLD 105,36,84,1024\ns_waitcnt vmcnt(8)\nds_read_b128 v[148:151], v23 offset:14336\n"
"ds_read_b128 v[152:155], v24 offset:14336\nWL 2\nMvav 176,236,140\ns_mov_b32 s43, 0x20000\n"
"v_lshrrev_b32 v18, 5, v16\nMvav 176,236,144\ns_mov_b32 s51, 0x20000\nv_mul_u32_u24 v18, 40960, v18\n"
"Mvvv 184,140,140\ns_mov_b32 s55, 0x20000\nv_lshl_add_u32 v18, v17, 7, v18\nMvvv 184,144,144\n"
"s_mov_b32 s59, 0x20000\nv_and_b32 v19, 3, v16\ns_addk_i32 m0, 2048\ns_addk_i32 s84, 128\nLD 104,36,84,0\n"
"LD 105,36,84,1024\ns_waitcnt vmcnt(8)\nds_read_b128 v[156:159], v23 offset:16384\n"
"ds_read_b128 v[160:163], v24 offset:16384\nWL 2\nMvav 176,236,148\ns_mov_b32 s63, 0x20000\n"
"v_lshl_add_u32 v18, v19, 3, v18\nMvav 176,236,152\ns_mov_b32 s67, 0x20000\nv_lshrrev_b32 v17, 1, v17\n"
"Mvvv 184,148,148\ns_mov_b32 s71, 0x20000\nv_lshl_add_u32 v0, v17, 5, v18\nMvvv 184,152,152\n"
"s_mul_i32 s73, s77, 41024\nv_xor_b32 v1, 32, v0\ns_addk_i32 m0, 2048\ns_addk_i32 s84, 128\nLD 104,36,84,0\n"
"LD 105,36,84,1024\ns_waitcnt vmcnt(8)\nds_read_b128 v[36:39], v23 offset:18432\n"
"ds_read_b128 v[44:47], v24 offset:18432\nWL 2\nMvav 176,236,156\ns_mov_b32 s89, 0xbf317218\n"
"v_xor_b32 v2, 64, v0\nMvav 176,236,160\ns_and_b32 s76, s76, 63\nv_xor_b32 v3, 96, v0\nMvvv 184,156,156\n"
"s_mul_i32 s76, s76, 1024\nv_add_u32 v4, 81920, v0\nMvvv 184,160,160\ns_mov_b32 s0, 0x22222222\n"
"v_add_u32 v5, 81920, v1\ns_addk_i32 m0, 2048\ns_addk_i32 s84, 128\nLD 104,36,84,0\nLD 105,36,84,1024\n"
"s_mov_b32 s1, 0x22222222\nv_add_u32 v6, 81920, v2\ns_mov_b32 s2, 0x44444444\nv_add_u32 v7, 81920, v3\n"
"s_mov_b32 s3, 0x44444444\nv_min_u32 v17, 7, v16\ns_mov_b32 s4, 0x88888888\nv_lshlrev_b32 v15, 7, v17\n"
"s_mov_b32 s5, 0x88888888\nv_bfe_u32 v17, v16, 3, 1\nv_mul_u32_u24 v17, 8064, v17\nv_add_u32 v109, v17, v13\n"
"v_min_u32 v17, 1, v16\nv_lshlrev_b32 v17, 17, v17\nv_add_u32 v17, s93, v17\n"
"v_min_u32 v17, Lend_%=-Lstart_%=, v17\ns_nop 3\nv_mul_f32 v250, 0xbae38e39, v176\nCV 232,250,250\n"
"v_lshlrev_b32 v233, 16, v232\nv_lshlrev_b32 v234, 16, v232\nv_lshlrev_b32 v235, 16, v232\n"
"v_lshlrev_b32 v232, 16, v232\ns_nop 1\nMvvvc 192,28,32,232\nMvvvc 196,26,32,232\nMvvvc 200,28,40,232\n"
"Mvvvc 204,26,40,232\ns_waitcnt vmcnt(8)\nds_read_b128 v[52:55], v23 offset:20480\n"
"ds_read_b128 v[60:63], v24 offset:20480\ns_nop 0\nWL 2\nMvavz 180,236,36\nCV 32,192,193\nCV 33,194,195\n"
"Mvav 180,236,44\nCV 34,196,197\nCV 35,198,199\nMvvvz 188,36,36\nCV 40,200,201\nCV 41,202,203\nMvvv 188,44,44\n"
"CV 42,204,205\nCV 43,206,207\ns_addk_i32 m0, 2048\ns_addk_i32 s84, 128\nLD 104,36,84,0\nLD 105,36,84,1024\n"
"Mvvvc 208,28,48,232\nMvvvc 212,26,48,232\nMvvvc 216,28,56,232\nMvvvc 220,26,56,232\ns_waitcnt vmcnt(8)\n"
"ds_read_b128 v[68:71], v23 offset:22528\nds_read_b128 v[76:79], v24 offset:22528\ns_nop 0\nWL 2\n"
"Mvav 180,236,52\nCV 48,208,209\nCV 49,210,211\nMvav 180,236,60\nCV 50,212,213\nCV 51,214,215\nMvvv 188,52,52\n"
"CV 56,216,217\nCV 57,218,219\nMvvv 188,60,60\nCV 58,220,221\nCV 59,222,223\ns_addk_i32 m0, 2048\n"
"s_addk_i32 s84, 128\nLD 104,36,84,0\nLD 105,36,84,1024\nMvvvc 192,28,64,232\nMvvvc 196,26,64,232\n"
"Mvvvc 200,28,72,232\nMvvvc 204,26,72,232\ns_waitcnt vmcnt(8)\nds_read_b128 v[84:87], v23 offset:24576\n"
"ds_read_b128 v[92:95], v24 offset:24576\ns_nop 0\nWL 2\nMvav 180,236,68\nCV 64,192,193\nCV 65,194,195\n"
"Mvav 180,236,76\nCV 66,196,197\nCV 67,198,199\nMvvv 188,68,68\nCV 72,200,201\nCV 73,202,203\nMvvv 188,76,76\n"
"CV 74,204,205\nCV 75,206,207\ns_addk_i32 m0, 2048\ns_addk_i32 s84, 128\nLD 104,36,84,0\nLD 105,36,84,1024\n"
"Mvvvc 208,28,80,232\nMvvvc 212,26,80,232\nMvvvc 216,28,88,232\nMvvvc 220,26,88,232\ns_waitcnt vmcnt(8)\n"
"ds_read_b128 v[100:103], v23 offset:26624\nds_read_b128 a[136:139], v24 offset:26624\ns_nop 0\nWL 2\n"
"Mvav 180,236,84\nCV 80,208,209\nCV 81,210,211\nMvav 180,236,92\nCV 82,212,213\nCV 83,214,215\nMvvv 188,84,84\n"
"CV 88,216,217\nCV 89,218,219\nMvvv 188,92,92\nCV 90,220,221\nCV 91,222,223\ns_add_u32 m0, s72, 0\n"
"s_mov_b32 s84, 0\nbuffer_load_dwordx4 v245, s[44:47], s84 offen lds\ns_add_u32 m0, s72, 1024\n"
"s_mov_b32 s84, 294912\nbuffer_load_dwordx4 v245, s[44:47], s84 offen lds\ns_add_u32 m0, s72, 2048\n"
"s_mov_b32 s84, 589824\nbuffer_load_dwordx4 v245, s[44:47], s84 offen lds\ns_add_u32 m0, s72, 3072\n"
"s_mov_b32 s84, 884736\nbuffer_load_dwordx4 v245, s[44:47], s84 offen lds\ns_add_u32 m0, s72, 4096\n"
"s_mov_b32 s84, 1179648\nbuffer_load_dwordx4 v245, s[44:47], s84 offen lds\ns_add_u32 m0, s72, 5120\n"
"s_mov_b32 s84, 1474560\nbuffer_load_dwordx4 v245, s[44:47], s84 offen lds\ns_add_u32 m0, s72, 6144\n"
"s_mov_b32 s84, 1769472\nbuffer_load_dwordx4 v245, s[44:47], s84 offen lds\ns_add_u32 m0, s72, 7168\n"
"s_mov_b32 s84, 2064384\nbuffer_load_dwordx4 v245, s[44:47], s84 offen lds\ns_add_u32 m0, s72, 8192\n"
"s_mov_b32 s84, 2359296\nbuffer_load_dwordx4 v245, s[44:47], s84 offen lds\nMvvvc 192,28,96,232\n"
"Mvvvc 196,26,96,232\nMvvvc 200,28,128,232\nMvvvc 204,26,128,232\ns_waitcnt vmcnt(15)\n"
"ds_read_b128 a[140:143], v23 offset:28672\nds_read_b128 a[144:147], v24 offset:28672\ns_nop 0\nWL 2\n"
"Mvav 180,236,100\nCV 96,192,193\nCV 97,194,195\nMvaa 180,236,136\nCV 98,196,197\nCV 99,198,199\n"
"Mvvv 188,100,100\nCV 128,200,201\nCV 129,202,203\nMvaa 188,136,136\nCV 130,204,205\nCV 131,206,207\n"
"Mvvvc 208,28,132,232\nMvvvc 212,26,132,232\nMvvvc 216,28,136,232\nMvvvc 220,26,136,232\ns_waitcnt vmcnt(13)\n"
"ds_read_b128 a[148:151], v23 offset:30720\nds_read_b128 a[152:155], v24 offset:30720\ns_nop 0\nWL 2\n"
"Mvaa 180,236,140\nCV 132,208,209\nCV 133,210,211\nMvaa 180,236,144\nCV 134,212,213\nCV 135,214,215\n"
"Mvaa 188,140,140\nCV 136,216,217\nCV 137,218,219\nMvaa 188,144,144\nCV 138,220,221\nCV 139,222,223\n"
"Mvvvc 192,28,140,232\nMvvvc 196,26,140,232\nMvvvc 200,28,144,232\nMvvvc 204,26,144,232\ns_waitcnt vmcnt(11)\n"
"ds_read_b128 a[156:159], v23 offset:32768\nds_read_b128 a[160:163], v24 offset:32768\ns_nop 0\nWL 2\n"
"Mvaa 180,236,148\nCV 140,192,193\nCV 141,194,195\nMvaa 180,236,152\nCV 142,196,197\nCV 143,198,199\n"
"Mvaa 188,148,148\nCV 144,200,201\nCV 145,202,203\nMvaa 188,152,152\nCV 146,204,205\nCV 147,206,207\n"
"s_cmp_eq_u32 s79, 1\ns_cbranch_scc1 Lodd_%=\nMvvvc 208,28,148,232\nMvvvc 212,26,148,232\nMvvvc 216,28,152,232\n"
"Mvvvc 220,26,152,232\ns_waitcnt vmcnt(9)\nds_read_b128 a[164:167], v23 offset:34816\n"
"ds_read_b128 a[168:171], v24 offset:34816\ns_nop 0\nWL 2\nMvaa 180,236,156\nCV 148,208,209\nCV 149,210,211\n"
"Mvaa 180,236,160\nCV 150,212,213\nCV 151,214,215\nMvaa 188,156,156\nCV 152,216,217\nCV 153,218,219\n"
"Mvaa 188,160,160\nCV 154,220,221\nCV 155,222,223\nglobal_load_dword v14, v17, s[30:31]\ns_xor_b32 s90, s81, 1\n"
"s_lshl_b32 s91, s81, 7\nv_readlane_b32 s80, v173, s90\ns_add_u32 s74, s98, 16384\ns_add_u32 s74, s74, s91\n"
"s_mov_b32 s75, s99\ns_mov_b64 exec, 0xffff\nglobal_store_dwordx4 v109, a[236:239], s[74:75] sc1\n"
"s_mov_b64 exec, -1\nMvvvc 192,28,156,232\nMvvvc 196,26,156,232\nMvvvc 200,28,160,232\nMvvvc 204,26,160,232\n"
"WL 0\ns_nop 3\nMvaa 180,236,164\nCV 156,192,193\nCV 157,194,195\ns_nop 0\n"
"Lw1_%=:\ns_cbranch_execz Lret0_%=\nMvaa 180,236,168\nCV 158,196,197\nCV 159,198,199\nMvaa 188,164,164\n"
"CV 160,200,201\nCV 161,202,203\nMvaa 188,168,168\nCV 162,204,205\nCV 163,206,207\n"
"v_mul_f32 v251, 0xbae38e39, v180\nCV 236,251,251\nv_lshlrev_b32 v237, 16, v236\nv_lshlrev_b32 v238, 16, v236\n"
"v_lshlrev_b32 v239, 16, v236\nv_lshlrev_b32 v236, 16, v236\ns_nop 1\nMvvvc 192,28,36,236\n"
"s_add_u32 m0, s72, 9216\nv_cndmask_b32 v252, v184, v185, s[0:1]\nv_cndmask_b32 v252, v252, v186, s[2:3]\n"
"s_mov_b32 s84, 2654208\ns_mov_b32 s85, 293888\nMvvvc 196,26,36,236\ns_waitcnt vmcnt(2)\nLD 245,44,84,0\n"
"s_add_u32 s84, s84, s85\nMvvvc 200,28,44,236\ns_barrier\nv_cndmask_b32 v253, v188, v189, s[0:1]\n"
"v_cndmask_b32 v252, v252, v187, s[4:5]\nMvvvc 204,26,44,236\ns_mov_b32 s86, 261120\n"
"ds_bpermute_b32 v174, v25, v252\nv_cndmask_b32 v253, v253, v190, s[2:3]\nMvvvc 208,28,52,236\n"
"s_add_u32 s93, s76, 0\nLD 245,44,84,1024\nMvvvc 212,26,52,236\ns_add_u32 s84, s84, s85\n"
"v_cndmask_b32 v253, v253, v191, s[4:5]\nCV 36,192,193\nMvvvc 216,28,60,236\ns_mov_b32 s4, 1.0\n"
"ds_bpermute_b32 v175, v25, v253\nDT 172,0,0\nLD 245,44,84,2048\nMvvvc 220,26,60,236\ns_add_u32 s84, s84, s85\n"
"DT 174,4,0\nCV 37,194,195\nMvvvc 224,28,68,236\ns_mov_b32 s5, 1.0\nDT 176,0,1024\nCV 38,196,197\n"
"Mvvvc 228,26,68,236\ns_mov_b32 s87, 32768\nLD 245,44,84,3072\nMvvvc 192,28,76,236\ns_mov_b32 s84, 3833856\n"
"DT 178,4,1024\nCV 39,198,199\nMvvvc 196,26,76,236\ns_addk_i32 m0, 4096\nDT 180,0,2048\nCV 44,200,201\n"
"CV 45,202,203\nMvvvc 200,28,84,236\ns_mov_b32 s88, 33792\nLD 245,44,84,0\nCV 46,204,205\nCV 47,206,207\n"
"Mvvvc 204,26,84,236\ns_add_u32 s84, s84, s85\nDT 182,4,2048\nCV 52,208,209\nCV 53,210,211\nMvvvc 208,28,92,236\n"
"DT 184,0,3072\nCV 54,212,213\nCV 55,214,215\nMvvvc 212,26,92,236\nLD 245,44,84,1024\nCV 60,216,217\n"
"CV 61,218,219\nMvvvc 216,28,100,236\ns_add_u32 s84, s84, s85\nDT 186,4,3072\nDT 188,0,4096\nCV 62,220,221\n"
"CV 63,222,223\nMvvvc 220,26,100,236\nDT 190,4,4096\nCV 68,224,225\nCV 69,226,227\nMvvac 224,28,136,236\n"
"LD 245,44,84,2048\nCV 70,228,229\nCV 71,230,231\nMvvac 228,26,136,236\ns_add_u32 s84, s84, s85\nDT 192,0,5120\n"
"DT 194,4,5120\nCV 76,192,193\nCV 77,194,195\nMvvac 192,28,140,236\nDT 196,0,6144\nCV 78,196,197\nCV 79,198,199\n"
"Mvvac 196,26,140,236\nLD 245,44,84,3072\nCV 84,200,201\nCV 85,202,203\nMvvac 200,28,144,236\n"
"s_mov_b32 s84, 5013504\nDT 198,4,6144\nDT 200,0,7168\nCV 86,204,205\nCV 87,206,207\nMvvac 204,26,144,236\n"
"s_addk_i32 m0, 4096\nDT 202,4,7168\nCV 92,208,209\nCV 93,210,211\nMvvac 208,28,148,236\nLD 245,44,84,0\n"
"CV 94,212,213\nCV 95,214,215\nMvvac 212,26,148,236\nWL 6\nDT 204,0,8192\nDT 206,4,8192\nCV 100,216,217\n"
"CV 101,218,219\nMvvac 216,28,152,236\ns_addk_i32 m0, 1024\nDT 208,1,0\nCV 102,220,221\nCV 103,222,223\n"
"Mvvac 220,26,152,236\ns_movk_i32 s84, 0\nDT 210,5,0\nCV 224,224,225\nMvavz 104,172,32\nDT 212,1,1024\n"
"CV 226,226,227\nMvav 104,176,40\nDT 214,5,1024\nAW 136,224\nMvav 104,180,48\nWL 6\nDT 216,1,2048\nAW 137,226\n"
"Mvav 104,184,56\nDT 218,5,2048\nCV 228,228,229\nMvav 104,188,64\nDT 220,1,3072\nCV 230,230,231\n"
"Mvav 104,192,72\nWL 6\nDT 222,5,3072\nAW 138,228\nMvav 104,196,80\nDT 224,1,4096\nAW 139,230\nMvav 104,200,88\n"
"DT 226,5,4096\nCV 192,192,193\nMvav 104,204,96\nDT 228,1,5120\nCV 194,194,195\nMvavz 108,172,36\nDT 230,5,5120\n"
"AW 140,192\nMvav 108,176,44\nDT 232,1,6144\nAW 141,194\nMvav 108,180,52\nDT 234,5,6144\nCV 196,196,197\n"
"Mvav 108,184,60\nLD 244,40,84,0\nMvav 108,188,68\ns_add_u32 s84, s84, s86\nDT 240,1,7168\nDT 242,5,7168\n"
"Mvav 108,192,76\nDT 244,1,8192\nCV 198,198,199\nMvav 108,196,84\nDT 246,5,8192\nAW 142,196\nMvav 108,200,92\n"
"DT 248,2,0\nAW 143,198\nMvav 108,204,100\nLD 244,40,84,1024\nMvvac 224,28,156,236\ns_add_u32 s84, s84, s86\n"
"DT 250,6,0\nDT 252,2,1024\nMvvac 228,26,156,236\nWL 7\nDT 254,6,1024\nCV 200,200,201\nMvvac 192,28,160,236\n"
"DT 172,2,2048\nCV 202,202,203\nMvvac 196,26,160,236\nDT 174,6,2048\nAW 144,200\nMvavz 112,208,32\n"
"LD 244,40,84,2048\nMvav 112,212,40\ns_add_u32 s84, s84, s86\nDT 176,2,3072\nDT 178,6,3072\nMvav 112,216,48\n"
"DT 180,2,4096\nAW 145,202\nMvav 112,220,56\nDT 182,6,4096\nCV 204,204,205\nMvav 112,224,64\nWL 6\n"
"DT 184,2,5120\nCV 206,206,207\nMvav 112,228,72\nLD 244,40,84,3072\nMvav 112,232,80\ns_mov_b32 s84, 1048576\n"
"DT 186,6,5120\nDT 188,2,6144\nMvav 112,240,88\ns_addk_i32 m0, 4096\nDT 190,6,6144\nAW 146,204\nMvav 112,244,96\n"
"DT 192,2,7168\nAW 147,206\nMvavz 116,208,36\nDT 194,6,7168\nCV 208,208,209\nMvav 116,212,44\nLD 244,40,84,0\n"
"Mvav 116,216,52\ns_add_u32 s84, s84, s86\nDT 196,2,8192\nDT 198,6,8192\nMvav 116,220,60\nDT 200,3,0\n"
"CV 210,210,211\nMvav 116,224,68\nDT 202,7,0\nAW 148,208\nMvav 116,228,76\nDT 204,3,1024\nAW 149,210\n"
"Mvav 116,232,84\nLD 244,40,84,1024\nMvav 116,240,92\ns_add_u32 s84, s84, s86\nDT 206,7,1024\nDT 208,3,2048\n"
"Mvav 116,244,100\nDT 210,7,2048\nCV 212,212,213\nMvvac 200,28,164,236\nDT 212,3,3072\nCV 214,214,215\n"
"Mvvac 204,26,164,236\nWL 6\nDT 214,7,3072\nAW 150,212\nMvavz 120,248,32\nLD 244,40,84,2048\nMvav 120,252,40\n"
"s_add_u32 s84, s84, s86\nDT 216,3,4096\nDT 218,7,4096\nMvav 120,172,48\nDT 220,3,5120\nAW 151,214\n"
"Mvav 120,176,56\nDT 222,7,5120\nCV 216,216,217\nMvav 120,180,64\nDT 224,3,6144\nCV 218,218,219\n"
"Mvav 120,184,72\nLD 244,40,84,3072\nMvav 120,188,80\ns_mov_b32 s84, 2097152\nDT 226,7,6144\nDT 228,3,7168\n"
"Mvav 120,192,88\ns_addk_i32 m0, 4096\nDT 230,7,7168\nCV 220,220,221\nMvav 120,196,96\ns_waitcnt vmcnt(8)\n"
"DT 232,3,8192\nCV 222,222,223\nMvavz 124,248,36\ns_barrier\nDT 234,7,8192\nAW 152,216\nMvav 124,252,44\n"
"LD 244,40,84,0\nMvav 124,172,52\ns_mov_b32 s84, 2359296\nDT 240,0,9216\nDT 242,4,9216\nMvav 124,176,60\n"
"s_addk_i32 m0, 5120\nDT 244,0,10240\nAW 153,218\nMvav 124,180,68\nDT 246,4,10240\nAW 154,220\nMvav 124,184,76\n"
"DT 248,0,11264\nAW 155,222\nMvav 124,188,84\nDT 250,4,11264\nCV 224,224,225\nMvav 124,192,92\nDT 252,0,12288\n"
"CV 226,226,227\nMvav 124,196,100\nWL 7\nDT 254,4,12288\nCV 228,228,229\nMvvac 208,28,168,236\nDT 172,0,13312\n"
"CV 230,230,231\nMvvac 212,26,168,236\nDT 174,4,13312\nAW 156,224\nMvavz 164,200,32\nDT 176,0,14336\nAW 157,226\n"
"Mvav 164,204,40\nDT 178,4,14336\nAW 158,228\nMvav 164,208,48\nDT 180,0,15360\nAW 159,230\nMvav 164,212,56\n"
"DT 182,4,15360\nCV 192,192,193\nMvav 164,216,64\nDT 184,0,16384\nCV 194,194,195\nMvav 164,220,72\n"
"DT 186,4,16384\nCV 196,196,197\nMvav 164,224,80\nDT 188,0,17408\nCV 198,198,199\nMvav 164,228,88\n"
"DT 190,4,17408\nAW 160,192\nMvav 164,232,96\nDT 192,1,9216\nAW 161,194\nMvavz 168,200,36\nDT 194,5,9216\n"
"AW 162,196\nMvav 168,204,44\nLD 244,40,84,0\nMvav 168,208,52\ns_add_u32 s84, s84, s86\nDT 196,1,10240\n"
"DT 198,5,10240\nMvav 168,212,60\nDT 200,1,11264\nAW 163,198\nMvav 168,216,68\nDT 202,5,11264\nCV 200,200,201\n"
"Mvav 168,220,76\nDT 204,1,12288\nCV 202,202,203\nMvav 168,224,84\nWL 6\nLD 244,40,84,1024\nMvav 168,228,92\n"
"s_add_u32 s84, s84, s86\nDT 206,5,12288\nDT 208,1,13312\nMvav 168,232,100\nDT 210,5,13312\nCV 204,204,205\n"
"Mvav 104,240,128\nDT 212,1,14336\nCV 206,206,207\nMvav 104,244,132\nDT 214,5,14336\nAW 164,200\n"
"Mvav 104,248,136\nLD 244,40,84,2048\nMvav 104,252,140\ns_add_u32 s84, s84, s86\nDT 216,1,15360\nDT 218,5,15360\n"
"Mvav 104,172,144\nDT 220,1,16384\nAW 165,202\nMvav 104,176,148\nDT 222,5,16384\nAW 166,204\nMvav 104,180,152\n"
"DT 224,1,17408\nAW 167,206\nMvav 104,184,156\nDT 226,5,17408\nCV 208,208,209\nMvav 104,188,160\nDT 228,2,9216\n"
"CV 210,210,211\nMvaa 108,240,136\nDT 230,6,9216\nCV 212,212,213\nMvaa 108,244,140\nDT 232,2,10240\n"
"CV 214,214,215\nMvaa 108,248,144\nDT 234,6,10240\nAW 168,208\nMvaa 108,252,148\nDT 240,2,11264\nAW 169,210\n"
"Mvaa 108,172,152\nDT 242,6,11264\nAW 170,212\nMvaa 108,176,156\nDT 244,2,12288\nAW 171,214\nMvaa 108,180,160\n"
"WL 6\nLD 244,40,84,3072\nMvaa 108,184,164\ns_mov_b32 s84, 3407872\nDT 246,6,12288\nDT 248,2,13312\n"
"Mvaa 108,188,168\ns_addk_i32 m0, 4096\nDT 250,6,13312\nv_mul_f32 v174, 0x3ae38e39, v174\nMvav 112,192,128\n"
"DT 252,2,14336\nv_fma_f32 v174, -v250, v250, v174\nMvav 112,196,132\nLD 244,40,84,0\nMvav 112,200,136\n"
"s_add_u32 s84, s84, s86\nDT 254,6,14336\nDT 172,2,15360\nMvav 112,204,140\nDT 174,6,15360\n"
"v_max_f32 v174, 0, v174\nMvav 112,208,144\nDT 176,2,16384\nCV 174,174,174\nMvav 112,212,148\nLD 244,40,84,1024\n"
"Mvav 112,216,152\ns_add_u32 s84, s84, s86\nDT 178,6,16384\nDT 180,2,17408\nMvav 112,220,156\nDT 182,6,17408\n"
"v_lshlrev_b32 v174, 16, v174\nMvav 112,224,160\nDT 184,3,9216\nv_add_f32 v174, 0x3727c5ac, v174\n"
"Mvaa 116,192,136\nLD 244,40,84,2048\nMvaa 116,196,140\ns_add_u32 s84, s84, s86\nDT 186,7,9216\nDT 188,3,10240\n"
"Mvaa 116,200,144\nDT 190,7,10240\nCV 174,174,174\nMvaa 116,204,148\nDT 192,3,11264\n"
"v_lshlrev_b32 v174, 16, v174\nMvaa 116,208,152\nLD 244,40,84,3072\nMvaa 116,212,156\ns_mov_b32 s84, 4456448\n"
"DT 194,7,11264\nDT 196,3,12288\nMvaa 116,216,160\nWL 6\nv_sqrt_f32 v174, v174\nMvaa 116,220,164\n"
"DT 198,7,12288\nDT 200,3,13312\nMvaa 116,224,168\ns_addk_i32 m0, 4096\nDT 202,7,13312\nCV 174,174,174\n"
"Mvav 120,228,128\nLD 244,40,84,0\nMvav 120,232,132\ns_movk_i32 s84, 128\nDT 204,3,14336\nDT 206,7,14336\n"
"Mvav 120,240,136\ns_add_u32 m0, s72, 0\nDT 208,3,15360\nv_lshlrev_b32 v174, 16, v174\nMvav 120,244,140\n"
"RC 174,174\nMvav 120,248,144\nDT 210,7,15360\nDT 212,3,16384\nMvav 120,252,148\nDT 214,7,16384\n"
"v_mul_f32 v240, 0xbfb8aa3b, v174\nMvav 120,172,152\ns_waitcnt vmcnt(9)\nDT 216,3,17408\n"
"v_mul_f32 v241, 0xbfb8aa3b, v174\nMvav 120,176,156\ns_barrier\nDT 218,7,17408\nML 104,104,240\n"
"Mvav 120,180,160\ns_load_dword s83, s[74:75], 0x800\nLD 245,44,84,0\nMvaa 124,228,136\ns_add_u32 s84, s84, s85\n"
"DT 220,0,18432\nDT 222,4,18432\nMvaa 124,232,140\ns_mov_b64 s[6:7], 0\nDT 224,0,19456\nML 105,105,241\n"
"Mvaa 124,240,144\n"
"Lw2_%=:\ns_cbranch_execz Lret0_%=\ns_mov_b32 s78, 0\nDT 226,4,19456\nML 106,106,240\nMvaa 124,244,148\n"
"s_bfe_u32 s3, s74, 0x10015\nDT 228,0,20480\nML 107,107,241\nMvaa 124,248,152\ns_lshl_b32 s82, s3, 13\n"
"DT 230,4,20480\nv_mul_f32 v175, 0x3ae38e39, v175\nMvaa 124,252,156\ns_lshl_b64 s[0:1], 1, s81\nEX 246,104\n"
"Mvaa 124,172,160\nWL 6\nDT 232,0,21504\nDT 234,4,21504\nMvaa 124,176,164\nLD 245,44,84,1024\nMvaa 124,180,168\n"
"s_add_u32 s84, s84, s85\nDT 240,0,22528\nDT 242,4,22528\nMvav 164,184,128\nEX 247,105\nMvav 164,188,132\n"
"DT 244,0,23552\nDT 246,4,23552\nMvav 164,192,136\nEX 248,106\nMvav 164,196,140\nDT 248,0,24576\nDT 250,4,24576\n"
"Mvav 164,200,144\nLD 245,44,84,2048\nMvav 164,204,148\ns_add_u32 s84, s84, s85\nDT 252,0,25600\nDT 254,4,25600\n"
"Mvav 164,208,152\nEX 249,107\nMvav 164,212,156\nDT 172,0,26624\nDT 174,4,26624\nMvav 164,216,160\n"
"DT 176,1,18432\nv_fma_f32 v175, -v251, v251, v175\nMvaa 168,184,136\nDT 178,5,18432\nv_max_f32 v175, 0, v175\n"
"Mvaa 168,188,140\nDT 180,1,19456\nCV 175,175,175\nMvaa 168,192,144\nLD 245,44,84,3072\nMvaa 168,196,148\n"
"s_mov_b32 s84, 1179776\nDT 182,5,19456\nDT 184,1,20480\nMvaa 168,200,152\ns_addk_i32 m0, 4096\nDT 186,5,20480\n"
"v_add_f32 v246, 1.0, v246\nMvaa 168,204,156\nDT 188,1,21504\nv_add_f32 v247, 1.0, v247\nMvaa 168,208,160\nWL 6\n"
"DT 190,5,21504\nv_add_f32 v248, 1.0, v248\nMvaa 168,212,164\nDT 192,1,22528\nv_add_f32 v249, 1.0, v249\n"
"Mvaa 168,216,168\nDT 194,5,22528\nv_lshlrev_b32 v175, 16, v175\nMvavz 176,220,32\nLD 245,44,84,0\n"
"Mvav 176,224,40\ns_add_u32 s84, s84, s85\nDT 196,1,23552\nDT 198,5,23552\nMvav 176,228,48\nRC 246,246\n"
"Mvav 176,232,56\nDT 200,1,24576\nDT 202,5,24576\nMvav 176,240,64\nRC 247,247\nMvav 176,244,72\nDT 204,1,25600\n"
"DT 206,5,25600\nMvav 176,248,80\nRC 248,248\nMvav 176,252,88\nDT 208,1,26624\nDT 210,5,26624\nMvav 176,172,96\n"
"LD 245,44,84,1024\nMvavz 180,220,36\ns_add_u32 s84, s84, s85\nDT 212,2,18432\nDT 214,6,18432\nMvav 180,224,44\n"
"RC 249,249\nMvav 180,228,52\nDT 216,2,19456\nDT 218,6,19456\nMvav 180,232,60\nDT 220,2,20480\n"
"v_add_f32 v175, 0x3727c5ac, v175\nMvav 180,240,68\nDT 222,6,20480\nCV 175,175,175\nMvav 180,244,76\n"
"LD 245,44,84,2048\nMvav 180,248,84\nWL 6\nDT 224,2,21504\nDT 226,6,21504\nMvav 180,252,92\n"
"s_add_u32 s84, s84, s85\nDT 228,2,22528\nv_lshlrev_b32 v175, 16, v175\nMvav 180,172,100\nv_sqrt_f32 v175, v175\n"
"Mvavz 184,176,32\nDT 230,6,22528\nDT 232,2,23552\nMvav 184,180,40\nDT 234,6,23552\nCV 175,175,175\n"
"Mvav 184,184,48\nDT 240,2,24576\nv_lshlrev_b32 v175, 16, v175\nMvav 184,188,56\nLD 245,44,84,3072\n"
"Mvav 184,192,64\ns_mov_b32 s84, 2359424\nDT 242,6,24576\nDT 244,2,25600\nMvav 184,196,72\ns_addk_i32 m0, 4096\n"
"RC 175,175\nMvav 184,200,80\nDT 246,6,25600\nDT 248,2,26624\nMvav 184,204,88\nDT 250,6,26624\n"
"v_mul_f32 v242, 0xbfb8aa3b, v175\nMvav 184,208,96\nDT 252,3,18432\nv_mul_f32 v243, 0xbfb8aa3b, v175\n"
"Mvavz 188,176,36\nDT 254,7,18432\nML 108,108,242\nMvav 188,180,44\nLD 245,44,84,0\nMvav 188,184,52\n"
"s_add_u32 s84, s84, s85\nDT 172,3,19456\nDT 174,7,19456\nMvav 188,188,60\nDT 176,3,20480\nML 109,109,243\n"
"Mvav 188,192,68\nDT 178,7,20480\nML 110,110,242\nMvav 188,196,76\nDT 180,3,21504\nML 111,111,243\n"
"Mvav 188,200,84\nWL 6\nEX 250,108\nMvav 188,204,92\nDT 182,7,21504\nDT 184,3,22528\nMvav 188,208,100\n"
"EX 251,109\nMvavz 192,212,32\nDT 186,7,22528\nDT 188,3,23552\nMvav 192,216,40\nEX 252,110\nMvav 192,220,48\n"
"DT 190,7,23552\nDT 192,3,24576\nMvav 192,224,56\nEX 253,111\nMvav 192,228,64\nDT 194,7,24576\nDT 196,3,25600\n"
"Mvav 192,232,72\nDT 198,7,25600\nv_add_f32 v250, 1.0, v250\nMvav 192,240,80\ns_waitcnt vmcnt(9)\n"
"DT 200,3,26624\nv_add_f32 v251, 1.0, v251\nMvav 192,244,88\ns_barrier\nDT 202,7,26624\n"
"v_add_f32 v252, 1.0, v252\nMvav 192,248,96\nLD 245,44,84,1024\nMvavz 196,212,36\n"
"s_load_dword s6, s[74:75], s82\nDT 204,0,31744\nDT 206,4,31744\nMvav 196,216,44\ns_add_u32 s84, s84, s85\n"
"DT 208,0,32768\nv_add_f32 v253, 1.0, v253\nMvav 196,220,52\nRC 250,250\nMvav 196,224,60\nDT 210,4,32768\n"
"DT 212,0,33792\nMvav 196,228,68\nRC 251,251\nMvav 196,232,76\nDT 214,4,33792\nDT 216,0,34816\nMvav 196,240,84\n"
"WL 7\nLD 245,44,84,2048\nMvav 196,244,92\ns_add_u32 s84, s84, s85\nDT 218,4,34816\nDT 220,0,35840\n"
"Mvav 196,248,100\nRC 252,252\nMvavz 200,252,32\nDT 222,4,35840\nDT 224,0,36864\nMvav 200,172,40\n"
"s_add_u32 s78, s78, s6\nRC 253,253\nMvav 200,176,48\nDT 226,4,36864\nDT 228,0,37888\nMvav 200,180,56\n"
"DT 230,4,37888\nML 112,112,240\nMvav 200,184,64\ns_add_u32 s78, s78, s6\nLD 245,44,84,3072\nMvav 200,188,72\n"
"s_mov_b32 s84, 3539072\nDT 232,0,38912\nDT 234,4,38912\nMvav 200,192,80\ns_addk_i32 m0, 4096\nDT 240,0,39936\n"
"ML 113,113,241\nMvav 200,196,88\ns_add_u32 s78, s78, s6\nDT 242,4,39936\nML 114,114,240\nMvav 200,200,96\n"
"DT 244,1,31744\nML 115,115,241\nMvavz 204,252,36\nEX 254,112\nMvav 204,172,44\ns_add_u32 s78, s78, s6\n"
"DT 246,5,31744\nDT 248,1,32768\nMvav 204,176,52\nLD 245,44,84,0\nMvav 204,180,60\ns_add_u32 s84, s84, s85\n"
"DT 250,5,32768\nDT 252,1,33792\nMvav 204,184,68\ns_add_u32 s78, s78, s6\nEX 255,113\nMvav 204,188,76\n"
"DT 254,5,33792\nDT 172,1,34816\nMvav 204,192,84\nWL 6\nEX 20,114\nMvav 204,196,92\ns_add_u32 s78, s78, s6\n"
"DT 174,5,34816\nDT 176,1,35840\nMvav 204,200,100\nEX 21,115\nMvav 176,204,128\nDT 178,5,35840\nDT 180,1,36864\n"
"Mvav 176,208,132\ns_add_u32 s78, s78, s6\nLD 245,44,84,1024\nMvav 176,212,136\ns_add_u32 s84, s84, s85\n"
"DT 182,5,36864\nDT 184,1,37888\nMvav 176,216,140\nDT 186,5,37888\nv_add_f32 v254, 1.0, v254\nMvav 176,220,144\n"
"s_add_u32 s78, s78, s6\nDT 188,1,38912\nv_add_f32 v255, 1.0, v255\nMvav 176,224,148\nDT 190,5,38912\n"
"v_add_f32 v20, 1.0, v20\nMvav 176,228,152\nDT 192,1,39936\nv_add_f32 v21, 1.0, v21\nMvav 176,232,156\n"
"s_add_u32 s78, s78, s6\nLD 245,44,84,2048\nMvav 176,240,160\ns_add_u32 s84, s84, s85\nDT 194,5,39936\n"
"DT 196,2,31744\nMvaa 180,204,136\nRC 254,254\nMvaa 180,208,140\ns_add_u32 s78, s78, s6\nDT 198,6,31744\n"
"DT 200,2,32768\nMvaa 180,212,144\nRC 255,255\nMvaa 180,216,148\nDT 202,6,32768\nDT 204,2,33792\n"
"Mvaa 180,220,152\ns_add_u32 s78, s78, s6\nRC 20,20\nMvaa 180,224,156\nDT 206,6,33792\nDT 208,2,34816\n"
"Mvaa 180,228,160\nWL 6\nLD 245,44,84,3072\nMvaa 180,232,164\ns_add_u32 s78, s78, s6\nDT 210,6,34816\n"
"DT 212,2,35840\nMvaa 180,240,168\ns_addk_i32 m0, 4096\nRC 21,21\nMvav 184,244,128\ns_mov_b32 s84, 4718720\n"
"DT 214,6,35840\nDT 216,2,36864\nMvav 184,248,132\ns_add_u32 s78, s78, s6\nDT 218,6,36864\nML 116,116,242\n"
"Mvav 184,252,136\nDT 220,2,37888\nML 117,117,243\nMvav 184,172,140\nLD 245,44,84,0\nMvav 184,176,144\n"
"s_add_u32 s78, s78, s6\nDT 222,6,37888\nDT 224,2,38912\nMvav 184,180,148\ns_add_u32 s84, s84, s85\n"
"DT 226,6,38912\nML 118,118,242\nMvav 184,184,152\nDT 228,2,39936\nML 119,119,243\nMvav 184,188,156\n"
"s_add_u32 s78, s78, s6\nEX 22,116\nMvav 184,192,160\nDT 230,6,39936\nDT 232,3,31744\nMvaa 188,244,136\n"
"EX 23,117\nMvaa 188,248,140\ns_add_u32 s78, s78, s6\nDT 234,7,31744\nDT 240,3,32768\nMvaa 188,252,144\n"
"LD 245,44,84,1024\nMvaa 188,172,148\ns_movk_i32 s84, 128\nDT 242,7,32768\nDT 244,3,33792\nMvaa 188,176,152\n"
"s_add_u32 s78, s78, s6\nEX 24,118\nMvaa 188,180,156\nDT 246,7,33792\nDT 248,3,34816\nMvaa 188,184,160\nWL 6\n"
"EX 25,119\nMvaa 188,188,164\ns_add_u32 s78, s78, s6\nDT 250,7,34816\nDT 252,3,35840\n"
"s_xor_b32 s82, s82, 0x2000\nMvaa 188,192,168\ns_addk_i32 m0, 2048\nDT 254,7,35840\nv_add_f32 v22, 1.0, v22\n"
"Mvav 192,196,128\nDT 172,3,36864\nv_add_f32 v23, 1.0, v23\nMvav 192,200,132\nDT 174,7,36864\n"
"v_add_f32 v24, 1.0, v24\nMvav 192,204,136\nDT 176,3,37888\nv_add_f32 v25, 1.0, v25\nMvav 192,208,140\nRC 22,22\n"
"Mvav 192,212,144\nDT 178,7,37888\nDT 180,3,38912\nMvav 192,216,148\nRC 23,23\nMvav 192,220,152\n"
"s_waitcnt vmcnt(9)\nDT 182,7,38912\nDT 184,3,39936\nMvav 192,224,156\ns_barrier\nRC 24,24\nMvav 192,228,160\n"
"DT 186,7,39936\nDT 188,0,0\nMvaa 196,196,136\ns_load_dword s7, s[74:75], s82\nLD 244,40,84,0\nMvaa 196,200,140\n"
"s_add_u32 s84, s84, s86\nDT 190,4,0\nDT 192,0,1024\nMvaa 196,204,144\nRC 25,25\nMvaa 196,208,148\n"
"DT 194,4,1024\nDT 196,0,2048\nMvaa 196,212,152\nDT 198,4,2048\nML 120,120,240\nMvaa 196,216,156\nDT 200,0,3072\n"
"ML 121,121,241\nMvaa 196,220,160\nWL 6\nLD 244,40,84,1024\nMvaa 196,224,164\ns_add_u32 s84, s84, s86\n"
"DT 202,4,3072\nDT 204,0,4096\nMvaa 196,228,168\nDT 206,4,4096\nML 122,122,240\nMvav 200,232,128\nDT 208,0,5120\n"
"ML 123,123,241\nMvav 200,240,132\ns_sub_u32 s78, s78, s7\nEX 28,120\nMvav 200,244,136\nDT 210,4,5120\n"
"DT 212,0,6144\nMvav 200,248,140\nEX 29,121\nMvav 200,252,144\ns_sub_u32 s78, s78, s7\nDT 214,4,6144\n"
"DT 216,0,7168\nMvav 200,172,148\nLD 244,40,84,2048\nMvav 200,176,152\ns_add_u32 s84, s84, s86\nDT 218,4,7168\n"
"DT 220,0,8192\nMvav 200,180,156\ns_sub_u32 s78, s78, s7\nEX 30,122\nMvav 200,184,160\nDT 222,4,8192\n"
"DT 224,1,0\nMvaa 204,232,136\nEX 31,123\nMvaa 204,240,140\ns_sub_u32 s78, s78, s7\nDT 226,5,0\nDT 228,1,1024\n"
"Mvaa 204,244,144\nLD 244,40,84,3072\nMvaa 204,248,148\ns_mov_b32 s84, 1048704\nDT 230,5,1024\nDT 232,1,2048\n"
"Mvaa 204,252,152\ns_sub_u32 s78, s78, s7\nDT 234,5,2048\nv_add_f32 v28, 1.0, v28\nMvaa 204,172,156\n"
"s_addk_i32 m0, 4096\nDT 240,1,3072\nv_add_f32 v29, 1.0, v29\nMvaa 204,176,160\nWL 6\nDT 242,5,3072\n"
"v_add_f32 v30, 1.0, v30\nMvaa 204,180,164\ns_sub_u32 s78, s78, s7\nDT 244,1,4096\nv_add_f32 v31, 1.0, v31\n"
"Mvaa 204,184,168\nRC 28,28\nMvavz 208,188,32\nDT 246,5,4096\nDT 248,1,5120\nMvav 208,192,40\n"
"s_sub_u32 s78, s78, s7\nLD 244,40,84,0\nMvav 208,196,48\ns_add_u32 s84, s84, s86\nDT 250,5,5120\nDT 252,1,6144\n"
"Mvav 208,200,56\nRC 29,29\nMvav 208,204,64\ns_sub_u32 s78, s78, s7\nDT 254,5,6144\nDT 172,1,7168\n"
"Mvav 208,208,72\nRC 30,30\nMvav 208,212,80\nDT 174,5,7168\nDT 176,1,8192\nMvav 208,216,88\n"
"s_sub_u32 s78, s78, s7\nLD 244,40,84,1024\nMvav 208,220,96\ns_add_u32 s84, s84, s86\nDT 178,5,8192\nDT 180,2,0\n"
"Mvavz 212,188,36\nRC 31,31\nMvav 212,192,44\ns_sub_u32 s78, s78, s7\nDT 182,6,0\nDT 184,2,1024\n"
"Mvav 212,196,52\nDT 186,6,1024\nML 124,124,242\nMvav 212,200,60\nDT 188,2,2048\nML 125,125,243\n"
"Mvav 212,204,68\ns_sub_u32 s78, s78, s7\nDT 190,6,2048\nML 126,126,242\nMvav 212,208,76\nLD 244,40,84,2048\n"
"Mvav 212,212,84\nWL 5\nDT 192,2,3072\nDT 194,6,3072\nMvav 212,216,92\ns_sub_u32 s78, s78, s7\nDT 196,2,4096\n"
"ML 127,127,243\nMvav 212,220,100\ns_add_u32 s84, s84, s86\nEX 26,124\nMvavz 216,224,32\nDT 198,6,4096\n"
"DT 200,2,5120\nMvav 216,228,40\ns_sub_u32 s78, s78, s7\nEX 27,125\nMvav 216,232,48\nDT 202,6,5120\n"
"DT 204,2,6144\nMvav 216,240,56\nLD 244,40,84,3072\nMvav 216,244,64\ns_sub_u32 s78, s78, s7\nDT 206,6,6144\n"
"DT 208,2,7168\nMvav 216,248,72\ns_addk_i32 m0, 4096\nDT 210,6,7168\nv_add_f32 v26, 1.0, v26\nMvav 216,252,80\n"
"Lw3_%=:\ns_cbranch_execz Lret0_%=\ns_mov_b32 s84, 2097280\nDT 212,2,8192\nv_add_f32 v27, 1.0, v27\n"
"Mvav 216,172,88\ns_sub_u32 s78, s78, s7\nRC 26,26\nMvav 216,176,96\nDT 214,6,8192\nDT 216,3,0\n"
"Mvavz 220,224,36\nRC 27,27\nMvav 220,228,44\ns_sub_u32 s78, s78, s7\nDT 218,7,0\nDT 220,3,1024\n"
"Mvav 220,232,52\nLD 244,40,84,0\nMvav 220,240,60\ns_mov_b32 s84, 2359424\nDT 222,7,1024\nDT 224,3,2048\n"
"Mvav 220,244,68\ns_sub_u32 s78, s78, s7\nDT 226,7,2048\nML 164,164,240\nMvav 220,248,76\ns_addk_i32 m0, 5120\n"
"DT 228,3,3072\nML 165,165,241\nMvav 220,252,84\nWL 6\nDT 230,7,3072\nML 166,166,240\nMvav 220,172,92\n"
"s_sub_u32 s78, s78, s7\nDT 232,3,4096\nML 167,167,241\nMvav 220,176,100\ns_bfe_u32 s78, s78, 0x10016\n"
"DT 234,7,4096\nv_mul_f32 v16, 0x3e0293ee, v174\nMvavz 224,180,32\ns_xor_b32 s78, s78, s3\nDT 240,3,5120\n"
"v_mul_f32 v17, 0x3e0293ee, v174\nMvav 224,184,40\ns_movk_i32 s74, 0\nDT 242,7,5120\nv_xor_b32 v174, s78, v172\n"
"Mvav 224,188,48\ns_movk_i32 s75, 2048\nDT 244,3,6144\nv_and_or_b32 v174, v174, 1, 2\nMvav 224,192,56\n"
"s_mov_b64 exec, s[0:1]\nglobal_atomic_add v13, v174, s[98:99]\ns_mov_b64 exec, -1\nMvav 224,196,64\n"
"DT 246,7,6144\nDT 248,3,7168\nMvav 224,200,72\nDT 250,7,7168\nv_mov_b32 v174, 2\nMvav 224,204,80\n"
"s_waitcnt vmcnt(10)\nDT 252,3,8192\nML 168,168,242\nML 169,169,243\nML 170,170,242\nML 171,171,243\n"
"v_pk_mul_f32 v[104:105], v[104:105], v[246:247]\nv_pk_mul_f32 v[106:107], v[106:107], v[248:249]\n"
"v_pk_mul_f32 v[108:109], v[108:109], v[250:251]\nv_pk_mul_f32 v[110:111], v[110:111], v[252:253]\n"
"v_pk_mul_f32 v[112:113], v[112:113], v[254:255]\nv_pk_mul_f32 v[114:115], v[114:115], v[20:21]\n"
"v_pk_mul_f32 v[116:117], v[116:117], v[22:23]\nv_pk_mul_f32 v[118:119], v[118:119], v[24:25]\n"
"v_pk_mul_f32 v[120:121], v[120:121], v[28:29]\nv_pk_mul_f32 v[122:123], v[122:123], v[30:31]\n"
"v_pk_mul_f32 v[124:125], v[124:125], v[26:27]\nv_pk_mul_f32 v[176:177], v[176:177], v[16:17]\n"
"v_pk_mul_f32 v[178:179], v[178:179], v[16:17]\nv_pk_mul_f32 v[184:185], v[184:185], v[16:17]\n"
"v_pk_mul_f32 v[186:187], v[186:187], v[16:17]\nv_pk_mul_f32 v[192:193], v[192:193], v[16:17]\n"
"v_pk_mul_f32 v[194:195], v[194:195], v[16:17]\nv_pk_mul_f32 v[200:201], v[200:201], v[16:17]\n"
"v_pk_mul_f32 v[202:203], v[202:203], v[16:17]\nCV 104,104,105\nMvav 224,208,88\ns_barrier\nDT 254,7,8192\n"
"CV 106,106,107\nMvav 224,212,96\nLD 244,40,84,0\nMvavz 228,180,36\ns_add_u32 s84, s84, s86\nDT 172,0,9216\n"
"DT 174,4,9216\nMvav 228,184,44\nDT 176,0,10240\nAW 64,104\nMvav 228,188,52\nDT 178,4,10240\nAW 65,106\n"
"Mvav 228,192,60\nDT 180,0,11264\nCV 108,108,109\nMvav 228,196,68\nDT 182,4,11264\nCV 110,110,111\n"
"Mvav 228,200,76\nDT 184,0,12288\nAW 68,108\nMvav 228,204,84\nWL 7\nLD 244,40,84,1024\nMvav 228,208,92\n"
"s_add_u32 s84, s84, s86\nDT 186,4,12288\nDT 188,0,13312\nMvav 228,212,100\nDT 190,4,13312\nAW 69,110\n"
"Mvavz 232,216,32\nDT 192,0,14336\nCV 112,112,113\nMvav 232,220,40\nDT 194,4,14336\nCV 114,114,115\n"
"Mvav 232,224,48\nDT 196,0,15360\nAW 66,112\nMvav 232,228,56\nDT 198,4,15360\nAW 67,114\nMvav 232,232,64\n"
"LD 244,40,84,2048\nMvav 232,240,72\ns_add_u32 s84, s84, s86\nDT 200,0,16384\nDT 202,4,16384\nMvav 232,244,80\n"
"DT 204,0,17408\nCV 116,116,117\nMvav 232,248,88\nDT 206,4,17408\nCV 118,118,119\nMvav 232,252,96\n"
"DT 208,1,9216\nAW 70,116\nMvavz 236,216,36\nDT 210,5,9216\nAW 71,118\nMvav 236,220,44\nDT 212,1,10240\n"
"CV 120,120,121\nMvav 236,224,52\nLD 244,40,84,3072\nMvav 236,228,60\ns_mov_b32 s84, 3408000\nDT 214,5,10240\n"
"DT 216,1,11264\nMvav 236,232,68\ns_addk_i32 m0, 4096\nDT 218,5,11264\nCV 122,122,123\nMvav 236,240,76\n"
"DT 220,1,12288\nAW 72,120\nMvav 236,244,84\nWL 6\nDT 222,5,12288\nAW 73,122\nMvav 236,248,92\nEX 246,126\n"
"Mvav 236,252,100\nDT 224,1,13312\nDT 226,5,13312\nMvav 208,172,128\nLD 244,40,84,0\nMvav 208,176,132\n"
"s_add_u32 s84, s84, s86\nDT 228,1,14336\nDT 230,5,14336\nMvav 208,180,136\nEX 247,127\nMvav 208,184,140\n"
"DT 232,1,15360\nDT 234,5,15360\nMvav 208,188,144\nDT 240,1,16384\nv_add_f32 v246, 1.0, v246\nMvav 208,192,148\n"
"DT 242,5,16384\nv_add_f32 v247, 1.0, v247\nMvav 208,196,152\nRC 246,246\nMvav 208,200,156\nDT 244,1,17408\n"
"DT 246,5,17408\nMvav 208,204,160\nLD 244,40,84,1024\nMvaa 212,172,136\ns_add_u32 s84, s84, s86\nDT 248,2,9216\n"
"DT 250,6,9216\nMvaa 212,176,140\nRC 247,247\nMvaa 212,180,144\nDT 252,2,10240\nDT 254,6,10240\n"
"Mvaa 212,184,148\nDT 172,2,11264\nCV 124,124,125\nMvaa 212,188,152\nDT 174,6,11264\nAW 76,124\n"
"Mvaa 212,192,156\nLD 244,40,84,2048\nMvaa 212,196,160\nWL 6\nDT 176,2,12288\nDT 178,6,12288\nMvaa 212,200,164\n"
"s_add_u32 s84, s84, s86\nEX 248,164\nMvaa 212,204,168\nDT 180,2,13312\nDT 182,6,13312\nMvav 216,208,128\n"
"EX 249,165\nMvav 216,212,132\nDT 184,2,14336\nDT 186,6,14336\nMvav 216,216,136\nEX 250,166\nMvav 216,220,140\n"
"DT 188,2,15360\nDT 190,6,15360\nMvav 216,224,144\nLD 244,40,84,3072\nMvav 216,228,148\ns_mov_b32 s84, 4456576\n"
"DT 192,2,16384\nDT 194,6,16384\nMvav 216,232,152\ns_addk_i32 m0, 4096\nEX 251,167\nMvav 216,240,156\n"
"DT 196,2,17408\nDT 198,6,17408\nMvav 216,244,160\nDT 200,3,9216\nv_add_f32 v248, 1.0, v248\nMvaa 220,208,136\n"
"DT 202,7,9216\nv_add_f32 v249, 1.0, v249\nMvaa 220,212,140\nLD 244,40,84,0\nMvaa 220,216,144\n"
"s_movk_i32 s84, 256\nDT 204,3,10240\nDT 206,7,10240\nMvaa 220,220,148\ns_add_u32 m0, s72, 0\nDT 208,3,11264\n"
"v_add_f32 v250, 1.0, v250\nMvaa 220,224,152\nDT 210,7,11264\nv_add_f32 v251, 1.0, v251\nMvaa 220,228,156\n"
"RC 248,248\nMvaa 220,232,160\nWL 5\nDT 212,3,12288\nDT 214,7,12288\nMvaa 220,240,164\nRC 249,249\n"
"Mvaa 220,244,168\nDT 216,3,13312\nDT 218,7,13312\nMvav 224,248,128\nRC 250,250\nMvav 224,252,132\n"
"DT 220,3,14336\nDT 222,7,14336\nMvav 224,172,136\nRC 251,251\nMvav 224,176,140\nDT 224,3,15360\nDT 226,7,15360\n"
"Mvav 224,180,144\nEX 252,168\nMvav 224,184,148\nDT 228,3,16384\nDT 230,7,16384\nMvav 224,188,152\n"
"s_waitcnt vmcnt(10)\nEX 253,169\nMvav 224,192,156\ns_barrier\nDT 232,3,17408\nDT 234,7,17408\nMvav 224,196,160\n"
"LD 245,44,84,0\nMvaa 228,248,136\ns_add_u32 s84, s84, s85\nDT 240,0,18432\nDT 242,4,18432\nMvaa 228,252,140\n"
"EX 254,170\nMvaa 228,172,144\nDT 244,0,19456\nDT 246,4,19456\nMvaa 228,176,148\nEX 255,171\nMvaa 228,180,152\n"
"DT 248,0,20480\nDT 250,4,20480\nMvaa 228,184,156\nDT 252,0,21504\nv_add_f32 v252, 1.0, v252\nMvaa 228,188,160\n"
"WL 7\nLD 245,44,84,1024\nMvaa 228,192,164\ns_add_u32 s84, s84, s85\nDT 254,4,21504\nDT 172,0,22528\n"
"Mvaa 228,196,168\nDT 174,4,22528\nv_add_f32 v253, 1.0, v253\nMvav 232,200,128\nDT 176,0,23552\n"
"v_add_f32 v254, 1.0, v254\nMvav 232,204,132\nDT 178,4,23552\nv_add_f32 v255, 1.0, v255\nMvav 232,208,136\n"
"RC 252,252\nMvav 232,212,140\nDT 180,0,24576\nDT 182,4,24576\nMvav 232,216,144\nLD 245,44,84,2048\n"
"Mvav 232,220,148\ns_add_u32 s84, s84, s85\nDT 184,0,25600\nDT 186,4,25600\nMvav 232,224,152\nRC 253,253\n"
"Mvav 232,228,156\nDT 188,0,26624\nDT 190,4,26624\nMvav 232,232,160\nRC 254,254\nMvaa 236,200,136\n"
"DT 192,1,18432\nDT 194,5,18432\nMvaa 236,204,140\nRC 255,255\nMvaa 236,208,144\nDT 196,1,19456\nDT 198,5,19456\n"
"Mvaa 236,212,148\nLD 245,44,84,3072\nMvaa 236,216,152\ns_mov_b32 s84, 1179904\nDT 200,1,20480\nDT 202,5,20480\n"
"Mvaa 236,220,156\ns_addk_i32 m0, 4096\nDT 204,1,21504\nCV 176,176,177\nMvaa 236,224,160\nWL 7\nDT 206,5,21504\n"
"CV 178,178,179\nMvaa 236,228,164\nDT 208,1,22528\nAW 0,176\nMvaa 236,232,168\nDT 210,5,22528\nAW 1,178\n"
"Mvavz 104,240,32\nLD 245,44,84,0\nMvav 104,244,40\ns_add_u32 s84, s84, s85\nDT 212,1,23552\nDT 214,5,23552\n"
"Mvav 104,248,48\nDT 216,1,24576\nv_mul_f32 v18, 0x3e0293ee, v175\nMvav 104,252,56\nDT 218,5,24576\n"
"v_mul_f32 v19, 0x3e0293ee, v175\nMvav 104,172,64\nDT 220,1,25600\nCV 184,184,185\nMvav 104,176,72\n"
"DT 222,5,25600\nCV 186,186,187\nMvav 104,180,80\nDT 224,1,26624\nAW 2,184\nAW 3,186\nCV 192,192,193\n"
"CV 194,194,195\nv_pk_mul_f32 v[126:127], v[126:127], v[246:247]\n"
"v_pk_mul_f32 v[164:165], v[164:165], v[248:249]\nv_pk_mul_f32 v[166:167], v[166:167], v[250:251]\n"
"v_pk_mul_f32 v[168:169], v[168:169], v[252:253]\nv_pk_mul_f32 v[170:171], v[170:171], v[254:255]\n"
"v_pk_mul_f32 v[180:181], v[180:181], v[18:19]\nv_pk_mul_f32 v[182:183], v[182:183], v[18:19]\n"
"v_pk_mul_f32 v[188:189], v[188:189], v[18:19]\nv_pk_mul_f32 v[190:191], v[190:191], v[18:19]\n"
"v_pk_mul_f32 v[196:197], v[196:197], v[18:19]\nv_pk_mul_f32 v[198:199], v[198:199], v[18:19]\n"
"v_pk_mul_f32 v[204:205], v[204:205], v[18:19]\nv_pk_mul_f32 v[206:207], v[206:207], v[18:19]\n"
"v_pk_mul_f32 v[208:209], v[208:209], v[240:241]\nv_pk_mul_f32 v[210:211], v[210:211], v[240:241]\n"
"v_pk_mul_f32 v[212:213], v[212:213], v[242:243]\nv_pk_mul_f32 v[214:215], v[214:215], v[242:243]\n"
"v_pk_mul_f32 v[216:217], v[216:217], v[240:241]\nv_pk_mul_f32 v[218:219], v[218:219], v[240:241]\n"
"v_pk_mul_f32 v[220:221], v[220:221], v[242:243]\nv_pk_mul_f32 v[222:223], v[222:223], v[242:243]\n"
"v_pk_mul_f32 v[224:225], v[224:225], v[240:241]\nv_pk_mul_f32 v[226:227], v[226:227], v[240:241]\n"
"v_pk_mul_f32 v[228:229], v[228:229], v[242:243]\nv_pk_mul_f32 v[230:231], v[230:231], v[242:243]\n"
"v_pk_mul_f32 v[232:233], v[232:233], v[240:241]\nv_pk_mul_f32 v[234:235], v[234:235], v[240:241]\n"
"v_pk_mul_f32 v[236:237], v[236:237], v[242:243]\nv_pk_mul_f32 v[238:239], v[238:239], v[242:243]\n"
"CV 126,126,127\nMvav 104,184,88\nLD 245,44,84,1024\nMvav 104,188,96\ns_add_u32 s84, s84, s85\nDT 226,5,26624\n"
"DT 228,2,18432\nMvavz 108,240,36\nDT 230,6,18432\nAW 77,126\nMvav 108,244,44\nDT 232,2,19456\nCV 164,164,165\n"
"Mvav 108,248,52\nDT 234,6,19456\nCV 166,166,167\nMvav 108,252,60\nDT 240,2,20480\nAW 74,164\nMvav 108,172,68\n"
"DT 242,6,20480\nAW 75,166\nMvav 108,176,76\nLD 245,44,84,2048\nMvav 108,180,84\nWL 5\nDT 244,2,21504\n"
"DT 246,6,21504\nMvav 108,184,92\ns_add_u32 s84, s84, s85\nDT 248,2,22528\nCV 168,168,169\nMvav 108,188,100\n"
"DT 250,6,22528\nCV 170,170,171\nMvavz 112,192,32\nDT 252,2,23552\nAW 78,168\nMvav 112,196,40\nDT 254,6,23552\n"
"AW 79,170\nMvav 112,200,48\nDT 172,2,24576\nCV 180,180,181\nMvav 112,204,56\nLD 245,44,84,3072\n"
"Mvav 112,208,64\ns_mov_b32 s84, 2359552\nDT 174,6,24576\nDT 176,2,25600\nMvav 112,212,72\ns_addk_i32 m0, 4096\n"
"DT 178,6,25600\nCV 182,182,183\nMvav 112,216,80\nDT 180,2,26624\nAW 16,180\nMvav 112,220,88\nDT 182,6,26624\n"
"AW 17,182\nMvav 112,224,96\nDT 184,3,18432\nCV 188,188,189\nMvavz 116,192,36\nDT 186,7,18432\nCV 190,190,191\n"
"Mvav 116,196,44\nLD 245,44,84,0\nMvav 116,200,52\ns_add_u32 s84, s84, s85\nDT 188,3,19456\nDT 190,7,19456\n"
"Mvav 116,204,60\nDT 192,3,20480\nAW 18,188\nMvav 116,208,68\nDT 194,7,20480\nAW 19,190\nMvav 116,212,76\n"
"DT 196,3,21504\nAW 4,192\nMvav 116,216,84\nWL 6\nDT 198,7,21504\nAW 5,194\nMvav 116,220,92\nDT 200,3,22528\n"
"CV 196,196,197\nMvav 116,224,100\nDT 202,7,22528\nCV 198,198,199\nMvavz 120,228,32\nDT 204,3,23552\nAW 20,196\n"
"Mvav 120,232,40\nDT 206,7,23552\nAW 21,198\nMvav 120,240,48\nDT 208,3,24576\nCV 200,200,201\nMvav 120,244,56\n"
"DT 210,7,24576\nCV 202,202,203\nMvav 120,248,64\nDT 212,3,25600\nAW 6,200\nMvav 120,252,72\nDT 214,7,25600\n"
"AW 7,202\nMvav 120,172,80\ns_waitcnt vmcnt(9)\nDT 216,3,26624\nCV 204,204,205\nMvav 120,176,88\ns_barrier\n"
"DT 218,7,26624\n"
"Lw4_%=:\ns_cbranch_execz Lret0_%=\nCV 206,206,207\nMvav 120,180,96\nLD 245,44,84,1024\nMvavz 124,228,36\n"
"s_add_u32 s84, s84, s85\nDT 220,0,31744\nDT 222,4,31744\nMvav 124,232,44\nDT 224,0,32768\nAW 22,204\n"
"Mvav 124,240,52\nDT 226,4,32768\nAW 23,206\nMvav 124,244,60\nEX 20,208\nMvav 124,248,68\nDT 228,0,33792\n"
"DT 230,4,33792\nMvav 124,252,76\nEX 21,209\nMvav 124,172,84\nWL 6\nDT 232,0,34816\nDT 234,4,34816\n"
"Mvav 124,176,92\nLD 245,44,84,2048\nMvav 124,180,100\ns_add_u32 s84, s84, s85\nDT 240,0,35840\nDT 242,4,35840\n"
"Mvavz 164,184,32\nEX 22,210\nMvav 164,188,40\nDT 244,0,36864\nDT 246,4,36864\nMvav 164,192,48\nEX 23,211\n"
"Mvav 164,196,56\nDT 248,0,37888\nDT 250,4,37888\nMvav 164,200,64\nLD 245,44,84,3072\nMvav 164,204,72\n"
"s_mov_b32 s84, 3539200\nDT 252,0,38912\nDT 254,4,38912\nMvav 164,208,80\ns_addk_i32 m0, 4096\nDT 172,0,39936\n"
"v_add_f32 v20, 1.0, v20\nMvav 164,212,88\nDT 174,4,39936\nv_add_f32 v21, 1.0, v21\nMvav 164,216,96\n"
"DT 176,1,31744\nv_add_f32 v22, 1.0, v22\nMvavz 168,184,36\nDT 178,5,31744\nv_add_f32 v23, 1.0, v23\n"
"Mvav 168,188,44\nRC 20,20\nMvav 168,192,52\nDT 180,1,32768\nDT 182,5,32768\nMvav 168,196,60\nLD 245,44,84,0\n"
"Mvav 168,200,68\ns_add_u32 s84, s84, s85\nDT 184,1,33792\nDT 186,5,33792\nMvav 168,204,76\nRC 21,21\n"
"Mvav 168,208,84\nWL 5\nDT 188,1,34816\nDT 190,5,34816\nMvav 168,212,92\nRC 22,22\nMvav 168,216,100\n"
"DT 192,1,35840\nDT 194,5,35840\nMvav 104,220,128\nLD 245,44,84,1024\nMvav 104,224,132\ns_add_u32 s84, s84, s85\n"
"DT 196,1,36864\nDT 198,5,36864\nMvav 104,228,136\nRC 23,23\nMvav 104,232,140\nDT 200,1,37888\nDT 202,5,37888\n"
"Mvav 104,240,144\nEX 24,212\nMvav 104,244,148\nDT 204,1,38912\nDT 206,5,38912\nMvav 104,248,152\nEX 25,213\n"
"Mvav 104,252,156\nDT 208,1,39936\nDT 210,5,39936\nMvav 104,172,160\nLD 245,44,84,2048\nMvaa 108,220,136\n"
"s_add_u32 s84, s84, s85\nDT 212,2,31744\nDT 214,6,31744\nMvaa 108,224,140\nEX 28,214\nMvaa 108,228,144\n"
"DT 216,2,32768\nDT 218,6,32768\nMvaa 108,232,148\nEX 29,215\nMvaa 108,240,152\nDT 220,2,33792\nDT 222,6,33792\n"
"Mvaa 108,244,156\nLD 245,44,84,3072\nMvaa 108,248,160\nWL 6\nDT 224,2,34816\nDT 226,6,34816\nMvaa 108,252,164\n"
"s_addk_i32 m0, 4096\nDT 228,2,35840\nv_add_f32 v24, 1.0, v24\nMvaa 108,172,168\ns_mov_b32 s84, 4718848\n"
"DT 230,6,35840\nv_add_f32 v25, 1.0, v25\nMvav 112,176,128\nDT 232,2,36864\nv_add_f32 v28, 1.0, v28\n"
"Mvav 112,180,132\nDT 234,6,36864\nv_add_f32 v29, 1.0, v29\nMvav 112,184,136\nRC 24,24\nMvav 112,188,140\n"
"DT 240,2,37888\nDT 242,6,37888\nMvav 112,192,144\nLD 245,44,84,0\nMvav 112,196,148\ns_add_u32 s84, s84, s85\n"
"DT 244,2,38912\nDT 246,6,38912\nMvav 112,200,152\nRC 25,25\nMvav 112,204,156\nDT 248,2,39936\nDT 250,6,39936\n"
"Mvav 112,208,160\nRC 28,28\nMvaa 116,176,136\nDT 252,3,31744\nDT 254,7,31744\nMvaa 116,180,140\n"
"LD 245,44,84,1024\nMvaa 116,184,144\ns_movk_i32 s84, 256\nDT 172,3,32768\nDT 174,7,32768\nMvaa 116,188,148\n"
"s_addk_i32 m0, 2048\nRC 29,29\nMvaa 116,192,152\nDT 176,3,33792\nDT 178,7,33792\nMvaa 116,196,156\nEX 30,216\n"
"Mvaa 116,200,160\nWL 6\nDT 180,3,34816\nDT 182,7,34816\nMvaa 116,204,164\nEX 31,217\nMvaa 116,208,168\n"
"DT 184,3,35840\nDT 186,7,35840\nMvav 120,212,128\nEX 26,218\nMvav 120,216,132\nDT 188,3,36864\nDT 190,7,36864\n"
"Mvav 120,220,136\nEX 27,219\nMvav 120,224,140\nDT 192,3,37888\nDT 194,7,37888\nMvav 120,228,144\n"
"DT 196,3,38912\nv_add_f32 v30, 1.0, v30\nMvav 120,232,148\nDT 198,7,38912\nv_add_f32 v31, 1.0, v31\n"
"Mvav 120,240,152\ns_waitcnt vmcnt(9)\nDT 200,3,39936\nv_add_f32 v26, 1.0, v26\nMvav 120,244,156\ns_barrier\n"
"DT 202,7,39936\nv_add_f32 v27, 1.0, v27\nMvav 120,248,160\nLD 244,40,84,0\nMvaa 124,212,136\n"
"s_add_u32 s84, s84, s86\nDT 204,0,0\nDT 206,4,0\nMvaa 124,216,140\nRC 30,30\nMvaa 124,220,144\nDT 208,0,1024\n"
"DT 210,4,1024\nMvaa 124,224,148\nRC 31,31\nMvaa 124,228,152\nDT 212,0,2048\nDT 214,4,2048\nMvaa 124,232,156\n"
"RC 26,26\nMvaa 124,240,160\nWL 6\nDT 216,0,3072\nDT 218,4,3072\nMvaa 124,244,164\nLD 244,40,84,1024\n"
"Mvaa 124,248,168\ns_add_u32 s84, s84, s86\nDT 220,0,4096\nDT 222,4,4096\nMvav 164,252,128\nRC 27,27\n"
"Mvav 164,172,132\nDT 224,0,5120\nDT 226,4,5120\nMvav 164,176,136\nEX 246,220\nMvav 164,180,140\nDT 228,0,6144\n"
"DT 230,4,6144\nMvav 164,184,144\nLD 244,40,84,2048\nMvav 164,188,148\ns_add_u32 s84, s84, s86\nDT 232,0,7168\n"
"DT 234,4,7168\nMvav 164,192,152\nEX 247,221\nMvav 164,196,156\nDT 240,0,8192\nDT 242,4,8192\nMvav 164,200,160\n"
"EX 248,222\nMvaa 168,252,136\nDT 244,1,0\nDT 246,5,0\nMvaa 168,172,140\nEX 249,223\nMvaa 168,176,144\n"
"DT 248,1,1024\nDT 250,5,1024\nMvaa 168,180,148\nLD 244,40,84,3072\nMvaa 168,184,152\ns_mov_b32 s84, 1048832\n"
"DT 252,1,2048\nDT 254,5,2048\nMvaa 168,188,156\ns_addk_i32 m0, 4096\nDT 172,1,3072\nv_add_f32 v246, 1.0, v246\n"
"Mvaa 168,192,160\nWL 7\nDT 174,5,3072\nv_add_f32 v247, 1.0, v247\nMvaa 168,196,164\nDT 176,1,4096\n"
"v_add_f32 v248, 1.0, v248\nMvaa 168,200,168\nDT 178,5,4096\nv_add_f32 v249, 1.0, v249\nMvavz 176,204,32\n"
"LD 244,40,84,0\nMvav 176,208,40\ns_add_u32 s84, s84, s86\nDT 180,1,5120\nDT 182,5,5120\nMvav 176,212,48\n"
"RC 246,246\nMvav 176,216,56\nDT 184,1,6144\nDT 186,5,6144\nMvav 176,220,64\nRC 247,247\nMvav 176,224,72\n"
"DT 188,1,7168\nDT 190,5,7168\nMvav 176,228,80\nRC 248,248\nMvav 176,232,88\nDT 192,1,8192\nDT 194,5,8192\n"
"Mvav 176,240,96\nLD 244,40,84,1024\nMvavz 180,204,36\ns_add_u32 s84, s84, s86\nDT 196,2,0\nDT 198,6,0\n"
"Mvav 180,208,44\nRC 249,249\nMvav 180,212,52\nDT 200,2,1024\nDT 202,6,1024\nMvav 180,216,60\nEX 250,224\n"
"Mvav 180,220,68\nDT 204,2,2048\nDT 206,6,2048\nMvav 180,224,76\nLD 244,40,84,2048\nMvav 180,228,84\nWL 6\n"
"DT 208,2,3072\nDT 210,6,3072\nMvav 180,232,92\ns_add_u32 s84, s84, s86\nEX 251,225\nMvav 180,240,100\n"
"DT 212,2,4096\nDT 214,6,4096\nMvavz 184,244,32\nEX 252,226\nMvav 184,248,40\nDT 216,2,5120\nDT 218,6,5120\n"
"Mvav 184,252,48\nEX 253,227\nMvav 184,172,56\nDT 220,2,6144\nDT 222,6,6144\nMvav 184,176,64\nLD 244,40,84,3072\n"
"Mvav 184,180,72\ns_mov_b32 s84, 2097408\nDT 224,2,7168\nDT 226,6,7168\nMvav 184,184,80\ns_addk_i32 m0, 4096\n"
"DT 228,2,8192\nv_add_f32 v250, 1.0, v250\nMvav 184,188,88\nDT 230,6,8192\nv_add_f32 v251, 1.0, v251\n"
"Mvav 184,192,96\nDT 232,3,0\nv_add_f32 v252, 1.0, v252\nMvavz 188,244,36\nDT 234,7,0\n"
"v_add_f32 v253, 1.0, v253\nMvav 188,248,44\nLD 244,40,84,0\nMvav 188,252,52\ns_mov_b32 s84, 2359552\n"
"DT 240,3,1024\nDT 242,7,1024\nMvav 188,172,60\ns_addk_i32 m0, 5120\nRC 250,250\nMvav 188,176,68\nDT 244,3,2048\n"
"DT 246,7,2048\nMvav 188,180,76\nRC 251,251\nMvav 188,184,84\nWL 5\nDT 248,3,3072\nDT 250,7,3072\n"
"Mvav 188,188,92\nRC 252,252\nMvav 188,192,100\nDT 252,3,4096\nDT 254,7,4096\nMvavz 192,196,32\nRC 253,253\n"
"EX 254,228\nv_pk_mul_f32 v[208:209], v[208:209], v[20:21]\nv_pk_mul_f32 v[210:211], v[210:211], v[22:23]\n"
"v_pk_mul_f32 v[212:213], v[212:213], v[24:25]\nv_pk_mul_f32 v[214:215], v[214:215], v[28:29]\n"
"v_pk_mul_f32 v[216:217], v[216:217], v[30:31]\nv_pk_mul_f32 v[218:219], v[218:219], v[26:27]\n"
"v_pk_mul_f32 v[220:221], v[220:221], v[246:247]\nv_pk_mul_f32 v[222:223], v[222:223], v[248:249]\n"
"v_pk_mul_f32 v[224:225], v[224:225], v[250:251]\nv_pk_mul_f32 v[226:227], v[226:227], v[252:253]\n"
"v_pk_mul_f32 v[104:105], v[104:105], v[16:17]\nv_pk_mul_f32 v[106:107], v[106:107], v[16:17]\n"
"v_pk_mul_f32 v[108:109], v[108:109], v[18:19]\nv_pk_mul_f32 v[110:111], v[110:111], v[18:19]\n"
"v_pk_mul_f32 v[112:113], v[112:113], v[16:17]\nv_pk_mul_f32 v[114:115], v[114:115], v[16:17]\n"
"v_pk_mul_f32 v[116:117], v[116:117], v[18:19]\nv_pk_mul_f32 v[118:119], v[118:119], v[18:19]\n"
"v_pk_mul_f32 v[120:121], v[120:121], v[16:17]\nv_pk_mul_f32 v[122:123], v[122:123], v[16:17]\n"
"v_pk_mul_f32 v[124:125], v[124:125], v[18:19]\nv_pk_mul_f32 v[126:127], v[126:127], v[18:19]\n"
"v_pk_mul_f32 v[164:165], v[164:165], v[16:17]\nv_pk_mul_f32 v[166:167], v[166:167], v[16:17]\n"
"v_pk_mul_f32 v[168:169], v[168:169], v[18:19]\nv_pk_mul_f32 v[170:171], v[170:171], v[18:19]\nCV 208,208,209\n"
"Mvav 192,200,40\nDT 172,3,5120\nDT 174,7,5120\nMvav 192,204,48\nDT 176,3,6144\nCV 210,210,211\nMvav 192,208,56\n"
"DT 178,7,6144\nAW 80,208\nMvav 192,212,64\nDT 180,3,7168\nAW 81,210\nMvav 192,216,72\nDT 182,7,7168\n"
"CV 212,212,213\nMvav 192,220,80\ns_waitcnt vmcnt(9)\nDT 184,3,8192\nCV 214,214,215\nMvav 192,224,88\ns_barrier\n"
"DT 186,7,8192\nAW 84,212\nMvav 192,228,96\nLD 244,40,84,0\nMvavz 196,196,36\ns_add_u32 s84, s84, s86\n"
"DT 188,0,9216\nDT 190,4,9216\nMvav 196,200,44\nDT 192,0,10240\nAW 85,214\nMvav 196,204,52\nDT 194,4,10240\n"
"CV 216,216,217\nMvav 196,208,60\nDT 196,0,11264\nCV 218,218,219\nMvav 196,212,68\nDT 198,4,11264\nAW 82,216\n"
"Mvav 196,216,76\nDT 200,0,12288\nAW 83,218\nMvav 196,220,84\nWL 7\nLD 244,40,84,1024\nMvav 196,224,92\n"
"s_add_u32 s84, s84, s86\nDT 202,4,12288\nDT 204,0,13312\nMvav 196,228,100\nDT 206,4,13312\nCV 220,220,221\n"
"Mvavz 200,232,32\nDT 208,0,14336\nCV 222,222,223\nMvav 200,240,40\nDT 210,4,14336\nAW 86,220\nMvav 200,244,48\n"
"DT 212,0,15360\nAW 87,222\nMvav 200,248,56\nDT 214,4,15360\nCV 224,224,225\nMvav 200,252,64\nLD 244,40,84,2048\n"
"Mvav 200,172,72\ns_add_u32 s84, s84, s86\nDT 216,0,16384\nDT 218,4,16384\nMvav 200,176,80\nDT 220,0,17408\n"
"CV 226,226,227\nMvav 200,180,88\nDT 222,4,17408\nAW 88,224\nMvav 200,184,96\nDT 224,1,9216\nAW 89,226\n"
"Mvavz 204,232,36\nEX 255,229\nMvav 204,240,44\nDT 226,5,9216\nDT 228,1,10240\nMvav 204,244,52\n"
"LD 244,40,84,3072\nMvav 204,248,60\ns_mov_b32 s84, 3408128\nDT 230,5,10240\nDT 232,1,11264\nMvav 204,252,68\n"
"s_addk_i32 m0, 4096\nEX 20,230\nMvav 204,172,76\nDT 234,5,11264\nDT 240,1,12288\nMvav 204,176,84\nWL 6\n"
"EX 21,231\nMvav 204,180,92\nDT 242,5,12288\nDT 244,1,13312\nMvav 204,184,100\nDT 246,5,13312\n"
"v_add_f32 v254, 1.0, v254\nMvav 176,188,128\nLD 244,40,84,0\nMvav 176,192,132\ns_add_u32 s84, s84, s86\n"
"DT 248,1,14336\nDT 250,5,14336\nMvav 176,196,136\nDT 252,1,15360\nv_add_f32 v255, 1.0, v255\nMvav 176,200,140\n"
"DT 254,5,15360\nv_add_f32 v20, 1.0, v20\nMvav 176,204,144\nDT 172,1,16384\nv_add_f32 v21, 1.0, v21\n"
"Mvav 176,208,148\nRC 254,254\nMvav 176,212,152\nDT 174,5,16384\nDT 176,1,17408\nMvav 176,216,156\n"
"LD 244,40,84,1024\nMvav 176,220,160\ns_add_u32 s84, s84, s86\nDT 178,5,17408\nDT 180,2,9216\nMvaa 180,188,136\n"
"RC 255,255\nMvaa 180,192,140\nDT 182,6,9216\nDT 184,2,10240\nMvaa 180,196,144\nRC 20,20\nMvaa 180,200,148\n"
"DT 186,6,10240\nDT 188,2,11264\nMvaa 180,204,152\nRC 21,21\nMvaa 180,208,156\nDT 190,6,11264\nDT 192,2,12288\n"
"Mvaa 180,212,160\nWL 6\nLD 244,40,84,2048\ns_nop 0\n"
"Lw5_%=:\ns_cbranch_execz Lret1_%=\nMvaa 180,216,164\ns_add_u32 s84, s84, s86\nDT 194,6,12288\nDT 196,2,13312\n"
"Mvaa 180,220,168\nEX 22,232\nMvav 184,224,128\nDT 198,6,13312\nDT 200,2,14336\nMvav 184,228,132\nEX 23,233\n"
"Mvav 184,232,136\nDT 202,6,14336\nDT 204,2,15360\nMvav 184,240,140\nLD 244,40,84,3072\nMvav 184,244,144\n"
"s_mov_b32 s84, 4456704\nDT 206,6,15360\nDT 208,2,16384\nMvav 184,248,148\ns_addk_i32 m0, 4096\nEX 24,234\n"
"Mvav 184,252,152\nDT 210,6,16384\nDT 212,2,17408\nMvav 184,172,156\nEX 25,235\nMvav 184,176,160\n"
"DT 214,6,17408\nDT 216,3,9216\nMvaa 188,224,136\nDT 218,7,9216\nv_add_f32 v22, 1.0, v22\nMvaa 188,228,140\n"
"LD 244,40,84,0\nMvaa 188,232,144\ns_movk_i32 s84, 384\nDT 220,3,10240\nDT 222,7,10240\nMvaa 188,240,148\n"
"s_add_u32 m0, s72, 0\nDT 224,3,11264\nv_add_f32 v23, 1.0, v23\nMvaa 188,244,152\nDT 226,7,11264\n"
"v_add_f32 v24, 1.0, v24\nMvaa 188,248,156\nDT 228,3,12288\nv_add_f32 v25, 1.0, v25\nMvaa 188,252,160\nWL 6\n"
"RC 22,22\nMvaa 188,172,164\nDT 230,7,12288\nDT 232,3,13312\nMvaa 188,176,168\nRC 23,23\nMvav 192,180,128\n"
"DT 234,7,13312\nDT 240,3,14336\nMvav 192,184,132\nRC 24,24\nMvav 192,188,136\nDT 242,7,14336\nDT 244,3,15360\n"
"Mvav 192,192,140\nRC 25,25\nMvav 192,196,144\nDT 246,7,15360\nDT 248,3,16384\nMvav 192,200,148\nEX 28,236\n"
"Mvav 192,204,152\ns_waitcnt vmcnt(9)\nDT 250,7,16384\nDT 252,3,17408\nMvav 192,208,156\ns_barrier\nEX 29,237\n"
"Mvav 192,212,160\nDT 254,7,17408\nDT 172,0,18432\nMvaa 196,180,136\nLD 245,44,84,0\nMvaa 196,184,140\n"
"s_add_u32 s84, s84, s85\nDT 174,4,18432\nDT 176,0,19456\nMvaa 196,188,144\nEX 30,238\nMvaa 196,192,148\n"
"DT 178,4,19456\nDT 180,0,20480\nMvaa 196,196,152\nEX 31,239\nMvaa 196,200,156\nDT 182,4,20480\nDT 184,0,21504\n"
"Mvaa 196,204,160\nWL 6\nLD 245,44,84,1024\nMvaa 196,208,164\ns_add_u32 s84, s84, s85\nDT 186,4,21504\n"
"DT 188,0,22528\nMvaa 196,212,168\nDT 190,4,22528\nv_add_f32 v28, 1.0, v28\nMvav 200,216,128\nDT 192,0,23552\n"
"v_add_f32 v29, 1.0, v29\nMvav 200,220,132\nDT 194,4,23552\nv_add_f32 v30, 1.0, v30\nMvav 200,224,136\n"
"DT 196,0,24576\nv_add_f32 v31, 1.0, v31\nMvav 200,228,140\nRC 28,28\nMvav 200,232,144\nDT 198,4,24576\n"
"DT 200,0,25600\nMvav 200,240,148\nLD 245,44,84,2048\nMvav 200,244,152\ns_add_u32 s84, s84, s85\nDT 202,4,25600\n"
"DT 204,0,26624\nMvav 200,248,156\nRC 29,29\nMvav 200,252,160\nDT 206,4,26624\nDT 208,1,18432\nMvaa 204,216,136\n"
"RC 30,30\nMvaa 204,220,140\nDT 210,5,18432\nDT 212,1,19456\nMvaa 204,224,144\nLD 245,44,84,3072\n"
"Mvaa 204,228,148\ns_mov_b32 s84, 1180032\nDT 214,5,19456\nDT 216,1,20480\nMvaa 204,232,152\n"
"s_addk_i32 m0, 4096\nRC 31,31\nMvaa 204,240,156\nDT 218,5,20480\nDT 220,1,21504\nMvaa 204,244,160\nWL 6\n"
"DT 222,5,21504\nCV 104,104,105\nMvaa 204,248,164\nDT 224,1,22528\nCV 106,106,107\nMvaa 204,252,168\n"
"DT 226,5,22528\nAW 8,104\nMvavz 208,172,32\nLD 245,44,84,0\nMvav 208,176,40\ns_add_u32 s84, s84, s85\n"
"DT 228,1,23552\nDT 230,5,23552\nMvav 208,180,48\nDT 232,1,24576\nAW 9,106\nMvav 208,184,56\nDT 234,5,24576\n"
"CV 108,108,109\nMvav 208,188,64\nDT 240,1,25600\nCV 110,110,111\nMvav 208,192,72\nDT 242,5,25600\nAW 24,108\n"
"Mvav 208,196,80\nDT 244,1,26624\nAW 25,110\nMvav 208,200,88\nLD 245,44,84,1024\nMvav 208,204,96\n"
"s_add_u32 s84, s84, s85\nDT 246,5,26624\nDT 248,2,18432\nMvavz 212,172,36\nDT 250,6,18432\nCV 112,112,113\n"
"Mvav 212,176,44\nDT 252,2,19456\nCV 114,114,115\nMvav 212,180,52\nDT 254,6,19456\nAW 10,112\nMvav 212,184,60\n"
"DT 172,2,20480\nAW 11,114\nMvav 212,188,68\nDT 174,6,20480\nCV 116,116,117\nMvav 212,192,76\nLD 245,44,84,2048\n"
"Mvav 212,196,84\nWL 5\nDT 176,2,21504\nDT 178,6,21504\nMvav 212,200,92\ns_add_u32 s84, s84, s85\n"
"DT 180,2,22528\nCV 118,118,119\nMvav 212,204,100\nDT 182,6,22528\nAW 26,116\nMvavz 216,208,32\nDT 184,2,23552\n"
"AW 27,118\nMvav 216,212,40\nDT 186,6,23552\nCV 120,120,121\nMvav 216,216,48\nDT 188,2,24576\nCV 122,122,123\n"
"Mvav 216,220,56\nLD 245,44,84,3072\nMvav 216,224,64\ns_mov_b32 s84, 2359680\nDT 190,6,24576\nDT 192,2,25600\n"
"Mvav 216,228,72\ns_addk_i32 m0, 4096\nDT 194,6,25600\nAW 12,120\nMvav 216,232,80\nDT 196,2,26624\nAW 13,122\n"
"Mvav 216,240,88\nDT 198,6,26624\nCV 124,124,125\nMvav 216,244,96\nDT 200,3,18432\nCV 126,126,127\n"
"Mvavz 220,208,36\nDT 202,7,18432\nAW 28,124\nAW 29,126\nCV 164,164,165\nCV 166,166,167\n"
"v_pk_mul_f32 v[228:229], v[228:229], v[254:255]\nv_pk_mul_f32 v[230:231], v[230:231], v[20:21]\n"
"v_pk_mul_f32 v[232:233], v[232:233], v[22:23]\nv_pk_mul_f32 v[234:235], v[234:235], v[24:25]\n"
"v_pk_mul_f32 v[236:237], v[236:237], v[28:29]\nv_pk_mul_f32 v[238:239], v[238:239], v[30:31]\n"
"v_pk_mul_f32 v[186:187], v[186:187], v[240:241]\nv_pk_mul_f32 v[188:189], v[188:189], v[242:243]\n"
"v_pk_mul_f32 v[190:191], v[190:191], v[242:243]\nv_pk_mul_f32 v[192:193], v[192:193], v[240:241]\n"
"v_pk_mul_f32 v[194:195], v[194:195], v[240:241]\nv_pk_mul_f32 v[196:197], v[196:197], v[242:243]\n"
"v_pk_mul_f32 v[176:177], v[176:177], v[240:241]\nv_pk_mul_f32 v[178:179], v[178:179], v[240:241]\n"
"v_pk_mul_f32 v[198:199], v[198:199], v[242:243]\nv_pk_mul_f32 v[200:201], v[200:201], v[240:241]\n"
"v_pk_mul_f32 v[180:181], v[180:181], v[242:243]\nv_pk_mul_f32 v[182:183], v[182:183], v[242:243]\n"
"v_pk_mul_f32 v[202:203], v[202:203], v[240:241]\nv_pk_mul_f32 v[204:205], v[204:205], v[242:243]\n"
"v_pk_mul_f32 v[184:185], v[184:185], v[240:241]\nv_pk_mul_f32 v[206:207], v[206:207], v[242:243]\n"
"CV 228,228,229\nMvav 220,212,44\nDT 204,3,19456\nCV 230,230,231\nMvav 220,216,52\nDT 206,7,19456\nAW 92,228\n"
"Mvav 220,220,60\nDT 208,3,20480\nAW 93,230\nMvav 220,224,68\nLD 245,44,84,0\nMvav 220,228,76\n"
"s_add_u32 s84, s84, s85\nDT 210,7,20480\nDT 212,3,21504\nMvav 220,232,84\nWL 6\nDT 214,7,21504\nCV 232,232,233\n"
"Mvav 220,240,92\nDT 216,3,22528\nCV 234,234,235\nMvav 220,244,100\nDT 218,7,22528\nAW 90,232\nMvavz 224,248,32\n"
"DT 220,3,23552\nAW 91,234\nMvav 224,252,40\nDT 222,7,23552\nCV 236,236,237\nMvav 224,172,48\nDT 224,3,24576\n"
"CV 238,238,239\nMvav 224,176,56\nDT 226,7,24576\nAW 94,236\nMvav 224,180,64\nDT 228,3,25600\nAW 95,238\n"
"Mvav 224,184,72\nDT 230,7,25600\nAW 14,164\nMvav 224,188,80\ns_waitcnt vmcnt(9)\nDT 232,3,26624\nAW 15,166\n"
"Mvav 224,192,88\ns_barrier\nDT 234,7,26624\nCV 168,168,169\nMvav 224,196,96\nLD 245,44,84,1024\n"
"Mvavz 228,248,36\ns_add_u32 s84, s84, s85\nDT 240,0,31744\nDT 242,4,31744\nMvav 228,252,44\nDT 244,0,32768\n"
"CV 170,170,171\nMvav 228,172,52\nDT 246,4,32768\nAW 30,168\nMvav 228,176,60\nDT 248,0,33792\nAW 31,170\n"
"Mvav 228,180,68\nEX 254,186\nMvav 228,184,76\nDT 250,4,33792\nDT 252,0,34816\nMvav 228,188,84\nWL 7\n"
"LD 245,44,84,2048\nMvav 228,192,92\ns_add_u32 s84, s84, s85\nDT 254,4,34816\nDT 172,0,35840\nMvav 228,196,100\n"
"EX 255,187\nMvavz 232,200,32\nDT 174,4,35840\nDT 176,0,36864\nMvav 232,204,40\nEX 20,188\nMvav 232,208,48\n"
"DT 178,4,36864\nDT 180,0,37888\nMvav 232,212,56\nEX 21,189\nMvav 232,216,64\nDT 182,4,37888\nDT 184,0,38912\n"
"Mvav 232,220,72\nLD 245,44,84,3072\nMvav 232,224,80\ns_mov_b32 s84, 3539328\nDT 186,4,38912\nDT 188,0,39936\n"
"Mvav 232,228,88\ns_addk_i32 m0, 4096\nDT 190,4,39936\nv_add_f32 v254, 1.0, v254\nMvav 232,232,96\n"
"DT 192,1,31744\nv_add_f32 v255, 1.0, v255\nMvavz 236,200,36\nDT 194,5,31744\nv_add_f32 v20, 1.0, v20\n"
"Mvav 236,204,44\nDT 196,1,32768\nv_add_f32 v21, 1.0, v21\nMvav 236,208,52\nLD 245,44,84,0\nMvav 236,212,60\n"
"s_add_u32 s84, s84, s85\nDT 198,5,32768\nDT 200,1,33792\nMvav 236,216,68\nRC 254,254\nMvav 236,220,76\n"
"DT 202,5,33792\nDT 204,1,34816\nMvav 236,224,84\nWL 6\nRC 255,255\nMvav 236,228,92\nDT 206,5,34816\n"
"DT 208,1,35840\nMvav 236,232,100\nRC 20,20\nMvav 208,240,128\nDT 210,5,35840\nDT 212,1,36864\nMvav 208,244,132\n"
"LD 245,44,84,1024\nMvav 208,248,136\ns_add_u32 s84, s84, s85\nDT 214,5,36864\nDT 216,1,37888\nMvav 208,252,140\n"
"RC 21,21\nMvav 208,172,144\nDT 218,5,37888\nDT 220,1,38912\nMvav 208,176,148\nEX 22,190\nMvav 208,180,152\n"
"DT 222,5,38912\nDT 224,1,39936\nMvav 208,184,156\nLD 245,44,84,2048\nMvav 208,188,160\ns_add_u32 s84, s84, s85\n"
"DT 226,5,39936\nDT 228,2,31744\nMvaa 212,240,136\nEX 23,191\nMvaa 212,244,140\nDT 230,6,31744\nDT 232,2,32768\n"
"Mvaa 212,248,144\nEX 24,192\nMvaa 212,252,148\nDT 234,6,32768\nDT 240,2,33792\nMvaa 212,172,152\nEX 25,193\n"
"Mvaa 212,176,156\nDT 242,6,33792\nDT 244,2,34816\nMvaa 212,180,160\nWL 6\nLD 245,44,84,3072\nMvaa 212,184,164\n"
"s_mov_b32 s84, 4718976\nDT 246,6,34816\nDT 248,2,35840\nMvaa 212,188,168\ns_addk_i32 m0, 4096\nDT 250,6,35840\n"
"v_add_f32 v22, 1.0, v22\nMvav 216,192,128\nDT 252,2,36864\nv_add_f32 v23, 1.0, v23\nMvav 216,196,132\n"
"DT 254,6,36864\nv_add_f32 v24, 1.0, v24\nMvav 216,200,136\nDT 172,2,37888\nv_add_f32 v25, 1.0, v25\n"
"Mvav 216,204,140\nLD 245,44,84,0\nMvav 216,208,144\ns_add_u32 s84, s84, s85\nDT 174,6,37888\nDT 176,2,38912\n"
"Mvav 216,212,148\nRC 22,22\nMvav 216,216,152\nDT 178,6,38912\nDT 180,2,39936\nMvav 216,220,156\nRC 23,23\n"
"Mvav 216,224,160\nDT 182,6,39936\nDT 184,3,31744\nMvaa 220,192,136\nRC 24,24\nMvaa 220,196,140\nDT 186,7,31744\n"
"DT 188,3,32768\nMvaa 220,200,144\nLD 245,44,84,1024\nMvaa 220,204,148\ns_movk_i32 s84, 384\nDT 190,7,32768\n"
"DT 192,3,33792\nMvaa 220,208,152\ns_addk_i32 m0, 2048\nRC 25,25\nMvaa 220,212,156\nDT 194,7,33792\n"
"DT 196,3,34816\nMvaa 220,216,160\nWL 6\nEX 28,194\nMvaa 220,220,164\nDT 198,7,34816\nDT 200,3,35840\n"
"Mvaa 220,224,168\nEX 29,195\nMvav 224,228,128\nDT 202,7,35840\nDT 204,3,36864\nMvav 224,232,132\nEX 30,196\n"
"Mvav 224,240,136\nDT 206,7,36864\nDT 208,3,37888\nMvav 224,244,140\nEX 31,197\nMvav 224,248,144\n"
"DT 210,7,37888\nDT 212,3,38912\nMvav 224,252,148\nDT 214,7,38912\nv_add_f32 v28, 1.0, v28\nMvav 224,172,152\n"
"s_waitcnt vmcnt(9)\nDT 216,3,39936\nv_add_f32 v29, 1.0, v29\nMvav 224,176,156\ns_barrier\n"
"s_bfm_b64 exec, 16, 0\nglobal_load_dword v175, v13, s[98:99] sc1\ns_mov_b64 exec, -1\nMvav 224,180,160\n"
"DT 218,7,39936\nDT 220,0,0\nMvaa 228,228,136\nDT 222,4,0\nv_add_f32 v30, 1.0, v30\nMvaa 228,232,140\n"
"DT 224,0,1024\nv_add_f32 v31, 1.0, v31\nMvaa 228,240,144\nLD 244,40,84,0\nMvaa 228,244,148\n"
"s_add_u32 s84, s84, s86\nDT 226,4,1024\nDT 228,0,2048\nMvaa 228,248,152\nRC 28,28\nMvaa 228,252,156\n"
"DT 230,4,2048\nDT 232,0,3072\nMvaa 228,172,160\nWL 6\nLD 244,40,84,1024\nMvaa 228,176,164\n"
"s_add_u32 s84, s84, s86\nDT 234,4,3072\nDT 240,0,4096\nMvaa 228,180,168\nRC 29,29\nMvav 232,184,128\n"
"DT 242,4,4096\nDT 244,0,5120\n"
"Lw6_%=:\ns_cbranch_execz Lret1_%=\nMvav 232,188,132\nRC 30,30\nMvav 232,192,136\nDT 246,4,5120\nDT 248,0,6144\n"
"Mvav 232,196,140\nRC 31,31\nMvav 232,200,144\nDT 250,4,6144\nDT 252,0,7168\nMvav 232,204,148\n"
"LD 244,40,84,2048\nMvav 232,208,152\ns_add_u32 s84, s84, s86\nDT 254,4,7168\nDT 172,0,8192\nMvav 232,212,156\n"
"EX 26,176\nMvav 232,216,160\nDT 174,4,8192\nDT 176,1,0\nMvaa 236,184,136\nEX 27,177\nMvaa 236,188,140\n"
"DT 178,5,0\nDT 180,1,1024\nMvaa 236,192,144\nLD 244,40,84,3072\nMvaa 236,196,148\ns_mov_b32 s84, 1048960\n"
"DT 182,5,1024\nDT 184,1,2048\nMvaa 236,200,152\ns_addk_i32 m0, 4096\nEX 246,178\nMvaa 236,204,156\n"
"DT 186,5,2048\nDT 188,1,3072\nMvaa 236,208,160\nWL 6\nEX 247,179\nMvaa 236,212,164\nDT 190,5,3072\n"
"DT 192,1,4096\nMvaa 236,216,168\nDT 194,5,4096\nv_add_f32 v26, 1.0, v26\nMvavz 104,220,32\nLD 244,40,84,0\n"
"Mvav 104,224,40\ns_add_u32 s84, s84, s86\nDT 196,1,5120\nDT 198,5,5120\nMvav 104,228,48\nDT 200,1,6144\n"
"v_add_f32 v27, 1.0, v27\nMvav 104,232,56\nDT 202,5,6144\nv_add_f32 v246, 1.0, v246\nMvav 104,240,64\n"
"DT 204,1,7168\nv_add_f32 v247, 1.0, v247\nMvav 104,244,72\nRC 26,26\nMvav 104,248,80\nDT 206,5,7168\n"
"DT 208,1,8192\nMvav 104,252,88\nLD 244,40,84,1024\nMvav 104,172,96\ns_add_u32 s84, s84, s86\nDT 210,5,8192\n"
"DT 212,2,0\nMvavz 108,220,36\nRC 27,27\nMvav 108,224,44\nDT 214,6,0\nDT 216,2,1024\nMvav 108,228,52\n"
"RC 246,246\nMvav 108,232,60\nDT 218,6,1024\nDT 220,2,2048\nMvav 108,240,68\nRC 247,247\nMvav 108,244,76\n"
"DT 222,6,2048\nDT 224,2,3072\nMvav 108,248,84\nWL 6\nLD 244,40,84,2048\nMvav 108,252,92\n"
"s_add_u32 s84, s84, s86\nDT 226,6,3072\nDT 228,2,4096\nMvav 108,172,100\nEX 248,180\nMvavz 112,176,32\n"
"DT 230,6,4096\nDT 232,2,5120\nMvav 112,180,40\nEX 249,181\nMvav 112,184,48\nDT 234,6,5120\nDT 240,2,6144\n"
"Mvav 112,188,56\nLD 244,40,84,3072\nMvav 112,192,64\ns_mov_b32 s84, 2097536\nDT 242,6,6144\nDT 244,2,7168\n"
"Mvav 112,196,72\ns_addk_i32 m0, 4096\nEX 250,182\nMvav 112,200,80\nDT 246,6,7168\nDT 248,2,8192\n"
"Mvav 112,204,88\nEX 251,183\nMvav 112,208,96\nDT 250,6,8192\nDT 252,3,0\nMvavz 116,176,36\nDT 254,7,0\n"
"v_add_f32 v248, 1.0, v248\nMvav 116,180,44\nLD 244,40,84,0\nMvav 116,184,52\ns_mov_b32 s84, 2359680\n"
"DT 172,3,1024\nDT 174,7,1024\nMvav 116,188,60\ns_addk_i32 m0, 5120\nDT 176,3,2048\nv_add_f32 v249, 1.0, v249\n"
"Mvav 116,192,68\nDT 178,7,2048\ns_waitcnt vmcnt(9)\nv_cmp_eq_u32 vcc, v172, v175\nMvav 116,196,76\n"
"s_and_b32 s92, vcc_lo, 0xffff\ns_cbranch_scc1 Lpr0s_%=\n"
"Lpr0r_%=:\nDT 180,3,3072\nv_and_b32 v175, 1, v175\nMvav 116,200,84\nWL 6\nDT 182,7,3072\n"
"v_cmp_eq_u32 vcc, 1, v175\nMvav 116,204,92\ns_and_b32 s78, vcc_lo, 0xffff\ns_mov_b64 exec, s[0:1]\n"
"global_atomic_add v13, v174, s[98:99]\ns_mov_b64 exec, -1\nMvav 116,208,100\ns_bcnt1_i32_b32 s78, s78\n"
"DT 184,3,4096\nDT 186,7,4096\nMvavz 120,212,32\ns_cmp_ge_u32 s78, 9\ns_cselect_b64 vcc, -1, 0\nDT 188,3,5120\n"
"v_add_f32 v250, 1.0, v250\nMvav 120,216,40\ns_and_b32 s78, vcc_lo, 0x2000\nDT 190,7,5120\n"
"v_add_f32 v251, 1.0, v251\nMvav 120,220,48\ns_or_b32 s60, s60, s78\nRC 248,248\nMvav 120,224,56\n"
"s_or_b32 s64, s64, s78\nDT 192,3,6144\nDT 194,7,6144\nMvav 120,228,64\ns_or_b32 s68, s68, s78\nRC 249,249\n"
"Mvav 120,232,72\ns_or_b32 s98, s98, s78\nDT 196,3,7168\nDT 198,7,7168\nMvav 120,240,80\ns_barrier\nRC 250,250\n"
"Mvav 120,244,88\nDT 200,3,8192\nDT 202,7,8192\nMvav 120,248,96\nLD 244,40,84,0\nMvavz 124,212,36\n"
"s_add_u32 s84, s84, s86\nDT 204,0,9216\nDT 206,4,9216\nMvav 124,216,44\nRC 251,251\nEX 252,184\n"
"v_cndmask_b32 v175, v172, v173, vcc\nv_pk_mul_f32 v[208:209], v[208:209], v[16:17]\n"
"v_pk_mul_f32 v[210:211], v[210:211], v[16:17]\nv_pk_mul_f32 v[212:213], v[212:213], v[18:19]\n"
"v_pk_mul_f32 v[214:215], v[214:215], v[18:19]\nv_pk_mul_f32 v[216:217], v[216:217], v[16:17]\n"
"v_pk_mul_f32 v[218:219], v[218:219], v[16:17]\nv_pk_mul_f32 v[220:221], v[220:221], v[18:19]\n"
"v_pk_mul_f32 v[222:223], v[222:223], v[18:19]\nv_pk_mul_f32 v[224:225], v[224:225], v[16:17]\n"
"v_pk_mul_f32 v[226:227], v[226:227], v[16:17]\nv_pk_mul_f32 v[228:229], v[228:229], v[18:19]\n"
"v_pk_mul_f32 v[230:231], v[230:231], v[18:19]\nv_pk_mul_f32 v[232:233], v[232:233], v[16:17]\n"
"v_pk_mul_f32 v[234:235], v[234:235], v[16:17]\nv_pk_mul_f32 v[236:237], v[236:237], v[18:19]\n"
"v_pk_mul_f32 v[238:239], v[238:239], v[18:19]\nv_pk_mul_f32 v[186:187], v[186:187], v[254:255]\n"
"v_pk_mul_f32 v[188:189], v[188:189], v[20:21]\nv_pk_mul_f32 v[190:191], v[190:191], v[22:23]\n"
"v_pk_mul_f32 v[192:193], v[192:193], v[24:25]\nv_pk_mul_f32 v[194:195], v[194:195], v[28:29]\n"
"v_pk_mul_f32 v[196:197], v[196:197], v[30:31]\nv_pk_mul_f32 v[176:177], v[176:177], v[26:27]\n"
"v_pk_mul_f32 v[178:179], v[178:179], v[246:247]\nv_pk_mul_f32 v[180:181], v[180:181], v[248:249]\n"
"v_pk_mul_f32 v[182:183], v[182:183], v[250:251]\nCV 208,208,209\nMvav 124,220,52\nDT 208,0,10240\n"
"DT 210,4,10240\nMvav 124,224,60\nDT 212,0,11264\nCV 210,210,211\nMvav 124,228,68\nDT 214,4,11264\nAW 32,208\n"
"Mvav 124,232,76\nLD 244,40,84,1024\nMvav 124,240,84\nWL 6\nDT 216,0,12288\nDT 218,4,12288\nMvav 124,244,92\n"
"s_add_u32 s84, s84, s86\nDT 220,0,13312\nAW 33,210\nMvav 124,248,100\nDT 222,4,13312\nCV 212,212,213\n"
"Mvavz 164,252,32\nDT 224,0,14336\nCV 214,214,215\nMvav 164,172,40\nDT 226,4,14336\nAW 48,212\nMvav 164,176,48\n"
"DT 228,0,15360\nAW 49,214\nMvav 164,180,56\nLD 244,40,84,2048\nMvav 164,184,64\ns_add_u32 s84, s84, s86\n"
"DT 230,4,15360\nDT 232,0,16384\nMvav 164,188,72\nDT 234,4,16384\nCV 216,216,217\nMvav 164,192,80\n"
"DT 240,0,17408\nCV 218,218,219\nMvav 164,196,88\nDT 242,4,17408\nAW 34,216\nMvav 164,200,96\nDT 244,1,9216\n"
"AW 35,218\nMvavz 168,252,36\nDT 246,5,9216\nCV 220,220,221\nMvav 168,172,44\nLD 244,40,84,3072\n"
"Mvav 168,176,52\ns_mov_b32 s84, 3408256\nDT 248,1,10240\nDT 250,5,10240\nMvav 168,180,60\ns_addk_i32 m0, 4096\n"
"DT 252,1,11264\nCV 222,222,223\nMvav 168,184,68\nDT 254,5,11264\nAW 50,220\nMvav 168,188,76\nDT 172,1,12288\n"
"AW 51,222\nMvav 168,192,84\nWL 6\nDT 174,5,12288\nCV 224,224,225\nMvav 168,196,92\nDT 176,1,13312\n"
"CV 226,226,227\nMvav 168,200,100\nLD 244,40,84,0\nMvav 104,204,128\ns_add_u32 s84, s84, s86\nDT 178,5,13312\n"
"DT 180,1,14336\nMvav 104,208,132\nDT 182,5,14336\nAW 36,224\nMvav 104,212,136\nDT 184,1,15360\nAW 37,226\n"
"Mvav 104,216,140\nDT 186,5,15360\nCV 228,228,229\nMvav 104,220,144\nDT 188,1,16384\nCV 230,230,231\n"
"Mvav 104,224,148\nDT 190,5,16384\nAW 52,228\nMvav 104,228,152\nLD 244,40,84,1024\nMvav 104,232,156\n"
"s_add_u32 s84, s84, s86\nDT 192,1,17408\nDT 194,5,17408\nMvav 104,240,160\nDT 196,2,9216\nAW 53,230\n"
"Mvaa 108,204,136\nDT 198,6,9216\nCV 232,232,233\nMvaa 108,208,140\nDT 200,2,10240\nCV 234,234,235\n"
"Mvaa 108,212,144\nDT 202,6,10240\nAW 38,232\nMvaa 108,216,148\nDT 204,2,11264\nAW 39,234\nMvaa 108,220,152\n"
"LD 244,40,84,2048\nMvaa 108,224,156\ns_add_u32 s84, s84, s86\nDT 206,6,11264\nDT 208,2,12288\nMvaa 108,228,160\n"
"WL 6\nDT 210,6,12288\nCV 236,236,237\nMvaa 108,232,164\nDT 212,2,13312\nCV 238,238,239\nMvaa 108,240,168\n"
"DT 214,6,13312\nAW 54,236\nMvav 112,244,128\nDT 216,2,14336\nAW 55,238\nMvav 112,248,132\nEX 26,198\n"
"Mvav 112,252,136\nDT 218,6,14336\nDT 220,2,15360\nMvav 112,172,140\nLD 244,40,84,3072\nMvav 112,176,144\n"
"s_mov_b32 s84, 4456832\nDT 222,6,15360\nDT 224,2,16384\nMvav 112,180,148\ns_addk_i32 m0, 4096\nEX 27,199\n"
"Mvav 112,184,152\nDT 226,6,16384\nDT 228,2,17408\nMvav 112,188,156\nEX 246,200\nMvav 112,192,160\n"
"DT 230,6,17408\nDT 232,3,9216\nMvaa 116,244,136\nLD 244,40,84,0\nMvaa 116,248,140\ns_movk_i32 s84, 512\n"
"DT 234,7,9216\nDT 240,3,10240\nMvaa 116,252,144\ns_add_u32 m0, s72, 0\nEX 247,201\nMvaa 116,172,148\n"
"DT 242,7,10240\nDT 244,3,11264\nMvaa 116,176,152\nDT 246,7,11264\nv_add_f32 v26, 1.0, v26\nMvaa 116,180,156\n"
"DT 248,3,12288\nv_add_f32 v27, 1.0, v27\nMvaa 116,184,160\nWL 6\nDT 250,7,12288\nv_add_f32 v246, 1.0, v246\n"
"Mvaa 116,188,164\nDT 252,3,13312\nv_add_f32 v247, 1.0, v247\nMvaa 116,192,168\nRC 26,26\nMvav 120,196,128\n"
"DT 254,7,13312\nDT 172,3,14336\nMvav 120,200,132\nRC 27,27\nMvav 120,204,136\nDT 174,7,14336\nDT 176,3,15360\n"
"Mvav 120,208,140\nRC 246,246\nMvav 120,212,144\nDT 178,7,15360\nDT 180,3,16384\nMvav 120,216,148\nRC 247,247\n"
"Mvav 120,220,152\ns_waitcnt vmcnt(10)\nDT 182,7,16384\nDT 184,3,17408\nMvav 120,224,156\ns_barrier\nEX 248,202\n"
"Mvav 120,228,160\nDT 186,7,17408\nDT 188,0,18432\nMvaa 124,196,136\nLD 245,44,84,0\nMvaa 124,200,140\n"
"s_add_u32 s84, s84, s85\nDT 190,4,18432\nDT 192,0,19456\nMvaa 124,204,144\nEX 249,203\nMvaa 124,208,148\n"
"DT 194,4,19456\nDT 196,0,20480\nMvaa 124,212,152\nEX 250,204\nMvaa 124,216,156\nDT 198,4,20480\nDT 200,0,21504\n"
"Mvaa 124,220,160\nWL 6\nLD 245,44,84,1024\nMvaa 124,224,164\ns_add_u32 s84, s84, s85\nDT 202,4,21504\n"
"DT 204,0,22528\nMvaa 124,228,168\nEX 251,205\nMvav 164,232,128\nDT 206,4,22528\nDT 208,0,23552\n"
"Mvav 164,240,132\nDT 210,4,23552\nv_add_f32 v248, 1.0, v248\nMvav 164,244,136\nDT 212,0,24576\n"
"v_add_f32 v249, 1.0, v249\nMvav 164,248,140\nDT 214,4,24576\nv_add_f32 v250, 1.0, v250\nMvav 164,252,144\n"
"LD 245,44,84,2048\nMvav 164,172,148\ns_add_u32 s84, s84, s85\nDT 216,0,25600\nDT 218,4,25600\nMvav 164,176,152\n"
"DT 220,0,26624\nv_add_f32 v251, 1.0, v251\nMvav 164,180,156\nRC 248,248\nMvav 164,184,160\nDT 222,4,26624\n"
"DT 224,1,18432\nMvaa 168,232,136\nRC 249,249\nMvaa 168,240,140\nDT 226,5,18432\nDT 228,1,19456\n"
"Mvaa 168,244,144\nLD 245,44,84,3072\nMvaa 168,248,148\ns_mov_b32 s84, 1180160\nDT 230,5,19456\nDT 232,1,20480\n"
"Mvaa 168,252,152\ns_addk_i32 m0, 4096\nRC 250,250\nMvaa 168,172,156\nDT 234,5,20480\nDT 240,1,21504\n"
"Mvaa 168,176,160\nWL 6\nRC 251,251\nMvaa 168,180,164\nDT 242,5,21504\nDT 244,1,22528\nMvaa 168,184,168\n"
"EX 253,185\nMvavz 208,188,32\nDT 246,5,22528\nDT 248,1,23552\nMvav 208,192,40\nLD 245,44,84,0\nMvav 208,196,48\n"
"s_add_u32 s84, s84, s85\nDT 250,5,23552\nDT 252,1,24576\nMvav 208,200,56\nDT 254,5,24576\n"
"v_add_f32 v252, 1.0, v252\nMvav 208,204,64\nDT 172,1,25600\nv_add_f32 v253, 1.0, v253\nMvav 208,208,72\n"
"RC 252,252\nMvav 208,212,80\nDT 174,5,25600\nDT 176,1,26624\nMvav 208,216,88\nLD 245,44,84,1024\n"
"Mvav 208,220,96\ns_add_u32 s84, s84, s85\nDT 178,5,26624\nDT 180,2,18432\nMvavz 212,188,36\nRC 253,253\n"
"Mvav 212,192,44\nDT 182,6,18432\nDT 184,2,19456\nMvav 212,196,52\nDT 186,6,19456\nCV 176,176,177\n"
"Mvav 212,200,60\nDT 188,2,20480\nCV 178,178,179\nMvav 212,204,68\nDT 190,6,20480\nAW 96,176\nMvav 212,208,76\n"
"LD 245,44,84,2048\nMvav 212,212,84\nWL 5\nDT 192,2,21504\nDT 194,6,21504\nMvav 212,216,92\n"
"s_add_u32 s84, s84, s85\nDT 196,2,22528\nAW 97,178\nMvav 212,220,100\nDT 198,6,22528\nCV 180,180,181\n"
"Mvavz 216,224,32\nDT 200,2,23552\nCV 182,182,183\ns_nop 0\n"
"Lw7_%=:\ns_cbranch_execz Lret1_%=\nMvav 216,228,40\nDT 202,6,23552\nAW 100,180\nMvav 216,232,48\n"
"DT 204,2,24576\nAW 101,182\nMvav 216,240,56\nLD 245,44,84,3072\nMvav 216,244,64\ns_mov_b32 s84, 2359808\n"
"DT 206,6,24576\nDT 208,2,25600\nMvav 216,248,72\ns_addk_i32 m0, 4096\nDT 210,6,25600\nCV 186,186,187\n"
"Mvav 216,252,80\nDT 212,2,26624\nAW 99,186\nCV 188,188,189\nCV 190,190,191\nAW 102,188\n"
"v_pk_mul_f32 v[104:105], v[104:105], v[240:241]\nv_pk_mul_f32 v[106:107], v[106:107], v[240:241]\n"
"v_pk_mul_f32 v[108:109], v[108:109], v[242:243]\nv_pk_mul_f32 v[110:111], v[110:111], v[242:243]\n"
"v_pk_mul_f32 v[112:113], v[112:113], v[240:241]\nv_pk_mul_f32 v[114:115], v[114:115], v[240:241]\n"
"v_pk_mul_f32 v[116:117], v[116:117], v[242:243]\nv_pk_mul_f32 v[118:119], v[118:119], v[242:243]\n"
"v_pk_mul_f32 v[198:199], v[198:199], v[26:27]\nv_pk_mul_f32 v[200:201], v[200:201], v[246:247]\n"
"v_pk_mul_f32 v[120:121], v[120:121], v[240:241]\nv_pk_mul_f32 v[122:123], v[122:123], v[240:241]\n"
"v_pk_mul_f32 v[202:203], v[202:203], v[248:249]\nv_pk_mul_f32 v[204:205], v[204:205], v[250:251]\n"
"v_pk_mul_f32 v[184:185], v[184:185], v[252:253]\nv_pk_mul_f32 v[124:125], v[124:125], v[242:243]\n"
"v_pk_mul_f32 v[126:127], v[126:127], v[242:243]\nv_pk_mul_f32 v[164:165], v[164:165], v[240:241]\n"
"v_pk_mul_f32 v[166:167], v[166:167], v[240:241]\nv_pk_mul_f32 v[168:169], v[168:169], v[242:243]\n"
"v_pk_mul_f32 v[170:171], v[170:171], v[242:243]\nCV 184,184,185\nMvav 216,172,88\nEX 254,104\nMvav 216,176,96\n"
"DT 214,6,26624\nDT 216,3,18432\nMvavz 220,224,36\nEX 255,105\nMvav 220,228,44\nDT 218,7,18432\nDT 220,3,19456\n"
"Mvav 220,232,52\nLD 245,44,84,0\nMvav 220,240,60\ns_add_u32 s84, s84, s85\nDT 222,7,19456\nDT 224,3,20480\n"
"Mvav 220,244,68\nEX 20,106\nMvav 220,248,76\nDT 226,7,20480\nDT 228,3,21504\nMvav 220,252,84\nWL 6\nEX 21,107\n"
"Mvav 220,172,92\nDT 230,7,21504\nDT 232,3,22528\nMvav 220,176,100\nEX 22,108\nMvavz 224,180,32\nDT 234,7,22528\n"
"DT 240,3,23552\nMvav 224,184,40\nEX 23,109\nMvav 224,188,48\nDT 242,7,23552\nDT 244,3,24576\nMvav 224,192,56\n"
"EX 24,110\nMvav 224,196,64\nDT 246,7,24576\nDT 248,3,25600\nMvav 224,200,72\nEX 25,111\nMvav 224,204,80\n"
"s_waitcnt vmcnt(9)\nDT 250,7,25600\nDT 252,3,26624\nMvav 224,208,88\ns_barrier\nEX 28,112\nMvav 224,212,96\n"
"DT 254,7,26624\nDT 172,0,31744\nMvavz 228,180,36\nLD 245,44,84,1024\nMvav 228,184,44\ns_add_u32 s84, s84, s85\n"
"DT 174,4,31744\nDT 176,0,32768\nMvav 228,188,52\nEX 29,113\nMvav 228,192,60\nDT 178,4,32768\nDT 180,0,33792\n"
"Mvav 228,196,68\nEX 30,114\nMvav 228,200,76\nDT 182,4,33792\nDT 184,0,34816\nMvav 228,204,84\nWL 6\n"
"LD 245,44,84,2048\nMvav 228,208,92\ns_add_u32 s84, s84, s85\nDT 186,4,34816\nDT 188,0,35840\nMvav 228,212,100\n"
"EX 31,115\nMvavz 232,216,32\nDT 190,4,35840\nDT 192,0,36864\nMvav 232,220,40\nEX 26,116\nMvav 232,224,48\n"
"DT 194,4,36864\nDT 196,0,37888\nMvav 232,228,56\nEX 27,117\nMvav 232,232,64\nDT 198,4,37888\nDT 200,0,38912\n"
"Mvav 232,240,72\nLD 245,44,84,3072\nMvav 232,244,80\ns_mov_b32 s84, 3539456\nDT 202,4,38912\nDT 204,0,39936\n"
"Mvav 232,248,88\ns_addk_i32 m0, 4096\nEX 246,118\nMvav 232,252,96\nDT 206,4,39936\nDT 208,1,31744\n"
"Mvavz 236,216,36\nEX 247,119\nMvav 236,220,44\nDT 210,5,31744\nDT 212,1,32768\nMvav 236,224,52\nLD 245,44,84,0\n"
"Mvav 236,228,60\ns_add_u32 s84, s84, s85\nDT 214,5,32768\nDT 216,1,33792\nMvav 236,232,68\nEX 248,120\n"
"Mvav 236,240,76\nDT 218,5,33792\nDT 220,1,34816\nMvav 236,244,84\nWL 6\nEX 249,121\nMvav 236,248,92\n"
"DT 222,5,34816\nDT 224,1,35840\nMvav 236,252,100\nEX 250,122\nMvav 208,172,128\nDT 226,5,35840\nDT 228,1,36864\n"
"Mvav 208,176,132\nLD 245,44,84,1024\nMvav 208,180,136\ns_add_u32 s84, s84, s85\nDT 230,5,36864\nDT 232,1,37888\n"
"Mvav 208,184,140\nEX 251,123\nMvav 208,188,144\nDT 234,5,37888\nDT 240,1,38912\nMvav 208,192,148\nEX 252,206\n"
"Mvav 208,196,152\nDT 242,5,38912\nDT 244,1,39936\nMvav 208,200,156\nLD 245,44,84,2048\nMvav 208,204,160\n"
"s_add_u32 s84, s84, s85\nDT 246,5,39936\nDT 248,2,31744\nMvaa 212,172,136\nEX 253,207\nMvaa 212,176,140\n"
"DT 250,6,31744\nDT 252,2,32768\nMvaa 212,180,144\nDT 254,6,32768\nv_add_f32 v252, 1.0, v252\nMvaa 212,184,148\n"
"DT 172,2,33792\nv_add_f32 v253, 1.0, v253\nMvaa 212,188,152\nRC 252,252\nMvaa 212,192,156\nDT 174,6,33792\n"
"DT 176,2,34816\nMvaa 212,196,160\nWL 6\nLD 245,44,84,3072\nMvaa 212,200,164\ns_mov_b32 s84, 4719104\n"
"DT 178,6,34816\nDT 180,2,35840\nMvaa 212,204,168\ns_addk_i32 m0, 4096\nRC 253,253\nMvav 216,208,128\n"
"DT 182,6,35840\nDT 184,2,36864\nMvav 216,212,132\nDT 186,6,36864\nAW 98,184\nMvav 216,216,136\nDT 188,2,37888\n"
"AW 103,190\nMvav 216,220,140\nLD 245,44,84,0\nMvav 216,224,144\ns_add_u32 s84, s84, s85\nDT 190,6,37888\n"
"DT 192,2,38912\nMvav 216,228,148\ns_mov_b32 s85, 35840\nDT 194,6,38912\nCV 192,192,193\nMvav 216,232,152\n"
"DT 196,2,39936\nCV 194,194,195\nMvav 216,240,156\nDT 198,6,39936\nAW 104,192\nMvav 216,244,160\nDT 200,3,31744\n"
"AW 105,194\nCV 196,196,197\nCV 198,198,199\nAW 108,196\nv_pk_add_f32 v[254:255], v[254:255], s[4:5]\n"
"v_pk_add_f32 v[20:21], v[20:21], s[4:5]\nv_pk_add_f32 v[22:23], v[22:23], s[4:5]\n"
"v_pk_add_f32 v[24:25], v[24:25], s[4:5]\nv_pk_add_f32 v[28:29], v[28:29], s[4:5]\n"
"v_pk_add_f32 v[30:31], v[30:31], s[4:5]\nv_pk_add_f32 v[26:27], v[26:27], s[4:5]\n"
"v_pk_add_f32 v[246:247], v[246:247], s[4:5]\nv_pk_add_f32 v[248:249], v[248:249], s[4:5]\n"
"v_pk_add_f32 v[250:251], v[250:251], s[4:5]\nv_pk_mul_f32 v[206:207], v[206:207], v[252:253]\n"
"v_pk_mul_f32 v[208:209], v[208:209], v[16:17]\nv_pk_mul_f32 v[210:211], v[210:211], v[16:17]\n"
"v_pk_mul_f32 v[216:217], v[216:217], v[16:17]\nv_pk_mul_f32 v[218:219], v[218:219], v[16:17]\n"
"v_pk_mul_f32 v[212:213], v[212:213], v[18:19]\nv_pk_mul_f32 v[214:215], v[214:215], v[18:19]\nCV 208,208,209\n"
"Mvaa 220,208,136\nRC 254,254\nMvaa 220,212,140\nDT 202,7,31744\nDT 204,3,32768\nMvaa 220,216,144\n"
"LD 245,44,84,1024\nMvaa 220,220,148\ns_movk_i32 s84, 0\nDT 206,7,32768\nDT 208,3,33792\nMvaa 220,224,152\n"
"s_add_u32 m0, s73, 27456\nRC 255,255\nMvaa 220,228,156\nDT 210,7,33792\nDT 212,3,34816\nMvaa 220,232,160\nWL 6\n"
"RC 20,20\nMvaa 220,240,164\nDT 214,7,34816\nDT 216,3,35840\nMvaa 220,244,168\nRC 21,21\nMvav 224,248,128\n"
"DT 218,7,35840\nDT 220,3,36864\nMvav 224,252,132\nRC 22,22\nMvav 224,172,136\nDT 222,7,36864\nDT 224,3,37888\n"
"Mvav 224,176,140\nRC 23,23\nMvav 224,180,144\nDT 226,7,37888\nDT 228,3,38912\nMvav 224,184,148\nRC 24,24\n"
"Mvav 224,188,152\ns_waitcnt vmcnt(9)\nDT 230,7,38912\nDT 232,3,39936\nMvav 224,192,156\ns_barrier\nRC 25,25\n"
"Mvav 224,196,160\nDT 234,7,39936\nDT 240,0,0\nMvaa 228,248,136\nLD 10,52,84,0\nMvaa 228,252,140\n"
"s_movk_i32 s84, 3072\nDT 242,4,0\nDT 244,0,1024\nMvaa 228,172,144\nRC 28,28\nMvaa 228,176,148\nDT 246,4,1024\n"
"DT 248,0,2048\nMvaa 228,180,152\nRC 29,29\nMvaa 228,184,156\nDT 250,4,2048\nDT 252,0,3072\nMvaa 228,188,160\n"
"WL 6\nLD 10,52,84,1024\nMvaa 228,192,164\ns_movk_i32 s84, 6144\nDT 254,4,3072\nDT 172,0,4096\nMvaa 228,196,168\n"
"RC 30,30\nMvav 232,200,128\nDT 174,4,4096\nDT 176,0,5120\nMvav 232,204,132\nRC 31,31\nMvav 232,208,136\n"
"DT 178,4,5120\nDT 180,0,6144\nMvav 232,212,140\nRC 26,26\nMvav 232,216,144\nDT 182,4,6144\nDT 184,0,7168\n"
"Mvav 232,220,148\nLD 10,52,84,2048\nMvav 232,224,152\ns_movk_i32 s84, 9216\nDT 186,4,7168\nDT 188,0,8192\n"
"Mvav 232,228,156\nRC 27,27\nMvav 232,232,160\nDT 190,4,8192\nDT 192,1,0\nMvaa 236,200,136\nRC 246,246\n"
"Mvaa 236,204,140\nDT 194,5,0\nDT 196,1,1024\nMvaa 236,208,144\nLD 10,52,84,3072\nMvaa 236,212,148\n"
"s_movk_i32 s84, 16384\nDT 198,5,1024\nDT 200,1,2048\nMvaa 236,216,152\ns_addk_i32 m0, -4928\nRC 247,247\n"
"Mvaa 236,220,156\nDT 202,5,2048\nDT 204,1,3072\nMvaa 236,224,160\nWL 6\nRC 248,248\nMvaa 236,228,164\n"
"DT 206,5,3072\nDT 208,1,4096\nMvaa 236,232,168\nRC 249,249\nRC 250,250\nCV 210,210,211\n"
"v_pk_mul_f32 v[104:105], v[104:105], v[254:255]\nv_pk_mul_f32 v[106:107], v[106:107], v[20:21]\n"
"v_pk_mul_f32 v[108:109], v[108:109], v[22:23]\nv_pk_mul_f32 v[110:111], v[110:111], v[24:25]\n"
"v_pk_mul_f32 v[112:113], v[112:113], v[28:29]\nv_pk_mul_f32 v[114:115], v[114:115], v[30:31]\n"
"v_pk_mul_f32 v[116:117], v[116:117], v[26:27]\nv_pk_mul_f32 v[118:119], v[118:119], v[246:247]\n"
"v_pk_mul_f32 v[120:121], v[120:121], v[248:249]\nv_pk_mul_f32 v[224:225], v[224:225], v[16:17]\n"
"v_pk_mul_f32 v[226:227], v[226:227], v[16:17]\nv_pk_mul_f32 v[232:233], v[232:233], v[16:17]\n"
"v_pk_mul_f32 v[234:235], v[234:235], v[16:17]\nv_pk_mul_f32 v[220:221], v[220:221], v[18:19]\n"
"v_pk_mul_f32 v[222:223], v[222:223], v[18:19]\nv_pk_mul_f32 v[228:229], v[228:229], v[18:19]\n"
"v_pk_mul_f32 v[230:231], v[230:231], v[18:19]\nv_pk_mul_f32 v[236:237], v[236:237], v[18:19]\n"
"v_pk_mul_f32 v[238:239], v[238:239], v[18:19]\nCV 104,104,105\nCV 106,106,107\nAW 112,104\nAW 113,106\n"
"Mvavz 104,240,32\nDT 210,5,4096\nDT 212,1,5120\nMvav 104,244,40\nDT 214,5,5120\nCV 108,108,109\n"
"Mvav 104,248,48\nDT 216,1,6144\nCV 110,110,111\nMvav 104,252,56\nDT 218,5,6144\nAW 116,108\nMvav 104,172,64\n"
"DT 220,1,7168\nAW 117,110\nMvav 104,176,72\nDT 222,5,7168\nCV 112,112,113\nMvav 104,180,80\nDT 224,1,8192\n"
"CV 114,114,115\nMvav 104,184,88\nDT 226,5,8192\nAW 114,112\nMvav 104,188,96\nDT 228,2,0\nAW 115,114\n"
"Mvavz 108,240,36\nDT 230,6,0\nCV 116,116,117\nMvav 108,244,44\nDT 232,2,1024\nCV 118,118,119\nMvav 108,248,52\n"
"DT 234,6,1024\nAW 118,116\nMvav 108,252,60\nDT 240,2,2048\nAW 119,118\nMvav 108,172,68\nRC 251,251\n"
"Mvav 108,176,76\nDT 242,6,2048\nDT 244,2,3072\nMvav 108,180,84\nWL 6\nDT 246,6,3072\nCV 120,120,121\n"
"Mvav 108,184,92\nDT 248,2,4096\nAW 120,120\nMvav 108,188,100\nEX 252,124\nMvavz 112,192,32\nDT 250,6,4096\n"
"DT 252,2,5120\nMvav 112,196,40\nEX 253,125\nMvav 112,200,48\nDT 254,6,5120\nDT 172,2,6144\nMvav 112,204,56\n"
"EX 254,126\nMvav 112,208,64\nDT 174,6,6144\nDT 176,2,7168\nMvav 112,212,72\nEX 255,127\nMvav 112,216,80\n"
"DT 178,6,7168\nDT 180,2,8192\nMvav 112,220,88\nEX 20,164\nMvav 112,224,96\nDT 182,6,8192\nDT 184,3,0\n"
"Mvavz 116,192,36\nEX 21,165\nMvav 116,196,44\nDT 186,7,0\nDT 188,3,1024\nMvav 116,200,52\nEX 22,166\n"
"Mvav 116,204,60\nDT 190,7,1024\nDT 192,3,2048\nMvav 116,208,68\nEX 23,167\nMvav 116,212,76\nDT 194,7,2048\n"
"DT 196,3,3072\nMvav 116,216,84\nWL 6\nEX 24,168\nMvav 116,220,92\nDT 198,7,3072\nDT 200,3,4096\n"
"Mvav 116,224,100\nEX 25,169\nEX 28,170\nCV 216,216,217\nv_pk_mul_f32 v[122:123], v[122:123], v[250:251]\n"
"v_pk_add_f32 v[252:253], v[252:253], s[4:5]\nv_pk_add_f32 v[254:255], v[254:255], s[4:5]\n"
"v_pk_add_f32 v[20:21], v[20:21], s[4:5]\nv_pk_add_f32 v[22:23], v[22:23], s[4:5]\n"
"v_pk_add_f32 v[24:25], v[24:25], s[4:5]\nCV 122,122,123\nAW 121,122\ns_nop 0\nMvavz 120,228,32\nDT 202,7,4096\n"
"DT 204,3,5120\nMvav 120,232,40\nRC 252,252\nMvav 120,240,48\nDT 206,7,5120\nDT 208,3,6144\nMvav 120,244,56\n"
"RC 253,253\nMvav 120,248,64\nDT 210,7,6144\nDT 212,3,7168\nMvav 120,252,72\nRC 254,254\nMvav 120,172,80\n"
"DT 214,7,7168\nDT 216,3,8192\nRC 255,255\nMvav 120,176,88\nRC 20,20\nMvav 120,180,96\nDT 218,7,8192\n"
"CV 218,218,219\nRC 21,21\nAW 40,208\nv_pk_mul_f32 v[124:125], v[124:125], v[252:253]\n"
"Lw8_%=:\ns_cbranch_execz Lret1_%=\nv_pk_mul_f32 v[126:127], v[126:127], v[254:255]\n"
"v_pk_mul_f32 v[164:165], v[164:165], v[20:21]\nCV 124,124,125\nCV 126,126,127\nAW 124,124\nAW 125,126\n"
"Mvavz 124,228,36\nRC 22,22\nMvav 124,232,44\nRC 23,23\nMvav 124,240,52\ns_waitcnt vmcnt(0)\nCV 164,164,165\n"
"AW 122,164\nMvav 124,244,60\ns_barrier\nEX 29,171\nMvav 124,248,68\nDK 220,8,27456\nAW 41,210\nMvav 124,252,76\n"
"RC 24,24\nMvav 124,172,84\nWL 1\nDK 222,9,27456\nDK 224,8,27520\nMvav 124,176,92\nRC 25,25\nMvav 124,180,100\n"
"DK 226,9,27520\nDK 228,8,27584\nAW 42,216\nAW 43,218\nCV 224,224,225\n"
"v_pk_mul_f32 v[166:167], v[166:167], v[22:23]\nv_pk_add_f32 v[28:29], v[28:29], s[4:5]\n"
"v_pk_mul_f32 v[168:169], v[168:169], v[24:25]\nCV 166,166,167\nAW 123,166\ns_nop 0\nMvavz 164,184,32\nRC 28,28\n"
"Mvav 164,188,40\nDK 230,9,27584\nDK 232,8,27648\nMvav 164,192,48\nRC 29,29\nMvav 164,196,56\nDK 234,9,27648\n"
"CV 168,168,169\nMvav 164,200,64\nAW 126,168\nCV 226,226,227\nMvav 164,204,72\nLD 10,52,84,0\nMvav 164,208,80\n"
"s_movk_i32 s84, 19456\nDT 240,0,9216\nCV 232,232,233\nMvav 164,212,88\nDT 242,4,9216\nCV 234,234,235\n"
"Mvav 164,216,96\nDT 244,0,10240\nAW 44,224\nAW 45,226\nAW 46,232\nAW 47,234\n"
"v_pk_mul_f32 v[170:171], v[170:171], v[28:29]\nCV 170,170,171\nAW 127,170\ns_nop 0\nMvavz 168,184,36\n"
"LD 10,52,84,1024\nMvav 168,188,44\ns_movk_i32 s84, 22528\nDT 246,4,10240\nDT 248,0,11264\nMvav 168,192,52\n"
"DT 250,4,11264\nCV 212,212,213\nMvav 168,196,60\nDT 252,0,12288\nCV 214,214,215\nMvav 168,200,68\n"
"DT 254,4,12288\nCV 220,220,221\nMvav 168,204,76\nDT 172,0,13312\nCV 222,222,223\nMvav 168,208,84\nWL 6\n"
"DT 174,4,13312\nAW 56,212\nMvav 168,212,92\nDT 176,0,14336\nAW 57,214\nMvav 168,216,100\nDT 178,4,14336\n"
"AW 58,220\nMvaaz 32,220,0\nDT 180,0,15360\nAW 59,222\nMvaa 32,224,4\nDT 182,4,15360\nCV 228,228,229\n"
"Mvaa 32,228,8\nDT 184,0,16384\nCV 230,230,231\nMvaa 32,232,12\nDT 186,4,16384\nCV 236,236,237\nMvaaz 36,220,16\n"
"DT 188,0,17408\nCV 238,238,239\nMvaa 36,224,20\nDT 190,4,17408\nAW 60,228\nMvaa 36,228,24\nDT 192,1,9216\n"
"AW 61,230\nMvaa 36,232,28\nDT 194,5,9216\nAW 62,236\nMvaaz 40,220,32\nDT 196,1,10240\nAW 63,238\n"
"Mvaa 40,224,36\nLD 10,52,84,2048\nMvaa 40,228,40\ns_movk_i32 s84, 25600\nDT 198,5,10240\nDT 200,1,11264\n"
"Mvaa 40,232,44\nDT 202,5,11264\nv_max3_f32 v16, v32, v33, v34\nMvaaz 44,220,48\nDT 204,1,12288\n"
"v_max_f32 v17, v16, v35\nMvaa 44,224,52\nLD 10,52,84,3072\nMvaa 44,228,56\nWL 5\nDT 206,5,12288\n"
"DT 208,1,13312\nMvaa 44,232,60\ns_add_u32 m0, s72, 18432\nDT 210,5,13312\nv_max_f32 v16, v16, v35\n"
"Mvav 104,240,128\ns_movk_i32 s84, 0\nv_permlane32_swap_b32 v16, v17\nMvav 104,244,132\nDT 212,1,14336\n"
"DT 214,5,14336\nMvav 104,248,136\nLD 11,56,84,0\nMvav 104,252,140\ns_movk_i32 s84, 7168\nDT 216,1,15360\n"
"DT 218,5,15360\nMvav 104,172,144\nDT 220,1,16384\nv_max_f32 v18, v16, v17\nMvav 104,176,148\nDT 222,5,16384\n"
"v_max_f32 v19, v16, v17\nMvav 104,180,152\nLD 11,56,84,1024\nMvav 104,184,156\ns_movk_i32 s84, 128\n"
"DT 224,1,17408\nDT 226,5,17408\nMvav 104,188,160\ns_addk_i32 m0, 2048\nv_max3_f32 v20, v36, v37, v38\n"
"v_max_f32 v21, v20, v39\nMvaa 108,240,136\nv_permlane16_swap_b32 v18, v19\nMvaa 108,244,140\nLD 11,56,84,0\n"
"Mvaa 108,248,144\ns_movk_i32 s84, 7296\nv_max_f32 v20, v20, v39\nv_min_f32 v16, -v18, -v19\nMvaa 108,252,148\n"
"v_min_f32 v17, -v18, -v19\nv_mov_b32 v18, v16\nMvaa 108,172,152\nDK 228,8,28480\nv_mov_b32 v19, v16\n"
"Mvaa 108,176,156\nLD 11,56,84,1024\nMvaa 108,180,160\ns_addk_i32 s84, 25472\nDK 230,9,28480\nDK 232,8,28544\n"
"Mvaa 108,184,164\nWL 3\nv_permlane32_swap_b32 v20, v21\nMvaa 108,188,168\nDK 234,9,28544\nDK 240,8,28608\n"
"Mvav 112,192,128\ns_add_u32 m0, s73, 35840\nDK 242,9,28608\nv_max_f32 v22, v20, v21\nMvav 112,196,132\n"
"DK 244,8,28672\nv_max_f32 v23, v20, v21\nMvav 112,200,136\nDK 246,9,28672\nv_max3_f32 v24, v40, v41, v42\n"
"Mvav 112,204,140\nv_permlane16_swap_b32 v22, v23\nMvav 112,208,144\nv_max_f32 v25, v24, v43\n"
"v_max_f32 v24, v24, v43\nMvav 112,212,148\nv_min_f32 v20, -v22, -v23\nv_min_f32 v21, -v22, -v23\n"
"Mvav 112,216,152\nDT 248,2,9216\nv_mov_b32 v22, v20\nMvav 112,220,156\nDT 250,6,9216\nv_mov_b32 v23, v20\n"
"Mvav 112,224,160\nv_permlane32_swap_b32 v24, v25\nMvaa 116,192,136\nDT 252,2,10240\nDT 254,6,10240\n"
"Mvaa 116,196,140\nDT 172,2,11264\nv_max_f32 v26, v24, v25\nMvaa 116,200,144\nDT 174,6,11264\n"
"v_max_f32 v27, v24, v25\nMvaa 116,204,148\nDT 176,2,12288\nv_max3_f32 v28, v44, v45, v46\nMvaa 116,208,152\n"
"v_permlane16_swap_b32 v26, v27\nMvaa 116,212,156\nDT 178,6,12288\nDT 180,2,13312\nMvaa 116,216,160\nWL 7\n"
"DT 182,6,13312\nv_max_f32 v29, v28, v47\nMvaa 116,220,164\nDT 184,2,14336\nv_max_f32 v28, v28, v47\n"
"Mvaa 116,224,168\nDT 186,6,14336\nv_min_f32 v24, -v26, -v27\nMvaac 48,228,0,16\nDT 188,2,15360\n"
"v_min_f32 v25, -v26, -v27\nMvaa 48,232,4\nDT 190,6,15360\nv_mov_b32 v26, v24\nMvaa 48,240,8\nDT 192,2,16384\n"
"v_mov_b32 v27, v24\nMvaa 48,244,12\nv_permlane32_swap_b32 v28, v29\nMvaac 52,228,16,20\nDT 194,6,16384\n"
"DT 196,2,17408\nMvaa 52,232,20\nDT 198,6,17408\nv_max_f32 v30, v28, v29\nMvaa 52,240,24\n"
"v_max_f32 v31, v28, v29\nAW 109,198\nMvaa 52,244,28\nv_permlane16_swap_b32 v30, v31\nMvaac 56,228,32,24\n"
"v_min_f32 v28, -v30, -v31\nv_min_f32 v29, -v30, -v31\nMvaa 56,232,36\nv_mov_b32 v30, v28\nv_mov_b32 v31, v28\n"
"Mvaa 56,240,40\nbuffer_load_dword v14, v15, s[52:55], s93 offen\nMvaa 56,244,44\ns_add_u32 s93, s76, 0\n"
"DK 200,8,29504\nCV 200,200,201\nMvaac 60,228,48,28\nEX 48,48\nMvaa 60,232,52\nDK 202,9,29504\nDK 204,8,29568\n"
"Mvaa 60,240,56\nWL 3\nbuffer_load_dword v14, v15, s[56:59], s93 offen\nMvaa 60,244,60\n"
"s_add_u32 s93, s76, 65536\nDK 206,9,29568\nDK 208,8,29632\nMvav 120,248,128\nEX 49,49\nMvav 120,252,132\n"
"DK 210,9,29632\nDK 212,8,29696\nMvav 120,172,136\nbuffer_load_dword v14, v15, s[52:55], s93 offen\n"
"Mvav 120,176,140\ns_add_u32 s93, s76, 65536\nDK 214,9,29696\nCV 66,48,49\nMvav 120,180,144\nEX 50,50\n"
"Mvav 120,184,148\nEX 51,51\nMvav 120,188,152\nDT 216,3,9216\nCV 67,50,51\nMvav 120,192,156\n"
"buffer_load_dword v14, v15, s[56:59], s93 offen\nMvav 120,196,160\ns_add_u32 s93, s76, 0\nDT 218,7,9216\n"
"DT 220,3,10240\nMvaa 124,248,136\nEX 52,52\nMvaa 124,252,140\nDT 222,7,10240\nDT 224,3,11264\nMvaa 124,172,144\n"
"EX 53,53\nMvaa 124,176,148\nDT 226,7,11264\nDT 228,3,12288\nMvaa 124,180,152\nEX 54,54\nMvaa 124,184,156\n"
"DT 230,7,12288\nDT 232,3,13312\nMvaa 124,188,160\nWL 6\nEX 55,55\nMvaa 124,192,164\nDT 234,7,13312\n"
"DT 240,3,14336\nCV 74,52,53\nCV 75,54,55\nAW 190,175\nv_pk_add_f32 v[32:33], v[32:33], v[16:17]\n"
"v_pk_add_f32 v[34:35], v[34:35], v[16:17]\nv_pk_add_f32 v[36:37], v[36:37], v[20:21]\n"
"v_pk_add_f32 v[38:39], v[38:39], v[20:21]\nv_pk_add_f32 v[40:41], v[40:41], v[24:25]\n"
"v_pk_add_f32 v[42:43], v[42:43], v[24:25]\nv_pk_add_f32 v[44:45], v[44:45], v[28:29]\n"
"v_pk_add_f32 v[46:47], v[46:47], v[28:29]\nv_pk_mul_f32 v[104:105], v[104:105], v[240:241]\n"
"v_pk_mul_f32 v[106:107], v[106:107], v[240:241]\nv_pk_mul_f32 v[108:109], v[108:109], v[242:243]\n"
"v_pk_mul_f32 v[110:111], v[110:111], v[242:243]\nv_pk_mul_f32 v[116:117], v[116:117], v[242:243]\n"
"v_pk_mul_f32 v[118:119], v[118:119], v[242:243]\nv_pk_mul_f32 v[112:113], v[112:113], v[240:241]\n"
"v_pk_mul_f32 v[114:115], v[114:115], v[240:241]\nv_pk_mul_f32 v[120:121], v[120:121], v[240:241]\n"
"v_pk_mul_f32 v[122:123], v[122:123], v[240:241]\nCV 202,202,203\nMvaa 124,196,168\nEX 32,32\nEX 33,33\n"
"EX 34,34\nEX 35,35\nCV 64,32,33\nCV 65,34,35\nMvaac 32,200,0,16\nDT 242,7,14336\nDT 244,3,15360\nMvaa 32,204,4\n"
"EX 36,36\nMvaa 32,208,8\nDT 246,7,15360\nDT 248,3,16384\nEX 37,37\nEX 38,38\nMvaa 32,212,12\nEX 39,39\n"
"CV 72,36,37\nCV 73,38,39\nMvaac 36,200,16,20\nDT 250,7,16384\nDT 252,3,17408\nMvaa 36,204,20\nEX 40,40\n"
"Mvaa 36,208,24\nDT 254,7,17408\nAW 106,200\nEX 41,41\nEX 42,42\nMvaa 36,212,28\nEX 43,43\nCV 80,40,41\n"
"CV 81,42,43\nMvaac 40,200,32,24\nEX 44,44\nMvaa 40,204,36\nEX 45,45\nMvaa 40,208,40\nEX 46,46\nEX 47,47\n"
"Mvaa 40,212,44\nDK 172,8,30528\nCV 88,44,45\nCV 89,46,47\nMvaac 44,200,48,28\nEX 56,56\nMvaa 44,204,52\n"
"DK 174,9,30528\nDK 176,8,30592\nMvaa 44,208,56\nWL 3\nEX 57,57\nMvaa 44,212,60\nDK 178,9,30592\nDK 180,8,30656\n"
"Mvav 164,216,128\nEX 58,58\nMvav 164,220,132\nDK 182,9,30656\nDK 184,8,30720\nMvav 164,224,136\nEX 59,59\n"
"Mvav 164,228,140\nDK 186,9,30720\nCV 82,56,57\nMvav 164,232,144\nCV 83,58,59\nAW 107,202\nMvav 164,240,148\n"
"EX 60,60\nMvav 164,244,152\nEX 61,61\nMvav 164,248,156\nEX 62,62\nMvav 164,252,160\nEX 63,63\nMvaa 168,216,136\n"
"s_waitcnt vmcnt(4)\nCV 90,60,61\nCV 91,62,63\nMvaa 168,220,140\ns_barrier\nEX 32,32\nMvaa 168,224,144\n"
"DK 144,8,22528\nCV 204,204,205\nMvaa 168,228,148\nEX 33,33\nMvaa 168,232,152\nDK 146,9,22528\nDK 148,8,22592\n"
"Mvaa 168,240,156\nEX 34,34\nMvaa 168,244,160\nWL 3\nDK 150,9,22592\nDK 152,8,22656\nMvaa 168,248,164\nEX 35,35\n"
"Mvaa 168,252,168\nDK 154,9,22656\nDK 156,8,22720\nMvaac 48,172,0,16\nDK 158,9,22720\nCV 68,32,33\n"
"Mvaa 48,176,4\nCV 69,34,35\nCV 206,206,207\nMvaa 48,180,8\nEX 36,36\nMvaa 48,184,12\nEX 37,37\n"
"Mvaac 52,172,16,20\nEX 38,38\nMvaa 52,176,20\nEX 39,39\nMvaa 52,180,24\nLD 10,52,84,0\nMvaa 52,184,28\n"
"s_addk_i32 s84, 3072\nCV 76,36,37\nCV 77,38,39\nMvaac 56,172,32,24\nEX 40,40\nMvaa 56,176,36\nEX 41,41\n"
"Mvaa 56,180,40\nEX 42,42\nMvaa 56,184,44\nDT 160,0,18432\nCV 84,40,41\nMvaac 60,172,48,28\nEX 43,43\n"
"Mvaa 60,176,52\nWL 1\nDT 162,4,18432\nDT 164,0,19456\nMvaa 60,180,56\nDT 166,4,19456\nCV 85,42,43\n"
"Mvaa 60,184,60\nLD 10,52,84,1024\nMvaac 32,144,0,16\ns_addk_i32 s84, 3072\nEX 44,44\nMvaa 32,148,4\nEX 45,45\n"
"Mvaa 32,152,8\nEX 46,46\nMvaa 32,156,12\nDT 168,1,18432\nCV 92,44,45\nMvaac 36,144,16,20\nEX 47,47\n"
"Mvaa 36,148,20\nDT 170,5,18432\nDT 192,1,19456\nMvaa 36,152,24\nDT 194,5,19456\nCV 93,46,47\nMvaa 36,156,28\n"
"EX 48,48\nMvaac 40,144,32,24\nEX 49,49\nMvaa 40,148,36\nDK 196,8,23552\nCV 70,48,49\nMvaa 40,152,40\nEX 50,50\n"
"Mvaa 40,156,44\nDK 198,9,23552\nDK 200,8,23616\nMvaac 44,144,48,28\nEX 51,51\nMvaa 44,148,52\nWL 3\n"
"DK 202,9,23616\nDK 204,8,23680\nMvaa 44,152,56\nLD 10,52,84,2048\nMvaa 44,156,60\ns_addk_i32 s84, 3072\n"
"DK 206,9,23680\nDK 208,8,23744\nMvavz 128,160,64\nDK 210,9,23744\nCV 71,50,51\ns_nop 1\nMvav 128,164,68\n"
"EX 52,52\nEX 53,53\nEX 54,54\nMvavz 132,160,72\nEX 55,55\nCV 78,52,53\nCV 79,54,55\ns_nop 1\nMvav 132,164,76\n"
"EX 56,56\nEX 57,57\nEX 58,58\nMvavz 136,160,80\nEX 59,59\nCV 86,56,57\nCV 87,58,59\ns_nop 1\nMvav 136,164,84\n"
"Lw9_%=:\ns_cbranch_execz Lret2_%=\nEX 60,60\nEX 61,61\nEX 62,62\nMvavz 140,160,88\nEX 63,63\nCV 94,60,61\n"
"CV 95,62,63\ns_nop 1\nMvav 140,164,92\nLD 10,52,84,3072\nMvavz 144,168,64\ns_add_u32 m0, s72, 31744\n"
"EX 254,104\nMvav 144,192,68\ns_movk_i32 s84, 16384\nEX 255,105\nMvavz 148,168,72\nEX 172,106\nMvav 148,192,76\n"
"DT 212,2,18432\nAW 110,204\nMvavz 152,168,80\nLD 11,56,84,0\nMvav 152,192,84\nWL 1\nDT 214,6,18432\n"
"DT 216,2,19456\nMvavz 156,168,88\ns_movk_i32 s84, 23552\nEX 173,107\nMvav 156,192,92\nDT 218,6,19456\n"
"AW 111,206\nMvaac 48,196,0,16\nLD 11,56,84,1024\nMvaa 48,200,4\ns_movk_i32 s84, 14464\nEX 246,108\n"
"Mvaa 48,204,8\nEX 247,109\nMvaa 48,208,12\nDT 220,3,18432\nMvaac 52,196,16,20\nLD 11,56,84,2048\n"
"Mvaa 52,200,20\ns_movk_i32 s84, 21632\nDT 222,7,18432\nDT 224,3,19456\nMvaa 52,204,24\nEX 248,110\n"
"Mvaa 52,208,28\nDT 226,7,19456\nEX 249,111\nEX 250,112\nv_pk_add_f32 v[254:255], v[254:255], s[4:5]\n"
"v_pk_add_f32 v[172:173], v[172:173], s[4:5]\nv_pk_add_f32 v[246:247], v[246:247], s[4:5]\n"
"v_pk_add_f32 v[248:249], v[248:249], s[4:5]\nv_pk_mul_f32 v[164:165], v[164:165], v[240:241]\n"
"v_pk_mul_f32 v[166:167], v[166:167], v[240:241]\nv_pk_mul_f32 v[124:125], v[124:125], v[242:243]\n"
"v_pk_mul_f32 v[126:127], v[126:127], v[242:243]\nv_pk_mul_f32 v[168:169], v[168:169], v[242:243]\n"
"v_pk_mul_f32 v[170:171], v[170:171], v[242:243]\ns_nop 0\nMvaac 56,196,32,24\nRC 254,254\nRC 255,255\n"
"RC 172,172\nMvaa 56,200,36\nDK 228,8,24576\nRC 173,173\nRC 246,246\n"
"v_pk_mul_f32 v[104:105], v[104:105], v[254:255]\nv_pk_mul_f32 v[106:107], v[106:107], v[172:173]\n"
"CV 104,104,105\nMvaa 56,204,40\nRC 247,247\nRC 248,248\nRC 249,249\nMvaa 56,208,44\nDK 230,9,24576\n"
"DK 232,8,24640\nEX 254,116\nCV 105,106,107\nv_pk_mul_f32 v[108:109], v[108:109], v[246:247]\n"
"v_pk_mul_f32 v[110:111], v[110:111], v[248:249]\nCV 108,108,109\nMvaac 60,196,48,28\nEX 255,117\nEX 172,118\n"
"EX 173,119\nMvaa 60,200,52\nWL 4\nDK 234,9,24640\nDK 172,8,24704\nEX 251,113\nCV 109,110,111\n"
"v_pk_add_f32 v[254:255], v[254:255], s[4:5]\nv_pk_add_f32 v[172:173], v[172:173], s[4:5]\n"
"v_pk_add_f32 v[250:251], v[250:251], s[4:5]\ns_nop 0\nMvaa 60,204,56\nEX 252,114\nEX 253,115\nEX 246,120\n"
"Mvaa 60,208,60\nDK 174,9,24704\nDK 176,8,24768\nEX 247,121\nv_pk_add_f32 v[252:253], v[252:253], s[4:5]\n"
"v_pk_add_f32 v[246:247], v[246:247], s[4:5]\ns_nop 0\nMvavz 160,212,64\nEX 248,122\nEX 249,123\nRC 254,254\n"
"Mvav 160,216,68\nDK 178,9,24768\nRC 255,255\nRC 172,172\nv_pk_add_f32 v[248:249], v[248:249], s[4:5]\n"
"v_pk_mul_f32 v[116:117], v[116:117], v[254:255]\nCV 110,116,117\nRC 173,173\nRC 246,246\n"
"v_pk_mul_f32 v[118:119], v[118:119], v[172:173]\nRC 247,247\nRC 248,248\nRC 249,249\nEX 254,164\nEX 255,165\n"
"EX 172,166\nEX 173,167\nv_pk_mul_f32 v[120:121], v[120:121], v[246:247]\n"
"v_pk_mul_f32 v[122:123], v[122:123], v[248:249]\nv_pk_add_f32 v[254:255], v[254:255], s[4:5]\n"
"v_pk_add_f32 v[172:173], v[172:173], s[4:5]\nRC 254,254\nRC 255,255\nRC 172,172\nRC 173,173\nCV 120,120,121\n"
"v_pk_mul_f32 v[164:165], v[164:165], v[254:255]\nv_pk_mul_f32 v[166:167], v[166:167], v[172:173]\n"
"CV 121,122,123\nCV 122,164,165\nCV 123,166,167\nMvavz 164,212,72\nRC 250,250\nRC 251,251\nRC 252,252\n"
"Mvav 164,216,76\nRC 253,253\nEX 246,168\nCV 111,118,119\nv_pk_mul_f32 v[112:113], v[112:113], v[250:251]\n"
"v_pk_mul_f32 v[114:115], v[114:115], v[252:253]\nCV 106,112,113\nEX 250,124\nEX 251,125\nEX 252,126\n"
"EX 253,127\nEX 247,169\nEX 248,170\nEX 249,171\nv_pk_add_f32 v[250:251], v[250:251], s[4:5]\n"
"v_pk_add_f32 v[252:253], v[252:253], s[4:5]\nRC 250,250\nRC 251,251\n"
"v_pk_add_f32 v[246:247], v[246:247], s[4:5]\nv_pk_add_f32 v[248:249], v[248:249], s[4:5]\nRC 252,252\n"
"RC 253,253\nRC 246,246\nRC 247,247\nRC 248,248\nRC 249,249\nv_pk_mul_f32 v[124:125], v[124:125], v[250:251]\n"
"v_pk_mul_f32 v[126:127], v[126:127], v[252:253]\nCV 124,124,125\n"
"v_pk_mul_f32 v[168:169], v[168:169], v[246:247]\nv_pk_mul_f32 v[170:171], v[170:171], v[248:249]\n"
"CV 125,126,127\nCV 126,168,169\nCV 127,170,171\nMvavz 168,212,80\nLD 11,56,84,3072\nMvav 168,216,84\n"
"s_add_u32 m0, s73, 4096\nEX 32,32\nMvavz 172,212,88\nWL 1\nEX 33,33\nMvav 172,216,92\ns_addk_i32 s84, 27520\n"
"EX 34,34\nMvavz 176,220,64\nEX 35,35\nMvav 176,224,68\nCV 96,32,33\nCV 97,34,35\nMvavz 180,220,72\nEX 36,36\n"
"Mvav 180,224,76\nDT 180,0,20480\nCV 107,114,115\nMvavz 184,220,80\nEX 37,37\nMvav 184,224,84\nDT 182,4,20480\n"
"DT 184,0,21504\nMvavz 188,220,88\nEX 38,38\nMvav 188,224,92\nDT 186,4,21504\nMvaac 32,228,0,16\nWL 4\nEX 39,39\n"
"Mvaa 32,232,4\nbuffer_store_dwordx4 v[104:107], v13, s[64:67], s87 offen sc1\nMvaa 32,172,8\nEX 40,40\n"
"Mvaa 32,176,12\ns_mov_b32 s87, 34816\nDT 144,1,20480\nCV 104,36,37\nCV 105,38,39\nMvaac 36,228,16,20\nEX 41,41\n"
"Mvaa 36,232,20\nDT 146,5,20480\nDT 148,1,21504\nMvaa 36,172,24\nEX 42,42\nEX 43,43\nMvaa 36,176,28\n"
"DT 150,5,21504\nCV 112,40,41\nCV 113,42,43\nMvaac 40,228,32,24\n"
"buffer_store_dwordx4 v[108:111], v13, s[64:67], s88 offen sc1\nMvaa 40,232,36\nDK 152,8,25600\nMvaa 40,172,40\n"
"s_mov_b32 s88, 35840\nEX 44,44\nEX 45,45\nEX 46,46\nMvaa 40,176,44\nDK 154,9,25600\nDK 156,8,25664\nEX 47,47\n"
"buffer_store_dwordx4 v[120:123], v13, s[64:67], s87 offen sc1\ns_nop 1\nCV 120,44,45\nCV 121,46,47\n"
"Mvaac 44,228,48,28\ns_movk_i32 s87, 0\nEX 48,48\nMvaa 44,232,52\nWL 4\nDK 158,9,25664\nDK 160,8,25728\n"
"Mvaa 44,172,56\nEX 49,49\nMvaa 44,176,60\nDK 162,9,25728\nDK 164,8,25792\nMvavz 192,180,64\nEX 50,50\n"
"Mvav 192,184,68\nDK 166,9,25792\nCV 98,48,49\nMvavz 196,180,72\nEX 51,51\nMvav 196,184,76\n"
"buffer_store_dwordx4 v[124:127], v13, s[64:67], s88 offen sc1\nMvavz 200,180,80\nEX 52,52\nMvav 200,184,84\n"
"s_movk_i32 s88, 1024\nEX 53,53\nMvavz 204,180,88\nWL 1\nCV 99,50,51\nCV 106,52,53\nMvav 204,184,92\nEX 54,54\n"
"Mvavz 208,144,64\nEX 55,55\nMvav 208,148,68\nbuffer_store_dwordx4 a[64:67], v13, s[64:67], s87 offen sc1\n"
"Mvavz 212,144,72\nEX 56,56\nMvav 212,148,76\ns_movk_i32 s87, 2048\nDT 168,2,20480\nCV 107,54,55\n"
"Mvavz 216,144,80\nEX 57,57\nMvav 216,148,84\nDT 170,6,20480\nDT 192,2,21504\nMvavz 220,144,88\nEX 58,58\n"
"Mvav 220,148,92\nDT 194,6,21504\nCV 114,56,57\nMvaac 48,152,0,16\nWL 4\nEX 59,59\nMvaa 48,156,4\n"
"buffer_store_dwordx4 a[68:71], v13, s[64:67], s88 offen sc1\nMvaa 48,160,8\nEX 60,60\nMvaa 48,164,12\n"
"s_movk_i32 s88, 3072\nDT 196,3,20480\nCV 115,58,59\nMvaac 52,152,16,20\nEX 61,61\nMvaa 52,156,20\n"
"DT 198,7,20480\nDT 200,3,21504\nMvaa 52,160,24\nEX 62,62\nMvaa 52,164,28\nDT 202,7,21504\nCV 122,60,61\n"
"Mvaac 56,152,32,24\nEX 63,63\nMvaa 56,156,36\nbuffer_store_dwordx4 a[72:75], v13, s[64:67], s87 offen sc1\n"
"Mvaa 56,160,40\nEX 32,32\nMvaa 56,164,44\ns_movk_i32 s87, 4096\nEX 33,33\nCV 123,62,63\nMvaac 60,152,48,28\n"
"EX 34,34\nMvaa 60,156,52\nWL 1\nbuffer_store_dwordx4 a[76:79], v13, s[64:67], s88 offen sc1\nMvaa 60,160,56\n"
"EX 35,35\nMvaa 60,164,60\ns_movk_i32 s88, 5120\nCV 100,32,33\nCV 101,34,35\nMvavz 224,168,64\nEX 36,36\n"
"Mvav 224,192,68\nbuffer_store_dwordx4 a[80:83], v13, s[64:67], s87 offen sc1\nMvavz 228,168,72\nEX 37,37\n"
"Mvav 228,192,76\ns_movk_i32 s87, 6144\nEX 38,38\nMvavz 232,168,80\nEX 39,39\nMvav 232,192,84\n"
"buffer_store_dwordx4 a[84:87], v13, s[64:67], s88 offen sc1\nMvavz 236,168,88\nWL 0\nCV 108,36,37\n"
"CV 109,38,39\nMvav 236,192,92\ns_waitcnt vmcnt(10)\nEX 40,40\nMvavz 240,196,64\ns_barrier\nDK 204,8,35840\n"
"DK 206,9,35840\nMvav 240,200,68\ns_movk_i32 s88, 7168\nLD 10,52,84,0\nMvavz 244,196,72\ns_addk_i32 s84, 3072\n"
"DK 208,8,35904\nDK 210,9,35904\nMvav 244,200,76\nEX 41,41\nMvavz 248,196,80\nDK 212,8,35968\nDK 214,9,35968\n"
"Mvav 248,200,84\nEX 42,42\nMvavz 252,196,88\nDK 216,8,36032\nDK 218,9,36032\nMvav 252,200,92\nEX 43,43\n"
"Maavz 240,236,64\nCV 116,40,41\nCV 117,42,43\nMaav 240,236,68\nLD 10,52,84,1024\nMaavz 244,236,72\n"
"s_addk_i32 s84, 3072\nEX 44,44\nMaav 244,236,76\nDT 220,0,31744\nMaavz 248,236,80\nEX 45,45\nMaav 248,236,84\n"
"WL 1\nDT 222,4,31744\nDT 224,0,32768\nMaavz 252,236,88\nEX 46,46\nMaav 252,236,92\nDT 226,4,32768\n"
"CV 124,44,45\nMvaac 32,204,0,16\nEX 47,47\nMvaa 32,208,4\nEX 48,48\nMvaa 32,212,8\nEX 49,49\nMvaa 32,216,12\n"
"DT 228,1,31744\nCV 125,46,47\nMvaac 36,204,16,20\nEX 50,50\nMvaa 36,208,20\nDT 230,5,31744\nDT 232,1,32768\n"
"Mvaa 36,212,24\nEX 51,51\nMvaa 36,216,28\nDT 234,5,32768\nCV 102,48,49\nMvaac 40,204,32,24\nLD 10,52,84,2048\n"
"Mvaa 40,208,36\ns_addk_i32 s84, 3072\nDK 172,8,36864\nCV 103,50,51\nMvaa 40,212,40\nEX 52,52\nMvaa 40,216,44\n"
"DK 174,9,36864\nDK 176,8,36928\nMvaac 44,204,48,28\nEX 53,53\nMvaa 44,208,52\nWL 4\nDK 178,9,36928\n"
"DK 180,8,36992\nMvaa 44,212,56\nEX 54,54\nMvaa 44,216,60\nDK 182,9,36992\nDK 184,8,37056\nMvav 128,220,96\n"
"EX 55,55\nMvav 128,224,100\nDK 186,9,37056\nCV 110,52,53\nMvav 132,220,104\nEX 56,56\nCV 111,54,55\ns_nop 1\n"
"Mvav 132,224,108\nEX 57,57\nEX 58,58\nEX 59,59\nMvav 136,220,112\nCV 118,56,57\nCV 119,58,59\ns_nop 1\n"
"Mvav 136,224,116\nEX 60,60\nEX 61,61\nEX 62,62\nMvav 140,220,120\nWL 1\nEX 63,63\nCV 126,60,61\nCV 127,62,63\n"
"s_nop 1\nMvav 140,224,124\nLD 10,52,84,3072\nMvav 144,228,96\ns_add_u32 m0, s72, 0\nEX 32,32\nMvav 144,232,100\n"
"s_addk_i32 s84, -25600\nEX 33,33\nMvav 148,228,104\nEX 34,34\nMvav 148,232,108\nDT 144,2,31744\nCV 64,32,33\n"
"Mvav 152,228,112\nLD 11,56,84,0\nMvav 152,232,116\ns_addk_i32 s84, 7168\nDT 146,6,31744\nDT 148,2,32768\n"
"Mvav 156,228,120\nEX 35,35\nMvav 156,232,124\nDT 150,6,32768\nCV 65,34,35\nMvaac 48,172,0,16\nWL 4\n"
"LD 11,56,84,1024\nMvaa 48,176,4\ns_movk_i32 s84, 30848\nEX 36,36\nMvaa 48,180,8\nEX 37,37\nMvaa 48,184,12\n"
"DT 152,3,31744\nCV 72,36,37\nMvaac 52,172,16,20\nLD 11,56,84,2048\nMvaa 52,176,20\ns_addk_i32 s84, 7168\n"
"DT 154,7,31744\nDT 156,3,32768\nMvaa 52,180,24\nEX 38,38\nMvaa 52,184,28\nDT 158,7,32768\nMvaac 56,172,32,24\n"
"LD 11,56,84,3072\nMvaa 56,176,36\ns_addk_i32 s84, 27520\nDK 160,8,37888\nMvaa 56,180,40\n"
"s_add_u32 m0, s73, 13312\nEX 39,39\nMvaa 56,184,44\nDK 162,9,37888\nDK 164,8,37952\nMvaac 60,172,48,28\n"
"DK 166,9,37952\nCV 73,38,39\nMvaa 60,176,52\nWL 5\nEX 40,40\nMvaa 60,180,56\nDK 168,8,38016\nDK 170,9,38016\n"
"Mvaa 60,184,60\nEX 41,41\nMvav 160,144,96\nDK 192,8,38080\nDK 194,9,38080\nMvav 160,148,100\nEX 42,42\n"
"Mvav 164,144,104\nEX 43,43\nMvav 164,148,108\nCV 80,40,41\nCV 81,42,43\nMvav 168,144,112\nEX 44,44\n"
"Mvav 168,148,116\nEX 45,45\nMvav 172,144,120\nWL 2\nEX 46,46\nMvav 172,148,124\nEX 47,47\nMvav 176,152,96\n"
"CV 88,44,45\nCV 89,46,47\nMvav 176,156,100\nbuffer_store_dwordx4 a[88:91], v13, s[64:67], s87 offen sc1\n"
"Mvav 180,152,104\nEX 48,48\nMvav 180,156,108\ns_movk_i32 s87, 16384\nDT 196,0,33792\nMvav 184,152,112\n"
"EX 49,49\nMvav 184,156,116\nDT 198,4,33792\nDT 200,0,34816\nMvav 188,152,120\n"
"buffer_store_dwordx4 a[92:95], v13, s[64:67], s88 offen sc1\nMvav 188,156,124\nDT 202,4,34816\nCV 66,48,49\n"
"Mvaac 32,160,0,16\nWL 4\nEX 50,50\nMvaa 32,164,4\ns_movk_i32 s88, 17408\nEX 51,51\nMvaa 32,168,8\n"
"buffer_store_dwordx4 a[96:99], v13, s[64:67], s87 offen sc1\nMvaa 32,192,12\nDT 204,1,33792\nCV 67,50,51\n"
"Mvaac 36,160,16,20\ns_movk_i32 s87, 18432\nEX 52,52\nMvaa 36,164,20\nDT 206,5,33792\nDT 208,1,34816\n"
"Mvaa 36,168,24\nbuffer_store_dwordx4 a[100:103], v13, s[64:67], s88 offen sc1\nMvaa 36,192,28\nDT 210,5,34816\n"
"Mvaac 40,160,32,24\ns_movk_i32 s88, 19456\nEX 53,53\nMvaa 40,164,36\ns_nop 0\n"
"Lw10_%=:\ns_cbranch_execz Lret2_%=\nDK 212,8,38912\nCV 74,52,53\nMvaa 40,168,40\nEX 54,54\nMvaa 40,192,44\n"
"DK 214,9,38912\nDK 216,8,38976\nMvaac 44,160,48,28\nEX 55,55\nMvaa 44,164,52\nWL 4\nDK 218,9,38976\n"
"DK 220,8,39040\nMvaa 44,168,56\nDK 222,9,39040\nCV 75,54,55\nMvaa 44,192,60\n"
"buffer_store_dwordx4 a[104:107], v13, s[64:67], s87 offen sc1\nMvav 192,196,96\nDK 224,8,39104\nDK 226,9,39104\n"
"Mvav 192,200,100\ns_movk_i32 s87, 20480\nEX 56,56\nMvav 196,196,104\nEX 57,57\nMvav 196,200,108\nEX 58,58\n"
"Mvav 200,196,112\nEX 59,59\nMvav 200,200,116\nCV 82,56,57\nCV 83,58,59\nMvav 204,196,120\nWL 2\n"
"buffer_store_dwordx4 a[108:111], v13, s[64:67], s88 offen sc1\nMvav 204,200,124\nEX 60,60\nMvav 208,204,96\n"
"s_movk_i32 s88, 21504\nEX 61,61\nMvav 208,208,100\nEX 62,62\nMvav 212,204,104\nEX 63,63\nMvav 212,208,108\n"
"DT 228,2,33792\nCV 90,60,61\nMvav 216,204,112\nDT 230,6,33792\nCV 91,62,63\nMvav 216,208,116\n"
"buffer_store_dwordx4 a[112:115], v13, s[64:67], s87 offen sc1\nMvav 220,204,120\nDT 232,2,34816\n"
"DT 234,6,34816\nMvav 220,208,124\ns_movk_i32 s87, 22528\nEX 32,32\nMvaac 48,212,0,16\nWL 4\nEX 33,33\n"
"Mvaa 48,216,4\nbuffer_store_dwordx4 a[116:119], v13, s[64:67], s88 offen sc1\nMvaa 48,220,8\nEX 34,34\n"
"Mvaa 48,224,12\ns_movk_i32 s88, 23552\nDT 172,3,33792\nCV 68,32,33\nMvaac 52,212,16,20\nEX 35,35\n"
"Mvaa 52,216,20\nDT 174,7,33792\nDT 176,3,34816\nMvaa 52,220,24\n"
"buffer_store_dwordx4 a[120:123], v13, s[64:67], s87 offen sc1\nMvaa 52,224,28\nDT 178,7,34816\nCV 69,34,35\n"
"Mvaac 56,212,32,24\ns_movk_i32 s87, 0\nEX 36,36\nMvaa 56,216,36\nEX 37,37\nMvaa 56,220,40\n"
"buffer_store_dwordx4 a[124:127], v13, s[64:67], s88 offen sc1\nMvaa 56,224,44\nEX 38,38\nMvaac 60,212,48,28\n"
"s_mov_b32 s88, 32768\nEX 39,39\nMvaa 60,216,52\nWL 1\nCV 76,36,37\nCV 77,38,39\nMvaa 60,220,56\nLD 10,52,84,0\n"
"Mvaa 60,224,60\ns_waitcnt vmcnt(11)\nEX 40,40\nMvav 224,228,96\ns_barrier\nEX 41,41\nMvav 224,232,100\n"
"s_addk_i32 s84, 3072\nDK 180,8,4096\nCV 84,40,41\nMvav 228,228,104\ns_and_b32 s40, s60, 0xffe02000\nEX 42,42\n"
"Mvav 228,232,108\ns_mov_b32 s41, s61\nDK 182,9,4096\nDK 184,8,4160\nMvav 232,228,112\n"
"s_mov_b64 s[44:45], s[34:35]\nEX 43,43\nMvav 232,232,116\ns_mov_b32 s46, 36864\nDK 186,9,4160\nDK 144,8,4224\n"
"Mvav 236,228,120\nWL 5\nDK 146,9,4224\nCV 85,42,43\nMvav 236,232,124\ns_lshr_b32 s91, s81, 1\nLD 10,52,84,1024\n"
"Mvav 240,172,96\ns_addk_i32 s84, 3072\nDK 148,8,4288\nDK 150,9,4288\nMvav 240,176,100\ns_addk_i32 s91, 8\n"
"EX 44,44\nMvav 244,172,104\ns_lshl_b32 s82, s91, 15\nEX 45,45\nMvav 244,176,108\ns_and_b32 s4, s91, 1\n"
"EX 46,46\nMvav 248,172,112\ns_mul_i32 s4, s4, 18400\nEX 47,47\nMvav 248,176,116\ns_lshl_b32 s5, s91, 5\n"
"CV 92,44,45\nCV 93,46,47\nMvav 252,172,120\ns_add_u32 s4, s4, s5\nEX 48,48\nMvav 252,176,124\n"
"s_bfe_u32 s5, s81, 0x10002\nEX 49,49\nMaav 240,236,96\ns_mul_i32 s5, s5, 18432\nEX 50,50\nMaav 240,236,100\n"
"s_and_b32 s78, s81, 8\nEX 51,51\nMaav 244,236,104\ns_lshl3_add_u32 s5, s78, s5\nLD 10,52,84,2048\n"
"Maav 244,236,108\ns_addk_i32 s84, 3072\nDT 152,0,0\nCV 70,48,49\nMaav 248,236,112\ns_addk_i32 s5, 512\n"
"DT 154,4,0\nCV 71,50,51\nMaav 248,236,116\nWL 2\nEX 52,52\nMaav 252,236,120\ns_and_b32 s78, s91, 12\n"
"DT 156,0,1024\nDT 158,4,1024\nMaav 252,236,124\ns_addk_i32 s78, 16\nEX 53,53\nMvaac 32,180,0,16\n"
"s_xor_b32 s35, s81, s77\nEX 54,54\nMvaa 32,184,4\ns_lshr_b32 s34, s35, 1\nEX 55,55\nMvaa 32,144,8\n"
"s_and_b32 s35, s35, 3\nCV 78,52,53\nCV 79,54,55\nMvaa 32,148,12\ns_min_u32 s35, s35, 1\nDT 160,1,0\n"
"Mvaac 36,180,16,20\ns_and_b32 s34, s34, s79\nEX 56,56\nMvaa 36,184,20\ns_mul_i32 s34, s34, 1179648\nDT 162,5,0\n"
"DT 164,1,1024\nMvaa 36,144,24\nEX 57,57\nMvaa 36,148,28\nDT 166,5,1024\nCV 86,56,57\nMvaac 40,180,32,24\n"
"EX 58,58\nMvaa 40,184,36\nDK 168,8,5120\nMvaa 40,144,40\nEX 59,59\nMvaa 40,148,44\nDK 170,9,5120\n"
"DK 192,8,5184\nMvaac 44,180,48,28\nDK 194,9,5184\nCV 87,58,59\nMvaa 44,184,52\nWL 5\nEX 60,60\nMvaa 44,144,56\n"
"DK 196,8,5248\nDK 198,9,5248\nMvaa 44,148,60\nEX 61,61\nMvav 128,152,64\nDK 200,8,5312\nDK 202,9,5312\n"
"Mvav 128,156,68\nEX 62,62\nMvav 132,152,72\nEX 63,63\nMvav 132,156,76\nLD 10,52,84,3072\nMvav 136,152,80\n"
"s_add_u32 m0, s72, 9216\nCV 94,60,61\nCV 95,62,63\nMvav 136,156,84\ns_addk_i32 s84, -25600\nEX 32,32\n"
"Mvav 140,152,88\nWL 2\nEX 33,33\nMvav 140,156,92\ns_waitcnt vmcnt(4)\ns_mov_b64 exec, 1\n"
"global_atomic_add v13, v4, s[94:95]\ns_mov_b64 exec, -1\nMvav 144,160,64\nEX 34,34\nMvav 144,164,68\nEX 35,35\n"
"Mvav 148,160,72\nCV 96,32,33\nCV 97,34,35\nMvav 148,164,76\nDT 204,2,0\nLD 11,56,84,0\nMvav 152,160,80\n"
"s_addk_i32 s84, 7168\nEX 36,36\nMvav 152,164,84\nDT 206,6,0\nDT 208,2,1024\nMvav 156,160,88\nEX 37,37\n"
"Mvav 156,164,92\nDT 210,6,1024\nCV 104,36,37\nMvaac 48,168,0,16\nWL 4\nLD 11,56,84,1024\nMvaa 48,192,4\n"
"s_addk_i32 s84, -9088\nEX 38,38\nMvaa 48,196,8\nEX 39,39\nMvaa 48,200,12\nDT 212,3,0\nCV 105,38,39\n"
"Mvaac 52,168,16,20\nLD 11,56,84,2048\nMvaa 52,192,20\ns_addk_i32 s84, 7168\nDT 214,7,0\nDT 216,3,1024\n"
"Mvaa 52,196,24\nEX 40,40\nMvaa 52,200,28\nDT 218,7,1024\nMvaac 56,168,32,24\nLD 11,56,84,3072\nMvaa 56,192,36\n"
"s_addk_i32 s84, 27520\nDK 220,8,6144\nMvaa 56,196,40\ns_add_u32 m0, s73, 22528\nEX 41,41\nMvaa 56,200,44\n"
"DK 222,9,6144\nDK 224,8,6208\nMvaac 60,168,48,28\nEX 42,42\nMvaa 60,192,52\nWL 4\nDK 226,9,6208\nDK 228,8,6272\n"
"Mvaa 60,196,56\nEX 43,43\nMvaa 60,200,60\nDK 230,9,6272\nDK 232,8,6336\nMvav 160,204,64\nDK 234,9,6336\n"
"CV 112,40,41\nMvav 160,208,68\nEX 44,44\nMvav 164,204,72\nEX 45,45\nMvav 164,208,76\nCV 113,42,43\n"
"CV 120,44,45\nMvav 168,204,80\nEX 46,46\nMvav 168,208,84\nEX 47,47\nMvav 172,204,88\nWL 1\nEX 48,48\n"
"Mvav 172,208,92\nEX 49,49\nMvav 176,212,64\nCV 121,46,47\nCV 98,48,49\nMvav 176,216,68\nEX 50,50\n"
"Mvav 180,212,72\nEX 51,51\nMvav 180,216,76\nDT 172,0,2048\nCV 99,50,51\nMvav 184,212,80\nEX 52,52\n"
"Mvav 184,216,84\nDT 174,4,2048\nDT 176,0,3072\nMvav 188,212,88\nEX 53,53\nMvav 188,216,92\nDT 178,4,3072\n"
"CV 106,52,53\nMvaac 32,220,0,16\nWL 4\nEX 54,54\nMvaa 32,224,4\nEX 55,55\nMvaa 32,228,8\nEX 56,56\n"
"Mvaa 32,232,12\nDT 180,1,2048\nCV 107,54,55\nMvaac 36,220,16,20\nEX 57,57\nMvaa 36,224,20\nDT 182,5,2048\n"
"DT 184,1,3072\nMvaa 36,228,24\nEX 58,58\nMvaa 36,232,28\nDT 186,5,3072\nCV 114,56,57\nMvaac 40,220,32,24\n"
"EX 59,59\nMvaa 40,224,36\nDK 144,8,7168\nCV 115,58,59\nMvaa 40,228,40\nEX 60,60\nMvaa 40,232,44\nDK 146,9,7168\n"
"DK 148,8,7232\nMvaac 44,220,48,28\nEX 61,61\nMvaa 44,224,52\nWL 4\nDK 150,9,7232\nDK 152,8,7296\n"
"Mvaa 44,228,56\nEX 62,62\nMvaa 44,232,60\nDK 154,9,7296\nDK 156,8,7360\nMvav 192,172,64\nEX 63,63\n"
"Mvav 192,176,68\nDK 158,9,7360\nCV 122,60,61\nMvav 196,172,72\nEX 32,32\nMvav 196,176,76\nEX 33,33\n"
"Mvav 200,172,80\nCV 123,62,63\nCV 100,32,33\nMvav 200,176,84\nEX 34,34\nMvav 204,172,88\nWL 1\nEX 35,35\n"
"Mvav 204,176,92\nEX 36,36\nMvav 208,180,64\nEX 37,37\nMvav 208,184,68\nCV 101,34,35\nCV 108,36,37\n"
"Mvav 212,180,72\nEX 38,38\nMvav 212,184,76\nDT 160,2,2048\nMvav 216,180,80\nEX 39,39\nMvav 216,184,84\n"
"DT 162,6,2048\nDT 164,2,3072\nMvav 220,180,88\nLD 10,52,84,0\nMvav 220,184,92\ns_addk_i32 s84, 3072\n"
"DT 166,6,3072\nCV 109,38,39\nMvaac 48,144,0,16\nWL 4\nEX 40,40\nMvaa 48,148,4\nEX 41,41\nMvaa 48,152,8\n"
"EX 42,42\nMvaa 48,156,12\nDT 168,3,2048\nCV 116,40,41\nMvaac 52,144,16,20\nEX 43,43\nMvaa 52,148,20\n"
"DT 170,7,2048\nDT 192,3,3072\nMvaa 52,152,24\nDT 194,7,3072\nCV 117,42,43\nMvaa 52,156,28\nLD 10,52,84,1024\n"
"Mvaac 56,144,32,24\ns_addk_i32 s84, 3072\nEX 44,44\nMvaa 56,148,36\nEX 45,45\nMvaa 56,152,40\nEX 46,46\n"
"Mvaa 56,156,44\nEX 47,47\nMvaac 60,144,48,28\nCV 124,44,45\nCV 125,46,47\nMvaa 60,148,52\nWL 0\nEX 48,48\n"
"Mvaa 60,152,56\nEX 49,49\nMvaa 60,156,60\ns_waitcnt vmcnt(2)\nEX 50,50\nMvav 224,160,64\ns_barrier\nEX 51,51\n"
"Mvav 224,164,68\nDK 196,8,13312\nCV 102,48,49\nMvav 228,160,72\nLD 10,52,84,2048\nMvav 228,164,76\n"
"s_addk_i32 s84, 3072\nDK 198,9,13312\nDK 200,8,13376\nMvav 232,160,80\nDK 202,9,13376\nCV 103,50,51\n"
"Mvav 232,164,84\nEX 52,52\nMvav 236,160,88\nDK 204,8,13440\nDK 206,9,13440\nMvav 236,164,92\nEX 53,53\n"
"Mvav 240,168,64\nDK 208,8,13504\nDK 210,9,13504\nMvav 240,192,68\nEX 54,54\nMvav 244,168,72\nEX 55,55\n"
"Mvav 244,192,76\nCV 110,52,53\nCV 111,54,55\nMvav 248,168,80\nEX 56,56\nMvav 248,192,84\nEX 57,57\n"
"Mvav 252,168,88\nEX 58,58\nMvav 252,192,92\nEX 59,59\nMaav 240,236,64\nCV 118,56,57\nCV 119,58,59\n"
"Maav 240,236,68\nEX 60,60\nMaav 244,236,72\nEX 61,61\nMaav 244,236,76\nDT 212,0,9216\nCV 126,60,61\n"
"Maav 248,236,80\nEX 62,62\nMaav 248,236,84\nWL 1\nDT 214,4,9216\nDT 216,0,10240\nMaav 252,236,88\nEX 63,63\n"
"Maav 252,236,92\nDT 218,4,10240\nCV 127,62,63\nMvaac 32,196,0,16\nLD 10,52,84,3072\nMvaa 32,200,4\n"
"s_add_u32 m0, s72, 18432\nMvaa 32,204,8\ns_addk_i32 s84, -25600\nMvaa 32,208,12\nDT 220,1,9216\n"
"Mvaac 36,196,16,20\nLD 11,56,84,0\nMvaa 36,200,20\ns_addk_i32 s84, 7168\nDT 222,5,9216\nDT 224,1,10240\n"
"Mvaa 36,204,24\nEX 32,32\nMvaa 36,208,28\nDT 226,5,10240\nMvaac 40,196,32,24\nLD 11,56,84,1024\nMvaa 40,200,36\n"
"s_addk_i32 s84, -9088\nDK 228,8,14336\nMvaa 40,204,40\nEX 33,33\nMvaa 40,208,44\nDK 230,9,14336\n"
"DK 232,8,14400\nMvaac 44,196,48,28\nLD 11,56,84,2048\nMvaa 44,200,52\nWL 4\nDK 234,9,14400\nDK 172,8,14464\n"
"Mvaa 44,204,56\ns_addk_i32 s84, 7168\nEX 34,34\nMvaa 44,208,60\nDK 174,9,14464\nDK 176,8,14528\n"
"Mvav 128,212,96\nLD 11,56,84,3072\nMvav 128,216,100\ns_addk_i32 s84, 27520\nDK 178,9,14528\nCV 64,32,33\n"
"Mvav 132,212,104\ns_add_u32 m0, s73, 35840\nEX 35,35\nMvav 132,216,108\nEX 36,36\nMvav 136,212,112\nEX 37,37\n"
"Mvav 136,216,116\nCV 65,34,35\nCV 72,36,37\nMvav 140,212,120\nWL 1\nEX 38,38\nMvav 140,216,124\nEX 39,39\n"
"Mvav 144,220,96\nEX 40,40\nMvav 144,224,100\nEX 41,41\nMvav 148,220,104\nCV 73,38,39\n"
"Lw11_%=:\ns_cbranch_execz Lret2_%=\nCV 80,40,41\nMvav 148,224,108\nDT 180,2,9216\nMvav 152,220,112\nEX 42,42\n"
"Mvav 152,224,116\nDT 182,6,9216\nDT 184,2,10240\nMvav 156,220,120\nEX 43,43\nMvav 156,224,124\nDT 186,6,10240\n"
"CV 81,42,43\nMvaac 48,228,0,16\nWL 4\nEX 44,44\nMvaa 48,232,4\nEX 45,45\nMvaa 48,172,8\nEX 46,46\n"
"Mvaa 48,176,12\nDT 144,3,9216\nCV 88,44,45\nMvaac 52,228,16,20\nEX 47,47\nMvaa 52,232,20\nDT 146,7,9216\n"
"DT 148,3,10240\nMvaa 52,172,24\nDT 150,7,10240\nCV 89,46,47\nMvaa 52,176,28\nEX 48,48\nMvaac 56,228,32,24\n"
"EX 49,49\nMvaa 56,232,36\nDK 152,8,15360\nCV 66,48,49\nMvaa 56,172,40\nEX 50,50\nMvaa 56,176,44\n"
"DK 154,9,15360\nDK 156,8,15424\nMvaac 60,228,48,28\nEX 51,51\nMvaa 60,232,52\nWL 3\nDK 158,9,15424\n"
"DK 160,8,15488\nMvaa 60,172,56\nDK 162,9,15488\nCV 67,50,51\nMvaa 60,176,60\nEX 52,52\nMvav 160,180,96\n"
"DK 164,8,15552\nDK 166,9,15552\nMvav 160,184,100\nEX 53,53\nMvav 164,180,104\nEX 54,54\nMvav 164,184,108\n"
"EX 55,55\nMvav 168,180,112\nCV 74,52,53\nCV 75,54,55\nMvav 168,184,116\nEX 56,56\nMvav 172,180,120\nEX 57,57\n"
"Mvav 172,184,124\nEX 58,58\nMvav 176,144,96\nEX 59,59\nMvav 176,148,100\nCV 82,56,57\nCV 83,58,59\n"
"Mvav 180,144,104\nEX 60,60\nMvav 180,148,108\nDT 168,0,11264\nMvav 184,144,112\nEX 61,61\nMvav 184,148,116\n"
"WL 1\nDT 170,4,11264\nDT 192,0,12288\nMvav 188,144,120\nEX 62,62\nMvav 188,148,124\nDT 194,4,12288\n"
"CV 90,60,61\nMvaac 32,152,0,16\nEX 63,63\nMvaa 32,156,4\nbuffer_load_dword a191, v15, s[48:51], s93 offen\n"
"Mvaa 32,160,8\ns_add_u32 s93, s76, 65536\nCV 91,62,63\nMvaa 32,164,12\nDT 196,1,11264\nMvaac 36,152,16,20\n"
"DT 198,5,11264\nMvaa 36,156,20\nbuffer_load_dword a191, v15, s[48:51], s93 offen\nMvaa 36,160,24\n"
"s_add_u32 s93, s76, 131072\nDT 200,1,12288\nDT 202,5,12288\nMvaa 36,164,28\nEX 32,32\nMvaac 40,152,32,24\n"
"EX 33,33\nMvaa 40,156,36\nDK 204,8,16384\nCV 68,32,33\nMvaa 40,160,40\n"
"buffer_load_dword a191, v15, s[48:51], s93 offen\nMvaa 40,164,44\ns_add_u32 s93, s76, 196608\nDK 206,9,16384\n"
"DK 208,8,16448\nMvaac 44,152,48,28\nEX 34,34\nMvaa 44,156,52\nWL 3\nDK 210,9,16448\nDK 212,8,16512\n"
"Mvaa 44,160,56\nEX 35,35\nMvaa 44,164,60\nDK 214,9,16512\nDK 216,8,16576\nMvav 192,168,96\nDK 218,9,16576\n"
"CV 69,34,35\nMvav 192,192,100\nEX 36,36\nMvav 196,168,104\nEX 37,37\nMvav 196,192,108\nEX 38,38\n"
"Mvav 200,168,112\nEX 39,39\nMvav 200,192,116\nLD 10,52,84,0\nMvav 204,168,120\ns_addk_i32 s84, 3072\n"
"CV 76,36,37\nCV 77,38,39\nMvav 204,192,124\nEX 40,40\nMvav 208,196,96\nEX 41,41\nMvav 208,200,100\nEX 42,42\n"
"Mvav 212,196,104\nEX 43,43\nMvav 212,200,108\nDT 220,2,11264\nCV 84,40,41\nMvav 216,196,112\nDT 222,6,11264\n"
"CV 85,42,43\nMvav 216,200,116\nWL 2\nLD 10,52,84,1024\nMvav 220,196,120\ns_addk_i32 s84, 3072\nDT 224,2,12288\n"
"DT 226,6,12288\nMvav 220,200,124\nEX 44,44\nMvaac 48,204,0,16\nEX 45,45\nMvaa 48,208,4\nEX 46,46\n"
"Mvaa 48,212,8\nEX 47,47\nMvaa 48,216,12\nDT 228,3,11264\nCV 92,44,45\nMvaac 52,204,16,20\nDT 230,7,11264\n"
"CV 93,46,47\nMvaa 52,208,20\nLD 10,52,84,2048\nMvaa 52,212,24\ns_addk_i32 s84, 3072\nDT 232,3,12288\n"
"DT 234,7,12288\nMvaa 52,216,28\nEX 48,48\nMvaac 56,204,32,24\nEX 49,49\nMvaa 56,208,36\nEX 50,50\n"
"Mvaa 56,212,40\nEX 51,51\nMvaa 56,216,44\nCV 70,48,49\nCV 71,50,51\nMvaac 60,204,48,28\nEX 52,52\n"
"Mvaa 60,208,52\nWL 0\nEX 53,53\nMvaa 60,212,56\nEX 54,54\nMvaa 60,216,60\ns_waitcnt vmcnt(6)\nEX 55,55\n"
"Mvav 224,220,96\ns_barrier\nCV 78,52,53\nCV 79,54,55\nMvav 224,224,100\nDK 172,8,22528\nMvav 228,220,104\n"
"EX 56,56\nMvav 228,224,108\nDK 174,9,22528\nDK 176,8,22592\nMvav 232,220,112\nEX 57,57\nMvav 232,224,116\n"
"DK 178,9,22592\nDK 180,8,22656\nMvav 236,220,120\nEX 58,58\nMvav 236,224,124\nDK 182,9,22656\nDK 184,8,22720\n"
"Mvav 240,228,96\nEX 59,59\nMvav 240,232,100\nDK 186,9,22720\nCV 86,56,57\nMvav 244,228,104\nEX 60,60\n"
"Mvav 244,232,108\nEX 61,61\nMvav 248,228,112\nCV 87,58,59\nCV 94,60,61\nMvav 248,232,116\nEX 62,62\n"
"Mvav 252,228,120\nEX 63,63\nMvav 252,232,124\nLD 10,52,84,3072\nMaav 240,236,96\ns_add_u32 m0, s72, 31744\n"
"CV 95,62,63\nMaav 240,236,100\ns_addk_i32 s84, -25600\nMaav 244,236,104\nMaav 244,236,108\nDT 144,0,18432\n"
"Maav 248,236,112\nLD 11,56,84,0\nMaav 248,236,116\nWL 1\nDT 146,4,18432\nDT 148,0,19456\nMaav 252,236,120\n"
"s_addk_i32 s84, 7168\nDT 150,4,19456\nMaav 252,236,124\nMvaac 32,172,0,16\nLD 11,56,84,1024\nMvaa 32,176,4\n"
"s_addk_i32 s84, -9088\nMvaa 32,180,8\nMvaa 32,184,12\nDT 152,1,18432\nMvaac 36,172,16,20\nLD 11,56,84,2048\n"
"Mvaa 36,176,20\ns_addk_i32 s84, 7168\nDT 154,5,18432\nDT 156,1,19456\nMvaa 36,180,24\nEX 32,32\nMvaa 36,184,28\n"
"DT 158,5,19456\nMvaac 40,172,32,24\nLD 11,56,84,3072\nMvaa 40,176,36\ns_addk_i32 s84, 27520\nDK 160,8,23552\n"
"Mvaa 40,180,40\ns_add_u32 m0, s73, 4096\nEX 33,33\nMvaa 40,184,44\nDK 162,9,23552\nDK 164,8,23616\n"
"Mvaac 44,172,48,28\nEX 34,34\nMvaa 44,176,52\nWL 4\nDK 166,9,23616\nDK 168,8,23680\nMvaa 44,180,56\nEX 35,35\n"
"Mvaa 44,184,60\nDK 170,9,23680\nDK 192,8,23744\nMvav 128,144,64\nDK 194,9,23744\nCV 96,32,33\nMvav 128,148,68\n"
"EX 36,36\nMvav 132,144,72\nEX 37,37\nMvav 132,148,76\nCV 97,34,35\nCV 104,36,37\nMvav 136,144,80\nEX 38,38\n"
"Mvav 136,148,84\nEX 39,39\nMvav 140,144,88\nWL 1\nEX 40,40\nMvav 140,148,92\nEX 41,41\nMvav 144,152,64\n"
"CV 105,38,39\nCV 112,40,41\nMvav 144,156,68\nEX 42,42\nMvav 148,152,72\nEX 43,43\nMvav 148,156,76\n"
"DT 196,2,18432\nCV 113,42,43\nMvav 152,152,80\nEX 44,44\nMvav 152,156,84\nDT 198,6,18432\nDT 200,2,19456\n"
"Mvav 156,152,88\nEX 45,45\nMvav 156,156,92\nDT 202,6,19456\nCV 120,44,45\nMvaac 48,160,0,16\nWL 4\nEX 46,46\n"
"Mvaa 48,164,4\nEX 47,47\nMvaa 48,168,8\nbuffer_load_dword a191, v15, s[48:51], s93 offen\nMvaa 48,192,12\n"
"s_add_u32 s93, s76, 262144\nDT 204,3,18432\nCV 121,46,47\nMvaac 52,160,16,20\nDT 206,7,18432\nMvaa 52,164,20\n"
"DT 208,3,19456\nMvaa 52,168,24\nEX 48,48\nMvaa 52,192,28\nDT 210,7,19456\nMvaac 56,160,32,24\nEX 49,49\n"
"Mvaa 56,164,36\nDK 212,8,24576\nCV 98,48,49\nMvaa 56,168,40\nEX 50,50\nMvaa 56,192,44\nDK 214,9,24576\n"
"DK 216,8,24640\nMvaac 60,160,48,28\nEX 51,51\nMvaa 60,164,52\nWL 4\nDK 218,9,24640\nDK 220,8,24704\n"
"Mvaa 60,168,56\nDK 222,9,24704\nCV 99,50,51\nMvaa 60,192,60\nEX 52,52\nMvav 160,196,64\nDK 224,8,24768\n"
"DK 226,9,24768\nMvav 160,200,68\nEX 53,53\nMvav 164,196,72\nEX 54,54\nMvav 164,200,76\nEX 55,55\n"
"Mvav 168,196,80\nCV 106,52,53\nCV 107,54,55\nMvav 168,200,84\nEX 56,56\nMvav 172,196,88\nWL 2\nEX 57,57\n"
"Mvav 172,200,92\nEX 58,58\nMvav 176,204,64\nEX 59,59\nMvav 176,208,68\nCV 114,56,57\nCV 115,58,59\n"
"Mvav 180,204,72\nEX 60,60\nMvav 180,208,76\nDT 228,0,20480\nMvav 184,204,80\nEX 61,61\nMvav 184,208,84\n"
"DT 230,4,20480\nDT 232,0,21504\nMvav 188,204,88\nEX 62,62\nMvav 188,208,92\nDT 234,4,21504\nCV 122,60,61\n"
"Mvaac 32,212,0,16\nWL 4\nEX 63,63\nMvaa 32,216,4\nbuffer_load_dword a191, v15, s[48:51], s93 offen\n"
"Mvaa 32,220,8\ns_add_u32 s93, s76, 327680\nCV 123,62,63\nMvaa 32,224,12\nDT 172,1,20480\nMvaac 36,212,16,20\n"
"DT 174,5,20480\nMvaa 36,216,20\nbuffer_load_dword a191, v15, s[48:51], s93 offen\nMvaa 36,220,24\n"
"s_add_u32 s93, s76, 393216\nDT 176,1,21504\nDT 178,5,21504\nMvaa 36,224,28\nEX 32,32\nMvaac 40,212,32,24\n"
"EX 33,33\nMvaa 40,216,36\nDK 180,8,25600\nCV 100,32,33\nMvaa 40,220,40\nEX 34,34\nMvaa 40,224,44\n"
"DK 182,9,25600\nDK 184,8,25664\nMvaac 44,212,48,28\nEX 35,35\nMvaa 44,216,52\nWL 3\nDK 186,9,25664\n"
"DK 144,8,25728\nMvaa 44,220,56\nDK 146,9,25728\nCV 101,34,35\nMvaa 44,224,60\nEX 36,36\nMvav 192,228,64\n"
"DK 148,8,25792\nDK 150,9,25792\nMvav 192,232,68\nEX 37,37\nMvav 196,228,72\nEX 38,38\nMvav 196,232,76\n"
"EX 39,39\nMvav 200,228,80\nLD 10,52,84,0\nMvav 200,232,84\ns_addk_i32 s84, 3072\nCV 108,36,37\nCV 109,38,39\n"
"Mvav 204,228,88\nEX 40,40\nMvav 204,232,92\nEX 41,41\nMvav 208,172,64\nEX 42,42\nMvav 208,176,68\nEX 43,43\n"
"Mvav 212,172,72\nCV 116,40,41\nCV 117,42,43\nMvav 212,176,76\nDT 152,2,20480\nMvav 216,172,80\n"
"LD 10,52,84,1024\nMvav 216,176,84\nWL 1\nDT 154,6,20480\nDT 156,2,21504\nMvav 220,172,88\ns_addk_i32 s84, 3072\n"
"EX 44,44\nMvav 220,176,92\nDT 158,6,21504\nMvaac 48,180,0,16\nEX 45,45\nMvaa 48,184,4\nEX 46,46\nMvaa 48,144,8\n"
"EX 47,47\nMvaa 48,148,12\nDT 160,3,20480\nCV 124,44,45\nMvaac 52,180,16,20\nDT 162,7,20480\nCV 125,46,47\n"
"Mvaa 52,184,20\nLD 10,52,84,2048\nMvaa 52,144,24\ns_addk_i32 s84, 3072\nDT 164,3,21504\nDT 166,7,21504\n"
"Mvaa 52,148,28\nEX 48,48\nMvaac 56,180,32,24\nEX 49,49\nMvaa 56,184,36\nEX 50,50\nMvaa 56,144,40\nEX 51,51\n"
"Mvaa 56,148,44\nCV 102,48,49\nCV 103,50,51\nMvaac 60,180,48,28\nEX 52,52\nMvaa 60,184,52\nWL 0\nEX 53,53\n"
"Mvaa 60,144,56\nEX 54,54\nMvaa 60,148,60\ns_waitcnt vmcnt(6)\nEX 55,55\nMvav 224,152,64\ns_barrier\n"
"CV 110,52,53\nCV 111,54,55\nMvav 224,156,68\nDK 168,8,35840\nMvav 228,152,72\nEX 56,56\nMvav 228,156,76\n"
"DK 170,9,35840\nDK 192,8,35904\nMvav 232,152,80\nEX 57,57\nMvav 232,156,84\nDK 194,9,35904\nDK 196,8,35968\n"
"Mvav 236,152,88\nEX 58,58\nMvav 236,156,92\nDK 198,9,35968\nDK 200,8,36032\nMvav 240,160,64\nEX 59,59\n"
"Mvav 240,164,68\nDK 202,9,36032\nCV 118,56,57\nMvav 244,160,72\nEX 60,60\nMvav 244,164,76\nEX 61,61\n"
"Mvav 248,160,80\nCV 119,58,59\nCV 126,60,61\nMvav 248,164,84\nEX 62,62\nMvav 252,160,88\nEX 63,63\n"
"Mvav 252,164,92\nLD 10,52,84,3072\nMaav 240,236,64\ns_add_u32 m0, s72, 0\nCV 127,62,63\ns_nop 0\n"
"Lw12_%=:\ns_cbranch_execz Lret2_%=\nv_accvgpr_read_b32 v10, a190\nMaav 240,236,68\ns_addk_i32 s84, -25600\n"
"Maav 244,236,72\nMaav 244,236,76\nDT 204,0,31744\nMaav 248,236,80\nLD 11,56,84,0\nMaav 248,236,84\nWL 1\n"
"DT 206,4,31744\nDT 208,0,32768\nMaav 252,236,88\ns_addk_i32 s84, 7168\nDT 210,4,32768\nMaav 252,236,92\n"
"Mvaac 32,168,0,16\nLD 11,56,84,1024\nMvaa 32,192,4\ns_addk_i32 s84, -9088\nMvaa 32,196,8\nMvaa 32,200,12\n"
"DT 212,1,31744\nMvaac 36,168,16,20\nLD 11,56,84,2048\nMvaa 36,192,20\ns_addk_i32 s84, 7168\nDT 214,5,31744\n"
"DT 216,1,32768\nMvaa 36,196,24\nEX 32,32\nMvaa 36,200,28\nDT 218,5,32768\nMvaac 40,168,32,24\nLD 11,56,84,3072\n"
"Mvaa 40,192,36\ns_addk_i32 s84, 11136\nDK 220,8,36864\nMvaa 40,196,40\ns_addk_i32 m0, 9216\nEX 33,33\n"
"Mvaa 40,200,44\nDK 222,9,36864\nDK 224,8,36928\nMvaac 44,168,48,28\nEX 34,34\nMvaa 44,192,52\nWL 4\n"
"DK 226,9,36928\nDK 228,8,36992\nMvaa 44,196,56\nEX 35,35\nMvaa 44,200,60\ns_barrier\nDK 230,9,36992\n"
"DK 232,8,37056\nMvav 128,204,96\nDK 234,9,37056\nCV 64,32,33\nMvav 128,208,100\nEX 36,36\nMvav 132,204,104\n"
"EX 37,37\nMvav 132,208,108\nCV 65,34,35\nCV 72,36,37\nMvav 136,204,112\nEX 38,38\nMvav 136,208,116\nEX 39,39\n"
"Mvav 140,204,120\nWL 1\nEX 40,40\nMvav 140,208,124\nEX 41,41\nMvav 144,212,96\nCV 73,38,39\nCV 80,40,41\n"
"Mvav 144,216,100\nEX 42,42\nMvav 148,212,104\nEX 43,43\nMvav 148,216,108\nDT 172,2,31744\nCV 81,42,43\n"
"Mvav 152,212,112\nEX 44,44\nMvav 152,216,116\nDT 174,6,31744\nDT 176,2,32768\nMvav 156,212,120\nEX 45,45\n"
"Mvav 156,216,124\nDT 178,6,32768\nCV 88,44,45\nMvaac 48,220,0,16\nWL 4\nEX 46,46\nMvaa 48,224,4\nEX 47,47\n"
"Mvaa 48,228,8\nLD 11,56,84,0\nMvaa 48,232,12\ns_addk_i32 s84, 7168\nDT 180,3,31744\nCV 89,46,47\n"
"Mvaac 52,220,16,20\nDT 182,7,31744\nMvaa 52,224,20\nDT 184,3,32768\nMvaa 52,228,24\nLD 11,56,84,1024\n"
"Mvaa 52,232,28\ns_addk_i32 s84, -9088\nDT 186,7,32768\nMvaac 56,220,32,24\nEX 48,48\nMvaa 56,224,36\n"
"DK 144,8,37888\nMvaa 56,228,40\nLD 11,56,84,2048\nMvaa 56,232,44\ns_addk_i32 s84, 7168\nDK 146,9,37888\n"
"DK 148,8,37952\nMvaac 60,220,48,28\nEX 49,49\nMvaa 60,224,52\nWL 4\nDK 150,9,37952\nDK 152,8,38016\n"
"Mvaa 60,228,56\nLD 11,56,84,3072\nMvaa 60,232,60\ns_movk_i32 s84, 0\nDK 154,9,38016\nDK 156,8,38080\n"
"Mvav 160,172,96\ns_addk_i32 m0, 9216\nEX 50,50\nMvav 160,176,100\nDK 158,9,38080\nCV 66,48,49\n"
"Mvav 164,172,104\nLD 12,48,84,0\nMvav 164,176,108\ns_add_u32 s84, s84, s85\nEX 51,51\nMvav 168,172,112\n"
"EX 52,52\nMvav 168,176,116\nEX 53,53\nMvav 172,172,120\nWL 1\nCV 67,50,51\nCV 74,52,53\nMvav 172,176,124\n"
"LD 12,48,84,1024\nMvav 176,180,96\ns_add_u32 s84, s84, s85\nEX 54,54\nMvav 176,184,100\nEX 55,55\n"
"Mvav 180,180,104\nEX 56,56\nMvav 180,184,108\nDT 160,0,33792\nCV 75,54,55\nMvav 184,180,112\nLD 12,48,84,2048\n"
"Mvav 184,184,116\ns_add_u32 s84, s84, s85\nDT 162,4,33792\nDT 164,0,34816\nMvav 188,180,120\nEX 57,57\n"
"Mvav 188,184,124\nDT 166,4,34816\nCV 82,56,57\nMvaac 32,144,0,16\nWL 4\nEX 58,58\nMvaa 32,148,4\nEX 59,59\n"
"Mvaa 32,152,8\nLD 12,48,84,3072\nMvaa 32,156,12\ns_mov_b32 s84, 147456\nDT 168,1,33792\nCV 83,58,59\n"
"Mvaac 36,144,16,20\ns_addk_i32 m0, 4096\nEX 60,60\nMvaa 36,148,20\nDT 170,5,33792\nDT 192,1,34816\n"
"Mvaa 36,152,24\nEX 61,61\nMvaa 36,156,28\nDT 194,5,34816\nCV 90,60,61\nMvaac 40,144,32,24\nEX 62,62\n"
"Mvaa 40,148,36\nDK 196,8,38912\nMvaa 40,152,40\nEX 63,63\nMvaa 40,156,44\nDK 198,9,38912\nDK 200,8,38976\n"
"Mvaac 44,144,48,28\nDK 202,9,38976\nCV 91,62,63\nMvaa 44,148,52\nWL 5\nLD 12,48,84,0\nMvaa 44,152,56\n"
"s_add_u32 s84, s84, s85\nDK 204,8,39040\nDK 206,9,39040\nMvaa 44,156,60\nEX 32,32\nMvav 192,160,96\n"
"DK 208,8,39104\nDK 210,9,39104\nMvav 192,164,100\nbuffer_load_dword a191, v15, s[48:51], s93 offen\n"
"Mvav 196,160,104\ns_add_u32 s93, s76, 458752\nEX 33,33\nMvav 196,164,108\nEX 34,34\nMvav 200,160,112\nEX 35,35\n"
"Mvav 200,164,116\nLD 12,48,84,1024\nMvav 204,160,120\nWL 2\nCV 68,32,33\nCV 69,34,35\nMvav 204,164,124\n"
"s_add_u32 s84, s84, s85\nEX 36,36\nMvav 208,168,96\nEX 37,37\nMvav 208,192,100\n"
"buffer_load_dword a191, v15, s[48:51], s93 offen\nMvav 212,168,104\ns_add_u32 s93, s76, 524288\nEX 38,38\n"
"Mvav 212,192,108\nDT 212,2,33792\nCV 76,36,37\nMvav 216,168,112\nEX 39,39\nMvav 216,192,116\nDT 214,6,33792\n"
"DT 216,2,34816\nMvav 220,168,120\nLD 12,48,84,2048\nMvav 220,192,124\ns_add_u32 s84, s84, s85\nDT 218,6,34816\n"
"CV 77,38,39\nMvaac 48,196,0,16\nWL 4\nEX 40,40\nMvaa 48,200,4\nEX 41,41\nMvaa 48,204,8\n"
"buffer_load_dword a191, v15, s[48:51], s93 offen\nMvaa 48,208,12\nDT 220,3,33792\nCV 84,40,41\n"
"Mvaac 52,196,16,20\nEX 42,42\nMvaa 52,200,20\nDT 222,7,33792\nDT 224,3,34816\nMvaa 52,204,24\nLD 12,48,84,3072\n"
"Mvaa 52,208,28\ns_movk_i32 s84, 128\nDT 226,7,34816\nMvaac 56,196,32,24\ns_addk_i32 m0, 9216\nEX 43,43\n"
"Mvaa 56,200,36\nEX 44,44\nMvaa 56,204,40\nEX 45,45\nMvaa 56,208,44\nCV 85,42,43\nCV 92,44,45\n"
"Mvaac 60,196,48,28\nEX 46,46\nMvaa 60,200,52\nWL 1\nEX 47,47\nMvaa 60,204,56\nEX 48,48\nMvaa 60,208,60\n"
"s_waitcnt vmcnt(15)\nEX 49,49\nMvav 224,212,96\ns_barrier\nCV 93,46,47\nCV 70,48,49\nMvav 224,216,100\n"
"DK 228,8,4096\nMvav 228,212,104\nEX 50,50\nMvav 228,216,108\nDK 230,9,4096\nDK 232,8,4160\nMvav 232,212,112\n"
"EX 51,51\nMvav 232,216,116\nDK 234,9,4160\nDK 172,8,4224\nMvav 236,212,120\nWL 5\nDK 174,9,4224\nCV 71,50,51\n"
"Mvav 236,216,124\nEX 52,52\nMvav 240,220,96\nDK 176,8,4288\nDK 178,9,4288\nMvav 240,224,100\nEX 53,53\n"
"Mvav 244,220,104\nEX 54,54\nMvav 244,224,108\nEX 55,55\nMvav 248,220,112\nCV 78,52,53\nCV 79,54,55\n"
"Mvav 248,224,116\nEX 56,56\nMvav 252,220,120\nEX 57,57\nMvav 252,224,124\nEX 58,58\nMaav 240,236,96\nEX 59,59\n"
"Maav 240,236,100\nCV 86,56,57\nCV 87,58,59\nMaav 244,236,104\nEX 60,60\nMaav 244,236,108\nDT 180,0,0\n"
"Maav 248,236,112\nEX 61,61\nMaav 248,236,116\nWL 1\nDT 182,4,0\nDT 184,0,1024\nMaav 252,236,120\nEX 62,62\n"
"Maav 252,236,124\nDT 186,4,1024\nCV 94,60,61\nMvaac 32,228,0,16\nEX 63,63\nMvaa 32,232,4\nCV 95,62,63\n"
"Mvaa 32,172,8\nMvaa 32,176,12\nDT 144,1,0\nMvaac 36,228,16,20\nDT 146,5,0\nMvaa 36,232,20\nDT 148,1,1024\n"
"Mvaa 36,172,24\nDT 150,5,1024\nMvaa 36,176,28\nEX 32,32\nMvaac 40,228,32,24\nEX 33,33\nMvaa 40,232,36\n"
"DK 152,8,5120\nCV 96,32,33\nMvaa 40,172,40\nEX 34,34\nMvaa 40,176,44\nDK 154,9,5120\nDK 156,8,5184\n"
"Mvaac 44,228,48,28\nEX 35,35\nMvaa 44,232,52\nWL 3\nDK 158,9,5184\nDK 160,8,5248\nMvaa 44,172,56\n"
"DK 162,9,5248\nCV 97,34,35\nMvaa 44,176,60\ns_barrier\nEX 36,36\nMvav 128,180,64\nDK 164,8,5312\nDK 166,9,5312\n"
"Mvav 128,184,68\nEX 37,37\nMvav 132,180,72\nEX 38,38\nMvav 132,184,76\nEX 39,39\nMvav 136,180,80\nCV 104,36,37\n"
"CV 105,38,39\nMvav 136,184,84\nEX 40,40\nMvav 140,180,88\nEX 41,41\nMvav 140,184,92\nEX 42,42\nMvav 144,144,64\n"
"EX 43,43\nMvav 144,148,68\nCV 112,40,41\nCV 113,42,43\nMvav 148,144,72\nEX 44,44\nMvav 148,148,76\nDT 168,2,0\n"
"Mvav 152,144,80\nEX 45,45\nMvav 152,148,84\nWL 1\nDT 170,6,0\nDT 192,2,1024\nMvav 156,144,88\nEX 46,46\n"
"Mvav 156,148,92\nDT 194,6,1024\nCV 120,44,45\nMvaac 48,152,0,16\nEX 47,47\nMvaa 48,156,4\nLD 12,48,84,0\n"
"Mvaa 48,160,8\ns_add_u32 s84, s84, s85\nCV 121,46,47\nMvaa 48,164,12\nDT 196,3,0\nMvaac 52,152,16,20\n"
"DT 198,7,0\nMvaa 52,156,20\nLD 12,48,84,1024\nMvaa 52,160,24\ns_add_u32 s84, s84, s85\nDT 200,3,1024\n"
"DT 202,7,1024\nMvaa 52,164,28\nEX 48,48\nMvaac 56,152,32,24\nEX 49,49\nMvaa 56,156,36\nDK 204,8,6144\n"
"CV 98,48,49\nMvaa 56,160,40\nEX 50,50\nMvaa 56,164,44\nDK 206,9,6144\nDK 208,8,6208\nMvaac 60,152,48,28\n"
"EX 51,51\nMvaa 60,156,52\nWL 3\nDK 210,9,6208\nDK 212,8,6272\nMvaa 60,160,56\nDK 214,9,6272\nCV 99,50,51\n"
"Mvaa 60,164,60\nEX 52,52\nMvav 160,168,64\nDK 216,8,6336\nDK 218,9,6336\nMvav 160,192,68\nEX 53,53\n"
"Mvav 164,168,72\nEX 54,54\nMvav 164,192,76\nEX 55,55\nMvav 168,168,80\nCV 106,52,53\nCV 107,54,55\n"
"Mvav 168,192,84\nEX 56,56\nMvav 172,168,88\nEX 57,57\nMvav 172,192,92\nEX 58,58\nMvav 176,196,64\nEX 59,59\n"
"Mvav 176,200,68\nCV 114,56,57\nCV 115,58,59\nMvav 180,196,72\nEX 60,60\nMvav 180,200,76\nDT 220,0,2048\n"
"Mvav 184,196,80\nEX 61,61\nMvav 184,200,84\nWL 1\nDT 222,4,2048\nDT 224,0,3072\nMvav 188,196,88\nEX 62,62\n"
"Mvav 188,200,92\nDT 226,4,3072\nCV 122,60,61\nMvaac 32,204,0,16\nEX 63,63\nMvaa 32,208,4\nLD 12,48,84,2048\n"
"Mvaa 32,212,8\ns_add_u32 s84, s84, s85\nCV 123,62,63\nMvaa 32,216,12\nDT 228,1,2048\nMvaac 36,204,16,20\n"
"DT 230,5,2048\nMvaa 36,208,20\nLD 12,48,84,3072\nMvaa 36,212,24\ns_mov_b32 s84, 147584\nDT 232,1,3072\n"
"DT 234,5,3072\nMvaa 36,216,28\ns_addk_i32 m0, 4096\nEX 32,32\nMvaac 40,204,32,24\nEX 33,33\nMvaa 40,208,36\n"
"DK 172,8,7168\nCV 100,32,33\nMvaa 40,212,40\nLD 12,48,84,0\nMvaa 40,216,44\ns_add_u32 s84, s84, s85\n"
"DK 174,9,7168\nDK 176,8,7232\nMvaac 44,204,48,28\nEX 34,34\nMvaa 44,208,52\nWL 3\nDK 178,9,7232\nDK 180,8,7296\n"
"Mvaa 44,212,56\nLD 12,48,84,1024\nMvaa 44,216,60\ns_add_u32 s84, s84, s85\nDK 182,9,7296\nDK 184,8,7360\n"
"Mvav 192,220,64\nEX 35,35\nMvav 192,224,68\nDK 186,9,7360\nCV 101,34,35\nMvav 196,220,72\nLD 12,48,84,2048\n"
"Mvav 196,224,76\ns_add_u32 s84, s84, s85\nEX 36,36\nMvav 200,220,80\nEX 37,37\nMvav 200,224,84\nEX 38,38\n"
"Mvav 204,220,88\nLD 12,48,84,3072\nMvav 204,224,92\ns_add_u32 m0, s72, 0\nEX 39,39\nMvav 208,228,64\n"
"s_movk_i32 s84, 256\nCV 108,36,37\nCV 109,38,39\nMvav 208,232,68\nEX 40,40\nMvav 212,228,72\nEX 41,41\n"
"Mvav 212,232,76\nDT 144,2,2048\nCV 116,40,41\n"
"Lw13_%=:\ns_cbranch_execz Lret3_%=\nMvav 216,228,80\nEX 42,42\nMvav 216,232,84\nWL 1\nDT 146,6,2048\n"
"DT 148,2,3072\nMvav 220,228,88\nEX 43,43\nMvav 220,232,92\nDT 150,6,3072\nCV 117,42,43\nMvaac 48,172,0,16\n"
"EX 44,44\nMvaa 48,176,4\nEX 45,45\nMvaa 48,180,8\nEX 46,46\nMvaa 48,184,12\nDT 152,3,2048\nCV 124,44,45\n"
"Mvaac 52,172,16,20\nEX 47,47\nMvaa 52,176,20\nDT 154,7,2048\nDT 156,3,3072\nMvaa 52,180,24\nEX 48,48\n"
"Mvaa 52,184,28\nDT 158,7,3072\nCV 125,46,47\nMvaac 56,172,32,24\nEX 49,49\nMvaa 56,176,36\nEX 50,50\n"
"Mvaa 56,180,40\nEX 51,51\nMvaa 56,184,44\nCV 102,48,49\nCV 103,50,51\nMvaac 60,172,48,28\nEX 52,52\n"
"Mvaa 60,176,52\nWL 1\nEX 53,53\nMvaa 60,180,56\nEX 54,54\nMvaa 60,184,60\nEX 55,55\nMvav 224,144,64\n"
"CV 110,52,53\nCV 111,54,55\nMvav 224,148,68\nEX 56,56\nMvav 228,144,72\nEX 57,57\nMvav 228,148,76\nEX 58,58\n"
"Mvav 232,144,80\nEX 59,59\nMvav 232,148,84\nCV 118,56,57\nCV 119,58,59\nMvav 236,144,88\nWL 0\nEX 60,60\n"
"Mvav 236,148,92\nEX 61,61\nMvav 240,152,64\nEX 62,62\nMvav 240,156,68\ns_waitcnt vmcnt(19)\nEX 63,63\n"
"Mvav 244,152,72\ns_barrier\nCV 126,60,61\nCV 127,62,63\nMvav 244,156,76\nDT 160,0,9216\nv_mov_b32 v9, 1\n"
"Mvav 248,152,80\nDT 162,4,9216\nMvav 248,156,84\nDT 164,0,10240\nMvav 252,152,88\nDT 166,4,10240\n"
"Mvav 252,156,92\nMaav 240,236,64\nMaav 240,236,68\nMaav 244,236,72\nMaav 244,236,76\nDT 168,1,9216\n"
"Maav 248,236,80\nDT 170,5,9216\nMaav 248,236,84\nDT 192,1,10240\nMaav 252,236,88\nDT 194,5,10240\n"
"Maav 252,236,92\nMaav 240,236,96\nMaav 240,236,100\nMaav 244,236,104\nMaav 244,236,108\nDT 196,2,9216\n"
"Maav 248,236,112\nDT 198,6,9216\nMaav 248,236,116\nWL 2\nDT 200,2,10240\nv_accvgpr_read_b32 v16, a240\n"
"Maav 252,236,120\nDT 202,6,10240\nv_accvgpr_read_b32 v18, a244\nMaav 252,236,124\nv_add_f32 v24, v16, v18\n"
"v_accvgpr_read_b32 v20, a248\nMvav 128,160,96\nMvav 128,164,100\nMvav 132,160,104\nMvav 132,164,108\n"
"DT 204,3,9216\nMvav 136,160,112\nDT 206,7,9216\nv_accvgpr_read_b32 v22, a252\nMvav 136,164,116\nDT 208,3,10240\n"
"v_add_f32 v25, v20, v22\nMvav 140,160,120\nDT 210,7,10240\nv_add_f32 v24, v24, v25\nMvav 140,164,124\n"
"v_cmp_nge_f32 vcc, 0x5d800000, v24\nMvav 144,168,96\ns_cbranch_vccnz Lfb_%=\n"
"Lfbr0_%=:\nRC 16,16\nMvav 144,192,100\nv_mov_b32 v17, v16\nML 128,128,16\nMvav 148,168,104\nML 129,129,17\n"
"ML 130,130,16\nMvav 148,192,108\nDT 212,0,11264\nML 131,131,17\nMvav 152,168,112\nDT 214,4,11264\n"
"CV 32,128,129\nMvav 152,192,116\nWL 2\nDT 216,0,12288\nCV 33,130,131\nMvav 156,168,120\nRC 20,20\n"
"Mvav 156,192,124\nDT 218,4,12288\nv_mov_b32 v21, v20\nMvav 160,196,96\nML 136,136,20\nML 137,137,21\n"
"Mvav 160,200,100\nML 138,138,20\nML 139,139,21\nMvav 164,196,104\nCV 64,136,137\nCV 65,138,139\n"
"Mvav 164,200,108\nDT 220,1,11264\nMvav 168,196,112\nRC 18,18\nRC 22,22\nv_mov_b32 v19, v18\n"
"v_pk_mul_f32 v[144:145], v[144:145], v[16:17]\nv_pk_mul_f32 v[146:147], v[146:147], v[16:17]\n"
"v_pk_mul_f32 v[160:161], v[160:161], v[16:17]\nv_pk_mul_f32 v[162:163], v[162:163], v[16:17]\n"
"v_pk_mul_f32 v[152:153], v[152:153], v[20:21]\nv_pk_mul_f32 v[154:155], v[154:155], v[20:21]\n"
"v_pk_mul_f32 v[148:149], v[148:149], v[18:19]\nv_pk_mul_f32 v[150:151], v[150:151], v[18:19]\n"
"v_pk_mul_f32 v[164:165], v[164:165], v[18:19]\nv_pk_mul_f32 v[166:167], v[166:167], v[18:19]\nCV 34,144,145\n"
"Mvav 168,200,116\nDT 222,5,11264\nDT 224,1,12288\nMvav 172,196,120\nDT 226,5,12288\nCV 35,146,147\n"
"Mvav 172,200,124\nCV 40,160,161\nCV 41,162,163\nMvav 176,204,96\nCV 66,152,153\nCV 67,154,155\n"
"Mvav 176,208,100\nML 132,132,18\nML 133,133,19\nMvav 180,204,104\nML 134,134,18\nML 135,135,19\n"
"Mvav 180,208,108\nDT 228,2,11264\nCV 36,132,133\nMvav 184,204,112\nDT 230,6,11264\nCV 37,134,135\n"
"Mvav 184,208,116\nWL 2\nDT 232,2,12288\nCV 38,148,149\nMvav 188,204,120\nDT 234,6,12288\nCV 39,150,151\n"
"CV 44,164,165\nCV 45,166,167\nv_mov_b32 v23, v22\nv_pk_mul_f32 v[176:177], v[176:177], v[16:17]\n"
"v_pk_mul_f32 v[178:179], v[178:179], v[16:17]\nv_pk_mul_f32 v[168:169], v[168:169], v[20:21]\n"
"v_pk_mul_f32 v[170:171], v[170:171], v[20:21]\nv_pk_mul_f32 v[184:185], v[184:185], v[20:21]\n"
"v_pk_mul_f32 v[186:187], v[186:187], v[20:21]\nv_pk_mul_f32 v[180:181], v[180:181], v[18:19]\n"
"v_pk_mul_f32 v[182:183], v[182:183], v[18:19]\nv_pk_mul_f32 v[156:157], v[156:157], v[22:23]\n"
"v_pk_mul_f32 v[158:159], v[158:159], v[22:23]\nv_pk_mul_f32 v[172:173], v[172:173], v[22:23]\n"
"v_pk_mul_f32 v[174:175], v[174:175], v[22:23]\nCV 42,176,177\nMvav 188,208,124\nCV 43,178,179\nCV 72,168,169\n"
"Mvav 192,212,96\nCV 73,170,171\nCV 74,184,185\nMvav 192,216,100\nDT 172,3,11264\nCV 75,186,187\n"
"Mvav 196,212,104\nDT 174,7,11264\nCV 46,180,181\nMvav 196,216,108\ns_waitcnt vmcnt(8)\nDT 176,3,12288\n"
"CV 47,182,183\nMvav 200,212,112\ns_barrier\nDT 178,7,12288\nML 140,140,22\nMvav 200,216,116\nLD 12,48,84,0\n"
"Mvav 204,212,120\ns_add_u32 s84, s84, s85\nDT 180,0,18432\nDT 182,4,18432\nMvav 204,216,124\nDT 184,0,19456\n"
"ML 141,141,23\nMvav 208,220,96\nDT 186,4,19456\nML 142,142,22\nMvav 208,224,100\nDT 144,0,20480\nML 143,143,23\n"
"Mvav 212,220,104\nDT 146,4,20480\nCV 68,140,141\nCV 69,142,143\nCV 70,156,157\nCV 71,158,159\n"
"v_pk_mul_f32 v[192:193], v[192:193], v[16:17]\nv_pk_mul_f32 v[194:195], v[194:195], v[16:17]\n"
"v_pk_mul_f32 v[208:209], v[208:209], v[16:17]\nv_pk_mul_f32 v[210:211], v[210:211], v[16:17]\n"
"v_pk_mul_f32 v[200:201], v[200:201], v[20:21]\nv_pk_mul_f32 v[202:203], v[202:203], v[20:21]\n"
"v_pk_mul_f32 v[196:197], v[196:197], v[18:19]\nv_pk_mul_f32 v[198:199], v[198:199], v[18:19]\n"
"v_pk_mul_f32 v[188:189], v[188:189], v[22:23]\nv_pk_mul_f32 v[190:191], v[190:191], v[22:23]\n"
"v_pk_mul_f32 v[204:205], v[204:205], v[22:23]\nv_pk_mul_f32 v[206:207], v[206:207], v[22:23]\nCV 48,192,193\n"
"Mvav 212,224,108\nDT 148,0,21504\nCV 49,194,195\nMvav 216,220,112\nLD 12,48,84,1024\nMvav 216,224,116\nWL 5\n"
"DT 150,4,21504\nDT 152,0,22528\nMvav 220,220,120\ns_add_u32 s84, s84, s85\nDT 154,4,22528\nCV 50,208,209\n"
"Mvav 220,224,124\nDT 156,0,23552\nCV 51,210,211\nMvav 224,228,96\nDT 158,4,23552\nCV 80,200,201\n"
"Mvav 224,232,100\nDT 160,0,24576\nCV 81,202,203\nMvav 228,228,104\nDT 162,4,24576\nCV 52,196,197\n"
"Mvav 228,232,108\nLD 12,48,84,2048\nMvav 232,228,112\ns_add_u32 s84, s84, s85\nDT 164,0,25600\nDT 166,4,25600\n"
"Mvav 232,232,116\nDT 168,1,18432\nCV 53,198,199\nMvav 236,228,120\nDT 170,5,18432\nCV 76,172,173\n"
"CV 77,174,175\nCV 78,188,189\nCV 79,190,191\nv_pk_mul_f32 v[224:225], v[224:225], v[16:17]\n"
"v_pk_mul_f32 v[226:227], v[226:227], v[16:17]\nv_pk_mul_f32 v[216:217], v[216:217], v[20:21]\n"
"v_pk_mul_f32 v[218:219], v[218:219], v[20:21]\nv_pk_mul_f32 v[232:233], v[232:233], v[20:21]\n"
"v_pk_mul_f32 v[234:235], v[234:235], v[20:21]\nv_pk_mul_f32 v[212:213], v[212:213], v[18:19]\n"
"v_pk_mul_f32 v[214:215], v[214:215], v[18:19]\nv_pk_mul_f32 v[228:229], v[228:229], v[18:19]\n"
"v_pk_mul_f32 v[230:231], v[230:231], v[18:19]\nv_pk_mul_f32 v[220:221], v[220:221], v[22:23]\n"
"v_pk_mul_f32 v[222:223], v[222:223], v[22:23]\nCV 56,224,225\nMvav 236,232,124\nDT 192,1,19456\nCV 57,226,227\n"
"Mvav 240,172,96\nDT 194,5,19456\nCV 82,216,217\nMvav 240,176,100\nDT 196,1,20480\nCV 83,218,219\n"
"Mvav 244,172,104\nDT 198,5,20480\nCV 88,232,233\nMvav 244,176,108\nDT 200,1,21504\nCV 89,234,235\n"
"Mvav 248,172,112\nLD 12,48,84,3072\nMvav 248,176,116\ns_mov_b32 s84, 147712\nDT 202,5,21504\nDT 204,1,22528\n"
"Mvav 252,172,120\nWL 6\nDT 206,5,22528\nCV 54,212,213\nMvav 252,176,124\ns_addk_i32 m0, 4096\nDT 208,1,23552\n"
"CV 55,214,215\nMvavz 96,180,32\nDT 210,5,23552\nCV 60,228,229\nCV 61,230,231\nCV 84,204,205\nCV 85,206,207\n"
"v_pk_mul_f32 v[240:241], v[240:241], v[16:17]\nv_pk_mul_f32 v[242:243], v[242:243], v[16:17]\n"
"v_pk_mul_f32 v[248:249], v[248:249], v[20:21]\nv_pk_mul_f32 v[250:251], v[250:251], v[20:21]\n"
"v_pk_mul_f32 v[244:245], v[244:245], v[18:19]\nv_pk_mul_f32 v[246:247], v[246:247], v[18:19]\n"
"v_pk_mul_f32 v[236:237], v[236:237], v[22:23]\nv_pk_mul_f32 v[238:239], v[238:239], v[22:23]\n"
"v_pk_mul_f32 v[252:253], v[252:253], v[22:23]\nv_pk_mul_f32 v[254:255], v[254:255], v[22:23]\nCV 58,240,241\n"
"Mvav 96,184,40\nDT 212,1,24576\nCV 59,242,243\nMvav 96,144,48\nDT 214,5,24576\nCV 90,248,249\nMvav 96,148,56\n"
"DT 216,1,25600\nCV 91,250,251\nMvav 96,152,64\nDT 218,5,25600\nCV 62,244,245\nMvav 96,156,72\nDT 220,2,18432\n"
"CV 63,246,247\nMvav 96,160,80\nDT 222,6,18432\nCV 86,220,221\nMvav 96,164,88\nDT 224,2,19456\nCV 87,222,223\n"
"Mvavz 100,180,36\nDT 226,6,19456\nCV 92,236,237\nMvav 100,184,44\nDT 228,2,20480\nCV 93,238,239\n"
"Mvav 100,144,52\nDT 230,6,20480\nCV 94,252,253\nMvav 100,148,60\nDT 232,2,21504\nCV 95,254,255\n"
"Mvav 100,152,68\nLD 12,48,84,0\nMvav 100,156,76\ns_add_u32 s84, s84, s85\nDT 234,6,21504\nDT 172,2,22528\n"
"Mvav 100,160,84\nWL 6\nDT 174,6,22528\nMvav 100,164,92\nDT 176,2,23552\nMvavz 104,168,32\nLD 12,48,84,1024\n"
"Mvav 104,192,40\ns_add_u32 s84, s84, s85\nDT 178,6,23552\nDT 180,2,24576\nMvav 104,196,48\nDT 182,6,24576\n"
"Mvav 104,200,56\nDT 184,2,25600\nMvav 104,204,64\nLD 12,48,84,2048\nMvav 104,208,72\ns_add_u32 s84, s84, s85\n"
"DT 186,6,25600\nDT 144,3,18432\nMvav 104,212,80\nDT 146,7,18432\nMvav 104,216,88\nDT 148,3,19456\n"
"Mvavz 108,168,36\nLD 12,48,84,3072\nMvav 108,192,44\ns_mov_b32 s84, 294912\nDT 150,7,19456\nDT 152,3,20480\n"
"Mvav 108,196,52\ns_addk_i32 m0, 5120\nDT 154,7,20480\nMvav 108,200,60\nDT 156,3,21504\nMvav 108,204,68\n"
"DT 158,7,21504\nMvav 108,208,76\nDT 160,3,22528\nMvav 108,212,84\nWL 6\nDT 162,7,22528\nMvav 108,216,92\n"
"DT 164,3,23552\nMvavz 112,220,32\nDT 166,7,23552\nMvav 112,224,40\nDT 168,3,24576\nMvav 112,228,48\n"
"DT 170,7,24576\nMvav 112,232,56\ns_waitcnt vmcnt(8)\nDT 192,3,25600\nMvav 112,172,64\ns_barrier\n"
"DT 194,7,25600\nMvav 112,176,72\nLD 12,48,84,0\nMvav 112,180,80\ns_add_u32 s84, s84, s85\nDT 196,0,31744\n"
"DT 198,4,31744\nMvav 112,184,88\nDT 200,0,32768\nMvavz 116,220,36\nDT 202,4,32768\nMvav 116,224,44\n"
"s_mov_b64 exec, 1\nglobal_load_dword v14, v13, s[96:97] sc1\ns_mov_b64 exec, -1\nMvav 116,228,52\n"
"DT 204,0,33792\nDT 206,4,33792\nMvav 116,232,60\nDT 208,0,34816\nMvav 116,172,68\nDT 210,4,34816\n"
"Mvav 116,176,76\nLD 12,48,84,1024\nMvav 116,180,84\nWL 5\nDT 212,0,35840\nDT 214,4,35840\nMvav 116,184,92\n"
"s_add_u32 s84, s84, s85\nDT 216,0,36864\nMvavz 120,144,32\nDT 218,4,36864\nMvav 120,148,40\nDT 220,0,37888\n"
"Mvav 120,152,48\nDT 222,4,37888\nMvav 120,156,56\nLD 12,48,84,2048\nMvav 120,160,64\ns_add_u32 s84, s84, s85\n"
"DT 224,0,38912\nDT 226,4,38912\nMvav 120,164,72\nDT 228,1,31744\nMvav 120,168,80\nDT 230,5,31744\n"
"Mvav 120,192,88\nDT 232,1,32768\nMvavz 124,144,36\nDT 234,5,32768\nMvav 124,148,44\nDT 172,1,33792\n"
"Mvav 124,152,52\nLD 12,48,84,3072\nMvav 124,156,60\ns_mov_b32 s84, 442368\nDT 174,5,33792\nDT 176,1,34816\n"
"Mvav 124,160,68\ns_addk_i32 m0, 4096\nDT 178,5,34816\nMvav 124,164,76\nDT 180,1,35840\nMvav 124,168,84\nWL 6\n"
"DT 182,5,35840\nMvav 124,192,92\nDT 184,1,36864\nMvavz 128,196,32\nDT 186,5,36864\nMvav 128,200,40\n"
"LD 12,48,84,0\nMvav 128,204,48\ns_add_u32 s84, s84, s85\nDT 144,1,37888\nDT 146,5,37888\ns_nop 0\n"
"Lw14_%=:\ns_cbranch_execz Lret3_%=\nMvav 128,208,56\nDT 148,1,38912\nMvav 128,212,64\nDT 150,5,38912\n"
"Mvav 128,216,72\nDT 152,2,31744\nMvav 128,220,80\nDT 154,6,31744\nMvav 128,224,88\nDT 156,2,32768\n"
"Mvavz 132,196,36\nLD 12,48,84,1024\nMvav 132,200,44\ns_add_u32 s84, s84, s85\nDT 158,6,32768\nDT 160,2,33792\n"
"Mvav 132,204,52\nDT 162,6,33792\nMvav 132,208,60\nDT 164,2,34816\nMvav 132,212,68\nDT 166,6,34816\n"
"s_waitcnt vmcnt(5)\nv_cmp_eq_u32 vcc, s80, v14\nMvav 132,216,76\ns_bitcmp1_b32 vcc_lo, 0\n"
"s_cbranch_scc1 Lpos_%=\n"
"Lpor_%=:\nbuffer_load_dwordx4 a[64:67], v13, s[68:71], s74 offen sc1\nMvav 132,220,84\nWL 5\nDT 168,2,35840\n"
"DT 170,6,35840\nMvav 132,224,92\ns_movk_i32 s74, 4096\nDT 192,2,36864\nMvavz 136,228,32\nDT 194,6,36864\n"
"Mvav 136,232,40\nbuffer_load_dwordx4 a[72:75], v13, s[68:71], s75 offen sc1\nMvav 136,172,48\n"
"s_movk_i32 s75, 6144\nDT 196,2,37888\nDT 198,6,37888\nMvav 136,176,56\nDT 200,2,38912\nMvav 136,180,64\n"
"DT 202,6,38912\nMvav 136,184,72\nbuffer_load_dwordx4 a[80:83], v13, s[68:71], s74 offen sc1\nMvav 136,144,80\n"
"s_movk_i32 s74, 16384\nDT 204,3,31744\nDT 206,7,31744\nMvav 136,148,88\nDT 208,3,32768\nMvavz 140,228,36\n"
"DT 210,7,32768\nMvav 140,232,44\nbuffer_load_dwordx4 a[88:91], v13, s[68:71], s75 offen sc1\nMvav 140,172,52\n"
"s_movk_i32 s75, 18432\nDT 212,3,33792\nDT 214,7,33792\nMvav 140,176,60\nDT 216,3,34816\nMvav 140,180,68\n"
"DT 218,7,34816\nMvav 140,184,76\nbuffer_load_dwordx4 a[96:99], v13, s[68:71], s74 offen sc1\nMvav 140,144,84\n"
"WL 5\nDT 220,3,35840\nDT 222,7,35840\nMvav 140,148,92\ns_movk_i32 s74, 20480\nDT 224,3,36864\nMvavz 144,152,32\n"
"DT 226,7,36864\nMvav 144,156,40\nbuffer_load_dwordx4 a[104:107], v13, s[68:71], s75 offen sc1\nMvav 144,160,48\n"
"s_movk_i32 s75, 22528\nDT 228,3,37888\nDT 230,7,37888\nMvav 144,164,56\ns_barrier\nDT 232,3,38912\n"
"Mvav 144,168,64\nDT 234,7,38912\nMvav 144,192,72\nbuffer_load_dwordx4 a[112:115], v13, s[68:71], s74 offen sc1\n"
"Mvav 144,196,80\ns_movk_i32 s74, 1024\nDT 172,0,0\nDT 174,4,0\nMvav 144,200,88\nDT 176,0,1024\n"
"Mvavz 148,152,36\nDT 178,4,1024\nMvav 148,156,44\nbuffer_load_dwordx4 a[120:123], v13, s[68:71], s75 offen sc1\n"
"Mvav 148,160,52\ns_movk_i32 s75, 3072\nDT 180,0,2048\nDT 182,4,2048\nMvav 148,164,60\nDT 184,0,3072\n"
"Mvav 148,168,68\nDT 186,4,3072\nMvav 148,192,76\nbuffer_load_dwordx4 a[68:71], v13, s[68:71], s74 offen sc1\n"
"Mvav 148,196,84\nWL 5\nDT 144,0,4096\nDT 146,4,4096\nMvav 148,200,92\ns_movk_i32 s74, 5120\nDT 148,0,5120\n"
"Mvavz 152,204,32\nDT 150,4,5120\nMvav 152,208,40\nbuffer_load_dwordx4 a[76:79], v13, s[68:71], s75 offen sc1\n"
"Mvav 152,212,48\ns_movk_i32 s75, 7168\nDT 152,0,6144\nDT 154,4,6144\nMvav 152,216,56\nDT 156,0,7168\n"
"Mvav 152,220,64\nDT 158,4,7168\nMvav 152,224,72\nbuffer_load_dwordx4 a[84:87], v13, s[68:71], s74 offen sc1\n"
"Mvav 152,228,80\ns_movk_i32 s74, 17408\nDT 160,1,0\nDT 162,5,0\nMvav 152,232,88\nDT 164,1,1024\n"
"Mvavz 156,204,36\nDT 166,5,1024\nMvav 156,208,44\nbuffer_load_dwordx4 a[92:95], v13, s[68:71], s75 offen sc1\n"
"Mvav 156,212,52\ns_movk_i32 s75, 19456\nDT 168,1,2048\nDT 170,5,2048\nMvav 156,216,60\nDT 192,1,3072\n"
"Mvav 156,220,68\nDT 194,5,3072\nMvav 156,224,76\nbuffer_load_dwordx4 a[100:103], v13, s[68:71], s74 offen sc1\n"
"Mvav 156,228,84\nWL 5\nDT 196,1,4096\nDT 198,5,4096\nMvav 156,232,92\ns_movk_i32 s74, 21504\nDT 200,1,5120\n"
"Mvavz 160,172,32\nDT 202,5,5120\nMvav 160,176,40\nbuffer_load_dwordx4 a[108:111], v13, s[68:71], s75 offen sc1\n"
"Mvav 160,180,48\ns_movk_i32 s75, 23552\nDT 204,1,6144\nDT 206,5,6144\nMvav 160,184,56\nDT 208,1,7168\n"
"Mvav 160,144,64\nDT 210,5,7168\nMvav 160,148,72\nbuffer_load_dwordx4 a[116:119], v13, s[68:71], s74 offen sc1\n"
"Mvav 160,152,80\ns_add_u32 s74, s82, 0\nDT 212,2,0\nDT 214,6,0\nMvav 160,156,88\nDT 216,2,1024\n"
"Mvavz 164,172,36\nDT 218,6,1024\nMvav 164,176,44\nbuffer_load_dwordx4 a[124:127], v13, s[68:71], s75 offen sc1\n"
"Mvav 164,180,52\ns_add_u32 s75, s82, 4096\nDT 220,2,2048\nDT 222,6,2048\nMvav 164,184,60\nDT 224,2,3072\n"
"Mvav 164,144,68\nDT 226,6,3072\nMvav 164,148,76\nLD 12,48,84,2048\ns_add_u32 s84, s84, s85\nMvav 164,152,84\n"
"WL 5\nDT 228,2,4096\nDT 230,6,4096\nMvav 164,156,92\nDT 232,2,5120\nMvavz 168,160,32\nDT 234,6,5120\n"
"Mvav 168,164,40\nLD 12,48,84,3072\ns_addk_i32 m0, 5120\ns_mov_b32 s84, 295040\nMvav 168,168,48\nDT 172,2,6144\n"
"DT 174,6,6144\nMvav 168,192,56\nDT 176,2,7168\nMvav 168,196,64\nDT 178,6,7168\nMvav 168,200,72\nLD 12,48,84,0\n"
"s_add_u32 s84, s84, s85\nMvav 168,204,80\nDT 180,3,0\nDT 182,7,0\nMvav 168,208,88\nDT 184,3,1024\n"
"Mvavz 172,160,36\nDT 186,7,1024\nMvav 172,164,44\nLD 12,48,84,1024\ns_add_u32 s84, s84, s85\nMvav 172,168,52\n"
"DT 144,3,2048\nDT 146,7,2048\nMvav 172,192,60\nDT 148,3,3072\nMvav 172,196,68\nDT 150,7,3072\nMvav 172,200,76\n"
"LD 12,48,84,2048\ns_add_u32 s84, s84, s85\nMvav 172,204,84\nWL 5\nDT 152,3,4096\nDT 154,7,4096\n"
"Mvav 172,208,92\nDT 156,3,5120\nMvavz 176,212,32\nDT 158,7,5120\nMvav 176,216,40\nLD 12,48,84,3072\n"
"s_addk_i32 m0, 4096\ns_mov_b32 s84, 442496\nMvav 176,220,48\nDT 160,3,6144\nDT 162,7,6144\nMvav 176,224,56\n"
"s_waitcnt vmcnt(4)\nDT 164,3,7168\nMvav 176,228,64\ns_barrier\nDT 166,7,7168\nMvav 176,232,72\nLD 12,48,84,0\n"
"s_add_u32 s84, s84, s85\nMvav 176,172,80\nDT 168,0,9216\nDT 170,4,9216\nMvav 176,176,88\nDT 192,0,10240\n"
"Mvavz 180,212,36\nDT 194,4,10240\nMvav 180,216,44\nLD 12,48,84,1024\ns_add_u32 s84, s84, s85\nMvav 180,220,52\n"
"DT 196,0,11264\nDT 198,4,11264\nMvav 180,224,60\nDT 200,0,12288\nMvav 180,228,68\nDT 202,4,12288\n"
"Mvav 180,232,76\nLD 12,48,84,2048\ns_add_u32 s84, s84, s85\nMvav 180,172,84\nWL 5\nDT 204,0,13312\n"
"DT 206,4,13312\nMvav 180,176,92\nDT 208,0,14336\nMvavz 184,180,32\nDT 210,4,14336\nMvav 184,184,40\n"
"LD 12,48,84,3072\nMvav 184,144,48\ns_mov_b32 s84, 295168\nDT 212,0,15360\nDT 214,4,15360\nMvav 184,148,56\n"
"s_addk_i32 m0, 9216\nDT 216,0,16384\nMvav 184,152,64\nDT 218,4,16384\nMvav 184,156,72\nLD 12,48,84,0\n"
"Mvav 184,160,80\ns_add_u32 s84, s84, s85\nDT 220,1,9216\nDT 222,5,9216\nMvav 184,164,88\nDT 224,1,10240\n"
"Mvavz 188,180,36\nDT 226,5,10240\nMvav 188,184,44\nLD 12,48,84,1024\nMvav 188,144,52\ns_add_u32 s84, s84, s85\n"
"DT 228,1,11264\nDT 230,5,11264\nMvav 188,148,60\nDT 232,1,12288\nMvav 188,152,68\nDT 234,5,12288\n"
"Mvav 188,156,76\nLD 12,48,84,2048\nMvav 188,160,84\nWL 5\nDT 172,1,13312\nDT 174,5,13312\nMvav 188,164,92\n"
"s_add_u32 s84, s84, s85\nDT 176,1,14336\nMvaa 96,168,64\nDT 178,5,14336\nMvaa 96,192,72\nLD 12,48,84,3072\n"
"Mvaa 96,196,80\ns_mov_b32 s84, 442624\nDT 180,1,15360\nDT 182,5,15360\nMvaa 96,200,88\ns_addk_i32 m0, 4096\n"
"DT 184,1,16384\nMvaa 96,204,96\nDT 186,5,16384\nMvaa 96,208,104\nLD 12,48,84,0\nMvaa 96,212,112\n"
"s_add_u32 s84, s84, s85\nDT 144,2,9216\nDT 146,6,9216\nMvaa 96,216,120\nDT 148,2,10240\nMvaa 100,168,68\n"
"DT 150,6,10240\nMvaa 100,192,76\nLD 12,48,84,1024\nMvaa 100,196,84\ns_add_u32 s84, s84, s85\nDT 152,2,11264\n"
"DT 154,6,11264\nMvaa 100,200,92\nDT 156,2,12288\nCV 24,96,97\nMvaa 100,204,100\nDT 158,6,12288\nCV 25,98,99\n"
"Mvaa 100,208,108\nDT 160,2,13312\nMvaa 100,212,116\nWL 6\nDT 162,6,13312\nMvaa 100,216,124\nLD 12,48,84,2048\n"
"Mvaa 104,220,64\ns_add_u32 s84, s84, s85\nDT 164,2,14336\nDT 166,6,14336\nMvaa 104,224,72\nDT 168,2,15360\n"
"Mvaa 104,228,80\nDT 170,6,15360\nCV 28,100,101\nMvaa 104,232,88\nDT 192,2,16384\nCV 29,102,103\n"
"Mvaa 104,172,96\nDT 194,6,16384\nMvaa 104,176,104\nDT 196,3,9216\nMvaa 104,180,112\nLD 12,48,84,3072\n"
"Mvaa 104,184,120\ns_movk_i32 s84, 384\nDT 198,7,9216\nDT 200,3,10240\nMvaa 108,220,68\ns_add_u32 m0, s72, 0\n"
"DT 202,7,10240\nMvaa 108,224,76\nDT 204,3,11264\nCV 26,104,105\nMvaa 108,228,84\nDT 206,7,11264\nCV 27,106,107\n"
"Mvaa 108,232,92\nbuffer_store_dwordx4 v[24:27], v13, s[60:63], s87 offen sc1\nMvaa 108,172,100\nDT 208,3,12288\n"
"DT 210,7,12288\nMvaa 108,176,108\ns_mov_b32 s87, 65536\nDT 212,3,13312\nMvaa 108,180,116\nWL 6\nDT 214,7,13312\n"
"Mvaa 108,184,124\nDT 216,3,14336\nMvaa 112,144,64\nDT 218,7,14336\nMvaa 112,148,72\nDT 220,3,15360\n"
"Mvaa 112,152,80\nDT 222,7,15360\nMvaa 112,156,88\ns_waitcnt vmcnt(9)\nDT 224,3,16384\nCV 30,108,109\n"
"Mvaa 112,160,96\ns_barrier\nDT 226,7,16384\nCV 31,110,111\nMvaa 112,164,104\nLD 12,48,84,0\nMvaa 112,168,112\n"
"s_add_u32 s84, s84, s85\nDT 228,0,18432\nDT 230,4,18432\nMvaa 112,192,120\nDT 232,0,19456\nMvaa 116,144,68\n"
"DT 234,4,19456\nMvaa 116,148,76\nbuffer_store_dwordx4 v[28:31], v13, s[60:63], s88 offen sc1\nMvaa 116,152,84\n"
"DT 172,0,20480\nDT 174,4,20480\nMvaa 116,156,92\ns_mov_b32 s88, 98304\nDT 176,0,21504\nCV 20,112,113\n"
"Mvaa 116,160,100\nDT 178,4,21504\nCV 21,114,115\nMvaa 116,164,108\nLD 12,48,84,1024\nMvaa 116,168,116\nWL 5\n"
"DT 180,0,22528\nDT 182,4,22528\nMvaa 116,192,124\ns_add_u32 s84, s84, s85\nDT 184,0,23552\nMvaa 120,196,64\n"
"DT 186,4,23552\nMvaa 120,200,72\nDT 144,0,24576\nMvaa 120,204,80\nDT 146,4,24576\nCV 24,116,117\n"
"Mvaa 120,208,88\nLD 12,48,84,2048\nMvaa 120,212,96\ns_add_u32 s84, s84, s85\nDT 148,0,25600\nDT 150,4,25600\n"
"Mvaa 120,216,104\nDT 152,1,18432\nCV 25,118,119\nMvaa 120,220,112\nDT 154,5,18432\nMvaa 120,224,120\n"
"DT 156,1,19456\nMvaa 124,196,68\nDT 158,5,19456\nMvaa 124,200,76\nDT 160,1,20480\nMvaa 124,204,84\n"
"LD 12,48,84,3072\nMvaa 124,208,92\ns_mov_b32 s84, 147840\nDT 162,5,20480\nDT 164,1,21504\nMvaa 124,212,100\n"
"s_addk_i32 m0, 4096\nDT 166,5,21504\nCV 22,120,121\nMvaa 124,216,108\nDT 168,1,22528\nCV 23,122,123\n"
"Mvaa 124,220,116\nWL 6\nbuffer_store_dwordx4 v[20:23], v13, s[60:63], s87 offen sc1\nMvaa 124,224,124\n"
"DT 170,5,22528\nDT 192,1,23552\nMvaa 128,228,64\ns_mov_b32 s87, 131072\nDT 194,5,23552\nMvaa 128,232,72\n"
"DT 196,1,24576\nMvaa 128,172,80\nLD 12,48,84,0\nMvaa 128,176,88\ns_add_u32 s84, s84, s85\nDT 198,5,24576\n"
"DT 200,1,25600\nMvaa 128,180,96\nDT 202,5,25600\nCV 26,124,125\nMvaa 128,184,104\nDT 204,2,18432\n"
"CV 27,126,127\nMvaa 128,144,112\nbuffer_store_dwordx4 v[24:27], v13, s[60:63], s88 offen sc1\nMvaa 128,148,120\n"
"DT 206,6,18432\nDT 208,2,19456\nMvaa 132,228,68\ns_mov_b32 s88, 163840\nDT 210,6,19456\nMvaa 132,232,76\n"
"DT 212,2,20480\nMvaa 132,172,84\nLD 12,48,84,1024\nMvaa 132,176,92\ns_add_u32 s84, s84, s85\nDT 214,6,20480\n"
"DT 216,2,21504\nMvaa 132,180,100\nDT 218,6,21504\nCV 28,128,129\nMvaa 132,184,108\nDT 220,2,22528\n"
"CV 29,130,131\nMvaa 132,144,116\nWL 6\n"
"Lw15_%=:\ns_cbranch_execz Lret3_%=\nDT 222,6,22528\nMvaa 132,148,124\ns_min_u32 s0, s77, 3\nLD 12,48,84,2048\n"
"Mvaa 136,152,64\ns_add_u32 s84, s84, s85\nDT 224,2,23552\nDT 226,6,23552\nMvaa 136,156,72\n"
"s_lshl_b32 s0, s0, 12\nDT 228,2,24576\nMvaa 136,160,80\ns_add_u32 s0, s0, Lw16_%=-Lstart_%=\nDT 230,6,24576\n"
"CV 20,132,133\nMvaa 136,164,88\ns_add_u32 s0, s0, s30\ns_addc_u32 s1, s31, 0\nDT 232,2,25600\nCV 21,134,135\n"
"Mvaa 136,168,96\ns_load_dword s83, s[0:1], 0\nDT 234,6,25600\nMvaa 136,192,104\nDT 172,3,18432\n"
"Mvaa 136,196,112\nLD 12,48,84,3072\nMvaa 136,200,120\ns_mov_b32 s84, 295296\nDT 174,7,18432\nDT 176,3,19456\n"
"Mvaa 140,152,68\ns_addk_i32 m0, 5120\nDT 178,7,19456\nMvaa 140,156,76\nDT 180,3,20480\nCV 30,136,137\n"
"Mvaa 140,160,84\nDT 182,7,20480\nCV 31,138,139\nMvaa 140,164,92\n"
"buffer_store_dwordx4 v[28:31], v13, s[60:63], s87 offen sc1\nMvaa 140,168,100\nDT 184,3,21504\nDT 186,7,21504\n"
"Mvaa 140,192,108\ns_mov_b32 s87, 196608\nDT 144,3,22528\nMvaa 140,196,116\nWL 6\nDT 146,7,22528\n"
"Mvaa 140,200,124\nDT 148,3,23552\nMvaa 144,204,64\nDT 150,7,23552\nMvaa 144,208,72\nDT 152,3,24576\n"
"Mvaa 144,212,80\nDT 154,7,24576\nMvaa 144,216,88\ns_waitcnt vmcnt(13)\nDT 156,3,25600\nCV 22,140,141\n"
"Mvaa 144,220,96\ns_barrier\nDT 158,7,25600\nCV 23,142,143\nMvaa 144,224,104\nLD 12,48,84,0\nMvaa 144,228,112\n"
"s_add_u32 s84, s84, s85\nDT 160,0,31744\nDT 162,4,31744\nMvaa 144,232,120\nDT 164,0,32768\nMvaa 148,204,68\n"
"DT 166,4,32768\nMvaa 148,208,76\nbuffer_store_dwordx4 v[20:23], v13, s[60:63], s88 offen sc1\nMvaa 148,212,84\n"
"DT 168,0,33792\nDT 170,4,33792\nMvaa 148,216,92\ns_mov_b32 s88, 229376\nDT 192,0,34816\nCV 24,144,145\n"
"Mvaa 148,220,100\nDT 194,4,34816\nCV 25,146,147\nMvaa 148,224,108\nLD 12,48,84,1024\nMvaa 148,228,116\nWL 5\n"
"DT 196,0,35840\nDT 198,4,35840\nMvaa 148,232,124\ns_add_u32 s84, s84, s85\nDT 200,0,36864\nMvaa 152,172,64\n"
"DT 202,4,36864\nMvaa 152,176,72\nDT 204,0,37888\nMvaa 152,180,80\nDT 206,4,37888\nCV 28,148,149\n"
"Mvaa 152,184,88\ns_waitcnt vmcnt(7)\ns_bfm_b64 exec, 1, 16\nglobal_atomic_add v13, v9, s[98:99]\n"
"s_mov_b64 exec, -1\nMvaa 152,144,96\nDT 208,0,38912\nDT 210,4,38912\nMvaa 152,148,104\nDT 212,1,31744\n"
"CV 29,150,151\nMvaa 152,152,112\nDT 214,5,31744\nMvaa 152,156,120\nLD 12,48,84,2048\nMvaa 156,172,68\n"
"s_add_u32 s84, s84, s85\nDT 216,1,32768\nDT 218,5,32768\nMvaa 156,176,76\nDT 220,1,33792\nMvaa 156,180,84\n"
"DT 222,5,33792\nCV 26,152,153\nMvaa 156,184,92\nLD 12,48,84,3072\nMvaa 156,144,100\ns_mov_b32 s84, 442752\n"
"DT 224,1,34816\nDT 226,5,34816\nMvaa 156,148,108\ns_addk_i32 m0, 4096\nDT 228,1,35840\nCV 27,154,155\n"
"Mvaa 156,152,116\nWL 7\nDT 230,5,35840\nMvaa 156,156,124\n"
"buffer_store_dwordx4 v[24:27], v13, s[60:63], s87 offen sc1\nMvaa 160,160,64\nDT 232,1,36864\nDT 234,5,36864\n"
"Mvaa 160,164,72\ns_mov_b32 s87, 262144\nDT 172,1,37888\nMvaa 160,168,80\nDT 174,5,37888\nCV 30,156,157\n"
"Mvaa 160,192,88\nLD 12,48,84,0\nMvaa 160,196,96\ns_add_u32 s84, s84, s85\nDT 176,1,38912\nDT 178,5,38912\n"
"Mvaa 160,200,104\ns_mov_b64 exec, 0\ns_setpc_b64 s[0:1]\n"
"Lrl00_%=:\ns_mov_b64 exec, -1\nDT 180,2,31744\nCV 31,158,159\nMvaa 160,204,112\nDT 182,6,31744\n"
"Mvaa 160,208,120\nbuffer_store_dwordx4 v[28:31], v13, s[60:63], s88 offen sc1\nMvaa 164,160,68\nDT 184,2,32768\n"
"DT 186,6,32768\nMvaa 164,164,76\ns_mov_b32 s88, 294912\nDT 144,2,33792\nMvaa 164,168,84\nDT 146,6,33792\n"
"CV 20,160,161\nMvaa 164,192,92\nLD 12,48,84,1024\nMvaa 164,196,100\ns_add_u32 s84, s84, s85\nDT 148,2,34816\n"
"DT 150,6,34816\nMvaa 164,200,108\nDT 152,2,35840\nCV 21,162,163\nMvaa 164,204,116\nWL 7\nDT 154,6,35840\n"
"Mvaa 164,208,124\nLD 12,48,84,2048\nMvaa 168,212,64\ns_add_u32 s84, s84, s85\nDT 156,2,36864\nDT 158,6,36864\n"
"Mvaa 168,216,72\nDT 160,2,37888\nMvaa 168,220,80\nDT 162,6,37888\nCV 24,164,165\nMvaa 168,224,88\n"
"DT 164,2,38912\nCV 25,166,167\nMvaa 168,228,96\nDT 166,6,38912\nMvaa 168,232,104\nDT 168,3,31744\n"
"Mvaa 168,172,112\nLD 12,48,84,3072\nMvaa 168,176,120\ns_movk_i32 s84, 512\nDT 170,7,31744\nDT 192,3,32768\n"
"Mvaa 172,212,68\ns_addk_i32 m0, 5120\nDT 194,7,32768\nMvaa 172,216,76\nDT 196,3,33792\nCV 22,168,169\n"
"Mvaa 172,220,84\nDT 198,7,33792\nCV 23,170,171\nMvaa 172,224,92\n"
"buffer_store_dwordx4 v[20:23], v13, s[60:63], s87 offen sc1\nMvaa 172,228,100\nDT 200,3,34816\nDT 202,7,34816\n"
"Mvaa 172,232,108\ns_mov_b32 s87, 327680\nDT 204,3,35840\nMvaa 172,172,116\nWL 6\nDT 206,7,35840\n"
"Mvaa 172,176,124\nDT 208,3,36864\nMvaa 176,180,64\nDT 210,7,36864\nMvaa 176,184,72\nDT 212,3,37888\n"
"Mvaa 176,144,80\nDT 214,7,37888\nMvaa 176,148,88\ns_waitcnt vmcnt(14)\nDT 216,3,38912\nCV 26,172,173\n"
"Mvaa 176,152,96\ns_barrier\nDT 218,7,38912\nv_lshrrev_b32 v17, 8, v13\nMvaa 176,156,104\nLD 12,48,84,0\n"
"Mvaa 176,160,112\ns_add_u32 s84, s84, s85\nDT 220,0,0\nDT 222,4,0\nMvaa 176,164,120\nDT 224,0,1024\n"
"v_bfe_u32 v18, v13, 4, 4\nMvaa 180,180,68\nDT 226,4,1024\nv_mul_u32_u24 v18, 1152, v18\nMvaa 180,184,76\n"
"DT 228,0,2048\nv_lshl_add_u32 v11, v17, 4, v18\nMvaa 180,144,84\nDT 230,4,2048\nv_bfe_u32 v18, v13, 6, 1\n"
"Mvaa 180,148,92\nDT 232,0,3072\nv_lshlrev_b32 v18, 1, v18\nMvaa 180,152,100\nLD 12,48,84,1024\n"
"Mvaa 180,156,108\ns_add_u32 s84, s84, s85\nDT 234,4,3072\nDT 172,0,4096\nMvaa 180,160,116\nWL 6\nDT 174,4,4096\n"
"v_sub_u32 v17, v17, v18\nMvaa 180,164,124\nDT 176,0,5120\nv_bfe_u32 v18, v13, 7, 1\nMvaa 184,168,64\n"
"DT 178,4,5120\nv_bfe_u32 v19, v13, 5, 1\nMvaa 184,192,72\nDT 180,0,6144\nv_lshl_add_u32 v18, v18, 1, v19\n"
"Mvaa 184,196,80\nDT 182,4,6144\nv_lshl_add_u32 v17, v17, 2, v18\nMvaa 184,200,88\nLD 12,48,84,2048\n"
"Mvaa 184,204,96\ns_add_u32 s84, s84, s85\nDT 184,0,7168\nDT 186,4,7168\nMvaa 184,208,104\nDT 144,1,0\n"
"v_and_b32 v18, 16, v13\nMvaa 184,212,112\nDT 146,5,0\nv_mov_b32 v19, 0x3f80\nMvaa 184,216,120\n"
"s_waitcnt vmcnt(7)\ns_bfm_b64 exec, 1, 20\nglobal_atomic_add v13, v9, s[98:99]\ns_mov_b64 exec, -1\n"
"Mvaa 188,168,68\nDT 148,1,1024\nDT 150,5,1024\nMvaa 188,192,76\nDT 152,1,2048\nv_lshlrev_b32 v18, v18, v19\n"
"Mvaa 188,196,84\nDT 154,5,2048\nv_cmp_eq_u32 vcc, 0, v17\nMvaa 188,200,92\nLD 12,48,84,3072\nMvaa 188,204,100\n"
"s_mov_b32 s84, 147968\nDT 156,1,3072\nDT 158,5,3072\nMvaa 188,208,108\ns_addk_i32 m0, 4096\nDT 160,1,4096\n"
"v_cndmask_b32 v19, 0, v18, vcc\nMvaa 188,212,116\nWL 7\nDT 162,5,4096\nv_cmp_eq_u32 vcc, 1, v17\n"
"Mvaa 188,216,124\nDT 164,1,5120\nAW 240,19\nMvavz 96,220,32\nDT 166,5,5120\nv_cndmask_b32 v19, 0, v18, vcc\n"
"Mvav 96,224,40\nLD 12,48,84,0\nMvav 96,228,48\ns_add_u32 s84, s84, s85\nDT 168,1,6144\nDT 170,5,6144\n"
"Mvav 96,232,56\nDT 192,1,7168\nv_cmp_eq_u32 vcc, 2, v17\nMvav 96,172,64\nDT 194,5,7168\nAW 241,19\n"
"Mvav 96,176,72\nDT 196,2,0\nv_cndmask_b32 v19, 0, v18, vcc\nMvav 96,180,80\nDT 198,6,0\n"
"v_cmp_eq_u32 vcc, 3, v17\nMvav 96,184,88\nDT 200,2,1024\nAW 242,19\nMvavz 100,220,36\nLD 12,48,84,1024\n"
"Mvav 100,224,44\ns_add_u32 s84, s84, s85\nDT 202,6,1024\nDT 204,2,2048\nMvav 100,228,52\nDT 206,6,2048\n"
"v_cndmask_b32 v19, 0, v18, vcc\nMvav 100,232,60\nDT 208,2,3072\nv_cmp_eq_u32 vcc, 4, v17\nMvav 100,172,68\n"
"DT 210,6,3072\nAW 243,19\nMvav 100,176,76\nDT 212,2,4096\nv_cndmask_b32 v19, 0, v18, vcc\nMvav 100,180,84\n"
"WL 6\nDT 214,6,4096\nv_cmp_eq_u32 vcc, 5, v17\nMvav 100,184,92\nLD 12,48,84,2048\nMvavz 104,144,32\n"
"s_add_u32 s84, s84, s85\nDT 216,2,5120\nDT 218,6,5120\nMvav 104,148,40\nDT 220,2,6144\nAW 244,19\n"
"Mvav 104,152,48\nDT 222,6,6144\nv_cndmask_b32 v19, 0, v18, vcc\nMvav 104,156,56\nDT 224,2,7168\n"
"v_cmp_eq_u32 vcc, 6, v17\nMvav 104,160,64\nDT 226,6,7168\nAW 245,19\nMvav 104,164,72\nDT 228,3,0\n"
"v_cndmask_b32 v19, 0, v18, vcc\nMvav 104,168,80\nLD 12,48,84,3072\nMvav 104,192,88\ns_mov_b32 s84, 295424\n"
"DT 230,7,0\nDT 232,3,1024\nMvavz 108,144,36\ns_addk_i32 m0, 9216\nDT 234,7,1024\nv_cmp_eq_u32 vcc, 7, v17\n"
"Mvav 108,148,44\nDT 172,3,2048\nAW 246,19\nMvav 108,152,52\nDT 174,7,2048\nv_cndmask_b32 v19, 0, v18, vcc\n"
"Mvav 108,156,60\nDT 176,3,3072\nAW 247,19\nMvav 108,160,68\nDT 178,7,3072\nv_mov_b32 v8, s35\nMvav 108,164,76\n"
"DT 180,3,4096\nCV 27,174,175\nMvav 108,168,84\nWL 6\n"
"buffer_store_dwordx4 v[24:27], v13, s[60:63], s88 offen sc1\nMvav 108,192,92\nDT 182,7,4096\nDT 184,3,5120\n"
"Mvavz 112,196,32\ns_mov_b32 s88, 360448\nDT 186,7,5120\nCV 28,176,177\nMvav 112,200,40\nDT 144,3,6144\n"
"CV 29,178,179\nMvav 112,204,48\nDT 146,7,6144\nCV 30,184,185\nMvav 112,208,56\ns_waitcnt vmcnt(11)\n"
"DT 148,3,7168\nCV 31,186,187\nMvav 112,212,64\ns_barrier\n"
"buffer_store_dwordx4 v[28:31], v13, s[60:63], s87 offen sc1\nMvav 112,216,72\nDT 150,7,7168\nDT 152,0,9216\n"
"Mvav 112,220,80\ns_mov_b32 s87, 393216\nDT 154,4,9216\nCV 20,180,181\nMvav 112,224,88\nDT 156,0,10240\n"
"CV 21,182,183\nMvavz 116,196,36\nLD 12,48,84,0\nMvav 116,200,44\ns_add_u32 s84, s84, s85\nDT 158,4,10240\n"
"DT 160,0,11264\nMvav 116,204,52\nDT 162,4,11264\nCV 22,188,189\nMvav 116,208,60\nDT 164,0,12288\nCV 23,190,191\n"
"Mvav 116,212,68\nLD 12,48,84,1024\nMvav 116,216,76\ns_add_u32 s84, s84, s85\nDT 166,4,12288\nDT 168,0,13312\n"
"Mvav 116,220,84\nWL 6\nDT 170,4,13312\nMvav 116,224,92\nDT 192,0,14336\nMvavz 120,228,32\n"
"buffer_store_dwordx4 v[20:23], v13, s[60:63], s88 offen sc1\nMvav 120,232,40\nDT 194,4,14336\nDT 196,0,15360\n"
"Mvav 120,172,48\ns_mov_b32 s88, 425984\nDT 198,4,15360\nMvav 120,176,56\nDT 200,0,16384\nMvav 120,180,64\n"
"LD 12,48,84,2048\nMvav 120,184,72\ns_add_u32 s84, s84, s85\nDT 202,4,16384\nDT 204,1,9216\nMvav 120,144,80\n"
"DT 206,5,9216\nMvav 120,148,88\nDT 208,1,10240\nMvavz 124,228,36\nDT 210,5,10240\nMvav 124,232,44\n"
"DT 212,1,11264\nMvav 124,172,52\nLD 12,48,84,3072\nMvav 124,176,60\ns_mov_b32 s84, 442880\nDT 214,5,11264\n"
"DT 216,1,12288\nMvav 124,180,68\ns_addk_i32 m0, 4096\nDT 218,5,12288\nMvav 124,184,76\nDT 220,1,13312\n"
"Mvav 124,144,84\nWL 6\nDT 222,5,13312\nMvav 124,148,92\nDT 224,1,14336\nMvaa 96,152,64\nDT 226,5,14336\n"
"Mvaa 96,156,72\nLD 12,48,84,0\nMvaa 96,160,80\ns_add_u32 s84, s84, s85\nDT 228,1,15360\nDT 230,5,15360\n"
"Mvaa 96,164,88\nDT 232,1,16384\nMvaa 96,168,96\nDT 234,5,16384\nMvaa 96,192,104\nDT 172,2,9216\n"
"Mvaa 96,196,112\nDT 174,6,9216\nMvaa 96,200,120\nDT 176,2,10240\nMvaa 100,152,68\nLD 12,48,84,1024\n"
"Mvaa 100,156,76\ns_add_u32 s84, s84, s85\nDT 178,6,10240\nDT 180,2,11264\nMvaa 100,160,84\nDT 182,6,11264\n"
"Lw16_%=:\ns_cbranch_execz Lrl00_%=\nCV 24,96,97\nMvaa 100,164,92\nDT 184,2,12288\nCV 25,98,99\n"
"Mvaa 100,168,100\nDT 186,6,12288\nMvaa 100,192,108\nDT 144,2,13312\nMvaa 100,196,116\nWL 6\nDT 146,6,13312\n"
"Mvaa 100,200,124\nLD 12,48,84,2048\nMvaa 104,204,64\ns_add_u32 s84, s84, s85\nDT 148,2,14336\nDT 150,6,14336\n"
"Mvaa 104,208,72\nDT 152,2,15360\nMvaa 104,212,80\nDT 154,6,15360\nCV 28,100,101\nMvaa 104,216,88\n"
"DT 156,2,16384\nCV 29,102,103\nMvaa 104,220,96\nDT 158,6,16384\nMvaa 104,224,104\nDT 160,3,9216\n"
"Mvaa 104,228,112\nLD 12,48,84,3072\nMvaa 104,232,120\ns_movk_i32 s84, 640\nDT 162,7,9216\nDT 164,3,10240\n"
"Mvaa 108,204,68\ns_add_u32 m0, s72, 0\nDT 166,7,10240\nMvaa 108,208,76\nDT 168,3,11264\nCV 26,104,105\n"
"Mvaa 108,212,84\nDT 170,7,11264\nCV 27,106,107\nMvaa 108,216,92\n"
"buffer_store_dwordx4 v[24:27], v13, s[60:63], s87 offen sc1\nMvaa 108,220,100\nDT 192,3,12288\nDT 194,7,12288\n"
"Mvaa 108,224,108\ns_mov_b32 s87, 458752\nDT 196,3,13312\nMvaa 108,228,116\nWL 6\nDT 198,7,13312\n"
"Mvaa 108,232,124\ns_waitcnt vmcnt(7)\ns_bfm_b64 exec, 1, 24\nglobal_atomic_add v13, v9, s[98:99]\n"
"s_mov_b64 exec, -1\nMvaa 112,172,64\nDT 200,3,14336\nDT 202,7,14336\nMvaa 112,176,72\nDT 204,3,15360\n"
"Mvaa 112,180,80\nDT 206,7,15360\nCV 30,108,109\nMvaa 112,184,88\ns_barrier\nDT 208,3,16384\nCV 31,110,111\n"
"Mvaa 112,144,96\nLD 12,48,84,0\nMvaa 112,148,104\ns_add_u32 s84, s84, s85\nDT 210,7,16384\nDT 212,0,18432\n"
"Mvaa 112,152,112\nDT 214,4,18432\nMvaa 112,156,120\nDT 216,0,19456\nMvaa 116,172,68\n"
"buffer_store_dwordx4 v[28:31], v13, s[60:63], s88 offen sc1\nMvaa 116,176,76\nDT 218,4,19456\nDT 220,0,20480\n"
"Mvaa 116,180,84\ns_mov_b32 s88, 491520\nDT 222,4,20480\nCV 20,112,113\nMvaa 116,184,92\nDT 224,0,21504\n"
"CV 21,114,115\nMvaa 116,144,100\nLD 12,48,84,1024\nMvaa 116,148,108\ns_add_u32 s84, s84, s85\nDT 226,4,21504\n"
"DT 228,0,22528\nMvaa 116,152,116\nWL 6\nDT 230,4,22528\nMvaa 116,156,124\nDT 232,0,23552\nMvaa 120,160,64\n"
"DT 234,4,23552\nMvaa 120,164,72\nDT 172,0,24576\nMvaa 120,168,80\nLD 12,48,84,2048\nMvaa 120,192,88\n"
"s_add_u32 s84, s84, s85\nDT 174,4,24576\nDT 176,0,25600\nMvaa 120,196,96\nDT 178,4,25600\nCV 24,116,117\n"
"Mvaa 120,200,104\nDT 180,1,18432\nCV 25,118,119\nMvaa 120,204,112\nDT 182,5,18432\nMvaa 120,208,120\n"
"DT 184,1,19456\nMvaa 124,160,68\nDT 186,5,19456\nMvaa 124,164,76\nLD 12,48,84,3072\nMvaa 124,168,84\n"
"s_mov_b32 s84, 148096\nDT 144,1,20480\nDT 146,5,20480\nMvaa 124,192,92\ns_addk_i32 m0, 4096\nDT 148,1,21504\n"
"CV 22,120,121\nMvaa 124,196,100\nDT 150,5,21504\nCV 23,122,123\nMvaa 124,200,108\n"
"buffer_store_dwordx4 v[20:23], v13, s[60:63], s87 offen sc1\nMvaa 124,204,116\nWL 5\nDT 152,1,22528\n"
"DT 154,5,22528\nMvaa 124,208,124\ns_mov_b32 s87, 524288\nDT 156,1,23552\nMvavz 128,212,32\nDT 158,5,23552\n"
"Mvav 128,216,40\nLD 12,48,84,0\nMvav 128,220,48\ns_add_u32 s84, s84, s85\nDT 160,1,24576\nDT 162,5,24576\n"
"Mvav 128,224,56\nDT 164,1,25600\nCV 26,124,125\nMvav 128,228,64\nDT 166,5,25600\nCV 27,126,127\n"
"Mvav 128,232,72\nbuffer_store_dwordx4 v[24:27], v13, s[60:63], s88 offen sc1\nMvav 128,172,80\nDT 168,2,18432\n"
"DT 170,6,18432\nMvav 128,176,88\ns_mov_b32 s88, 557056\nDT 192,2,19456\nMvavz 132,212,36\nDT 194,6,19456\n"
"Mvav 132,216,44\nLD 12,48,84,1024\nMvav 132,220,52\ns_add_u32 s84, s84, s85\nDT 196,2,20480\nDT 198,6,20480\n"
"Mvav 132,224,60\nDT 200,2,21504\nMvav 132,228,68\nDT 202,6,21504\nMvav 132,232,76\nDT 204,2,22528\n"
"Mvav 132,172,84\nWL 6\nLD 12,48,84,2048\nMvav 132,176,92\ns_add_u32 s84, s84, s85\nDT 206,6,22528\n"
"DT 208,2,23552\nMvavz 136,180,32\nDT 210,6,23552\nMvav 136,184,40\nDT 212,2,24576\nMvav 136,144,48\n"
"DT 214,6,24576\nMvav 136,148,56\nDT 216,2,25600\nMvav 136,152,64\nDT 218,6,25600\nMvav 136,156,72\n"
"LD 12,48,84,3072\nMvav 136,160,80\ns_mov_b32 s84, 295552\nDT 220,3,18432\nDT 222,7,18432\nMvav 136,164,88\n"
"s_addk_i32 m0, 5120\nDT 224,3,19456\nMvavz 140,180,36\nDT 226,7,19456\nMvav 140,184,44\nDT 228,3,20480\n"
"Mvav 140,144,52\nDT 230,7,20480\nMvav 140,148,60\nDT 232,3,21504\nMvav 140,152,68\nDT 234,7,21504\n"
"Mvav 140,156,76\nDT 172,3,22528\nMvav 140,160,84\nWL 6\nDT 174,7,22528\nMvav 140,164,92\nDT 176,3,23552\n"
"Mvavz 144,168,32\nDT 178,7,23552\nMvav 144,192,40\nDT 180,3,24576\nMvav 144,196,48\nDT 182,7,24576\n"
"Mvav 144,200,56\ns_waitcnt vmcnt(13)\nDT 184,3,25600\nMvav 144,204,64\ns_barrier\nDT 186,7,25600\n"
"Mvav 144,208,72\nLD 12,48,84,0\nMvav 144,212,80\ns_add_u32 s84, s84, s85\nDT 144,0,31744\nDT 146,4,31744\n"
"Mvav 144,216,88\nDT 148,0,32768\nMvavz 148,168,36\nDT 150,4,32768\nMvav 148,192,44\nDT 152,0,33792\n"
"Mvav 148,196,52\nDT 154,4,33792\nMvav 148,200,60\nDT 156,0,34816\nMvav 148,204,68\nLD 12,48,84,1024\n"
"Mvav 148,208,76\ns_add_u32 s84, s84, s85\nDT 158,4,34816\nDT 160,0,35840\nMvav 148,212,84\nWL 6\n"
"DT 162,4,35840\nMvav 148,216,92\nDT 164,0,36864\nMvavz 152,220,32\nDT 166,4,36864\nMvav 152,224,40\n"
"DT 168,0,37888\nMvav 152,228,48\ns_waitcnt vmcnt(5)\ns_bfm_b64 exec, 1, 28\n"
"global_atomic_add v13, v9, s[98:99]\ns_mov_b64 exec, -1\nMvav 152,232,56\nDT 170,4,37888\nDT 192,0,38912\n"
"Mvav 152,172,64\nDT 194,4,38912\nMvav 152,176,72\nDT 196,1,31744\nMvav 152,180,80\nLD 12,48,84,2048\n"
"Mvav 152,184,88\ns_add_u32 s84, s84, s85\nDT 198,5,31744\nDT 200,1,32768\nMvavz 156,220,36\nDT 202,5,32768\n"
"Mvav 156,224,44\nDT 204,1,33792\nMvav 156,228,52\nLD 12,48,84,3072\nMvav 156,232,60\ns_mov_b32 s84, 443008\n"
"DT 206,5,33792\nDT 208,1,34816\nMvav 156,172,68\ns_addk_i32 m0, 4096\nDT 210,5,34816\nMvav 156,176,76\n"
"DT 212,1,35840\nMvav 156,180,84\nWL 6\nDT 214,5,35840\nMvav 156,184,92\nDT 216,1,36864\nMvaa 128,144,64\n"
"DT 218,5,36864\nMvaa 128,148,72\nLD 12,48,84,0\nMvaa 128,152,80\ns_add_u32 s84, s84, s85\nDT 220,1,37888\n"
"DT 222,5,37888\nMvaa 128,156,88\nDT 224,1,38912\nMvaa 128,160,96\nDT 226,5,38912\nMvaa 128,164,104\n"
"DT 228,2,31744\nMvaa 128,168,112\nDT 230,6,31744\nMvaa 128,192,120\nDT 232,2,32768\nMvaa 132,144,68\n"
"LD 12,48,84,1024\nMvaa 132,148,76\ns_add_u32 s84, s84, s85\nDT 234,6,32768\nDT 172,2,33792\nMvaa 132,152,84\n"
"DT 174,6,33792\nCV 28,128,129\nMvaa 132,156,92\nDT 176,2,34816\nCV 29,130,131\nMvaa 132,160,100\n"
"DT 178,6,34816\nMvaa 132,164,108\nDT 180,2,35840\nMvaa 132,168,116\nWL 6\nDT 182,6,35840\nMvaa 132,192,124\n"
"LD 12,48,84,2048\nMvaa 136,196,64\ns_add_u32 s84, s84, s85\nDT 184,2,36864\nDT 186,6,36864\nMvaa 136,200,72\n"
"DT 144,2,37888\nMvaa 136,204,80\nDT 146,6,37888\nCV 20,132,133\nMvaa 136,208,88\nDT 148,2,38912\nCV 21,134,135\n"
"Mvaa 136,212,96\nDT 150,6,38912\nMvaa 136,216,104\nDT 152,3,31744\nMvaa 136,220,112\nLD 12,48,84,3072\n"
"Mvaa 136,224,120\ns_movk_i32 s84, 768\nDT 154,7,31744\nDT 156,3,32768\nMvaa 140,196,68\ns_addk_i32 m0, 5120\n"
"DT 158,7,32768\nMvaa 140,200,76\nDT 160,3,33792\nCV 30,136,137\nMvaa 140,204,84\nDT 162,7,33792\nCV 31,138,139\n"
"Mvaa 140,208,92\nbuffer_store_dwordx4 v[28:31], v13, s[60:63], s87 offen sc1\nMvaa 140,212,100\nDT 164,3,34816\n"
"DT 166,7,34816\nMvaa 140,216,108\ns_mov_b32 s87, 589824\nDT 168,3,35840\nMvaa 140,220,116\nWL 6\n"
"DT 170,7,35840\nMvaa 140,224,124\nDT 192,3,36864\nMvaa 144,228,64\nDT 194,7,36864\nMvaa 144,232,72\n"
"DT 196,3,37888\nMvaa 144,172,80\nDT 198,7,37888\nMvaa 144,176,88\ns_waitcnt vmcnt(10)\nDT 200,3,38912\n"
"CV 22,140,141\nMvaa 144,180,96\ns_barrier\nDT 202,7,38912\nCV 23,142,143\nMvaa 144,184,104\nLD 12,48,84,0\n"
"Mvaa 144,144,112\ns_add_u32 s84, s84, s85\nDT 204,0,0\nDT 206,4,0\nMvaa 144,148,120\nDT 208,0,1024\n"
"Mvaa 148,228,68\nDT 210,4,1024\nMvaa 148,232,76\nbuffer_store_dwordx4 v[20:23], v13, s[60:63], s88 offen sc1\n"
"Mvaa 148,172,84\nDT 212,0,2048\nDT 214,4,2048\nMvaa 148,176,92\ns_mov_b32 s88, 622592\nDT 216,0,3072\n"
"CV 24,144,145\nMvaa 148,180,100\nDT 218,4,3072\nCV 25,146,147\nMvaa 148,184,108\nLD 12,48,84,1024\n"
"Mvaa 148,144,116\nWL 5\nDT 220,0,4096\nDT 222,4,4096\nMvaa 148,148,124\ns_add_u32 s84, s84, s85\nDT 224,0,5120\n"
"Mvaa 152,152,64\nDT 226,4,5120\nMvaa 152,156,72\nDT 228,0,6144\nMvaa 152,160,80\nDT 230,4,6144\nCV 28,148,149\n"
"Mvaa 152,164,88\nLD 12,48,84,2048\nMvaa 152,168,96\ns_add_u32 s84, s84, s85\nDT 232,0,7168\nDT 234,4,7168\n"
"Mvaa 152,192,104\nDT 172,1,0\nCV 29,150,151\nMvaa 152,196,112\nDT 174,5,0\nMvaa 152,200,120\nDT 176,1,1024\n"
"Mvaa 156,152,68\nDT 178,5,1024\nMvaa 156,156,76\nDT 180,1,2048\nMvaa 156,160,84\nLD 12,48,84,3072\n"
"Mvaa 156,164,92\ns_mov_b32 s84, 148224\nDT 182,5,2048\nDT 184,1,3072\nMvaa 156,168,100\ns_addk_i32 m0, 4096\n"
"DT 186,5,3072\nCV 26,152,153\nMvaa 156,192,108\nDT 144,1,4096\nCV 27,154,155\nMvaa 156,196,116\nWL 6\n"
"buffer_store_dwordx4 v[24:27], v13, s[60:63], s87 offen sc1\nMvaa 156,200,124\nDT 146,5,4096\nDT 148,1,5120\n"
"Mvavz 96,204,32\ns_mov_b32 s87, 655360\nDT 150,5,5120\nMvav 96,208,40\nDT 152,1,6144\nMvav 96,212,48\n"
"LD 12,48,84,0\nMvav 96,216,56\ns_add_u32 s84, s84, s85\nDT 154,5,6144\nDT 156,1,7168\nMvav 96,220,64\n"
"DT 158,5,7168\nCV 30,156,157\nMvav 96,224,72\nDT 160,2,0\nCV 31,158,159\nMvav 96,228,80\n"
"buffer_store_dwordx4 v[28:31], v13, s[60:63], s88 offen sc1\nMvav 96,232,88\nDT 162,6,0\nDT 164,2,1024\n"
"Mvavz 100,204,36\ns_mov_b32 s88, 688128\nDT 166,6,1024\nMvav 100,208,44\nDT 168,2,2048\nMvav 100,212,52\n"
"LD 12,48,84,1024\nMvav 100,216,60\ns_add_u32 s84, s84, s85\nDT 170,6,2048\nDT 192,2,3072\nMvav 100,220,68\n"
"DT 194,6,3072\nMvav 100,224,76\nDT 196,2,4096\nMvav 100,228,84\nWL 6\nDT 198,6,4096\nMvav 100,232,92\n"
"LD 12,48,84,2048\nMvavz 104,172,32\ns_add_u32 s84, s84, s85\nDT 200,2,5120\nDT 202,6,5120\nMvav 104,176,40\n"
"DT 204,2,6144\nMvav 104,180,48\nDT 206,6,6144\nMvav 104,184,56\nDT 208,2,7168\nMvav 104,144,64\nDT 210,6,7168\n"
"Mvav 104,148,72\nDT 212,3,0\nMvav 104,152,80\n"
"Lw17_%=:\ns_cbranch_execz Lrl00_%=\nLD 12,48,84,3072\nMvav 104,156,88\ns_mov_b32 s84, 295680\nDT 214,7,0\n"
"DT 216,3,1024\nMvavz 108,172,36\ns_addk_i32 m0, 9216\nDT 218,7,1024\nMvav 108,176,44\nDT 220,3,2048\n"
"Mvav 108,180,52\nDT 222,7,2048\nMvav 108,184,60\nDT 224,3,3072\nMvav 108,144,68\nDT 226,7,3072\n"
"Mvav 108,148,76\nDT 228,3,4096\nMvav 108,152,84\nWL 6\nDT 230,7,4096\nMvav 108,156,92\nDT 232,3,5120\n"
"Mvavz 112,160,32\nDT 234,7,5120\nMvav 112,164,40\nDT 172,3,6144\nMvav 112,168,48\nDT 174,7,6144\n"
"Mvav 112,192,56\ns_waitcnt vmcnt(12)\nDT 176,3,7168\nMvav 112,196,64\ns_barrier\ns_mov_b32 exec_lo, 0x11110000\n"
"s_mov_b32 exec_hi, 0x11111\nglobal_load_dword v14, v13, s[98:99] sc1\ns_mov_b64 exec, -1\nMvav 112,200,72\n"
"DT 178,7,7168\nDT 180,0,9216\nMvav 112,204,80\nDT 182,4,9216\nMvav 112,208,88\nDT 184,0,10240\n"
"Mvavz 116,160,36\nLD 12,48,84,0\nMvav 116,164,44\ns_add_u32 s84, s84, s85\nDT 186,4,10240\nDT 144,0,11264\n"
"Mvav 116,168,52\nDT 146,4,11264\nMvav 116,192,60\nDT 148,0,12288\nMvav 116,196,68\nLD 12,48,84,1024\n"
"Mvav 116,200,76\ns_add_u32 s84, s84, s85\nDT 150,4,12288\nDT 152,0,13312\nMvav 116,204,84\nWL 6\n"
"DT 154,4,13312\nMvav 116,208,92\nDT 156,0,14336\nMvavz 120,212,32\nDT 158,4,14336\nMvav 120,216,40\n"
"DT 160,0,15360\nMvav 120,220,48\nDT 162,4,15360\nMvav 120,224,56\ns_waitcnt vmcnt(6)\ns_bfm_b64 exec, 1, 32\n"
"global_atomic_add v13, v9, s[98:99]\ns_mov_b64 exec, -1\nMvav 120,228,64\nDT 164,0,16384\nDT 166,4,16384\n"
"Mvav 120,232,72\nDT 168,1,9216\nMvav 120,172,80\nDT 170,5,9216\nMvav 120,176,88\nLD 12,48,84,2048\n"
"Mvavz 124,212,36\ns_add_u32 s84, s84, s85\nDT 192,1,10240\nDT 194,5,10240\nMvav 124,216,44\nDT 196,1,11264\n"
"Mvav 124,220,52\nDT 198,5,11264\nMvav 124,224,60\nLD 12,48,84,3072\nMvav 124,228,68\ns_mov_b32 s84, 443136\n"
"DT 200,1,12288\nDT 202,5,12288\nMvav 124,232,76\ns_addk_i32 m0, 4096\nDT 204,1,13312\nMvav 124,172,84\nWL 7\n"
"DT 206,5,13312\nMvav 124,176,92\nDT 208,1,14336\nMvaa 96,180,64\nDT 210,5,14336\nMvaa 96,184,72\nLD 12,48,84,0\n"
"Mvaa 96,144,80\ns_add_u32 s84, s84, s85\nDT 212,1,15360\nDT 214,5,15360\nMvaa 96,148,88\nDT 216,1,16384\n"
"Mvaa 96,152,96\nDT 218,5,16384\nMvaa 96,156,104\nDT 220,2,9216\nMvaa 96,160,112\nDT 222,6,9216\n"
"Mvaa 96,164,120\nDT 224,2,10240\nMvaa 100,180,68\nLD 12,48,84,1024\nMvaa 100,184,76\ns_add_u32 s84, s84, s85\n"
"DT 226,6,10240\nDT 228,2,11264\nMvaa 100,144,84\nDT 230,6,11264\nCV 20,96,97\nMvaa 100,148,92\nDT 232,2,12288\n"
"CV 21,98,99\nMvaa 100,152,100\nDT 234,6,12288\nMvaa 100,156,108\nDT 172,2,13312\nMvaa 100,160,116\nWL 6\n"
"DT 174,6,13312\nMvaa 100,164,124\nLD 12,48,84,2048\nMvaa 104,168,64\ns_add_u32 s84, s84, s85\nDT 176,2,14336\n"
"DT 178,6,14336\nMvaa 104,192,72\nDT 180,2,15360\nMvaa 104,196,80\nDT 182,6,15360\nCV 24,100,101\n"
"Mvaa 104,200,88\nDT 184,2,16384\nCV 25,102,103\nMvaa 104,204,96\nDT 186,6,16384\nMvaa 104,208,104\n"
"DT 144,3,9216\nMvaa 104,212,112\nLD 12,48,84,3072\nMvaa 104,216,120\ns_movk_i32 s84, 896\nDT 146,7,9216\n"
"DT 148,3,10240\nMvaa 108,168,68\ns_add_u32 m0, s72, 0\nDT 150,7,10240\nMvaa 108,192,76\nDT 152,3,11264\n"
"CV 22,104,105\nMvaa 108,196,84\nDT 154,7,11264\nCV 23,106,107\nMvaa 108,200,92\n"
"buffer_store_dwordx4 v[20:23], v13, s[60:63], s87 offen sc1\nMvaa 108,204,100\nDT 156,3,12288\nDT 158,7,12288\n"
"Mvaa 108,208,108\ns_mov_b32 s87, 720896\nDT 160,3,13312\nMvaa 108,212,116\nWL 6\nDT 162,7,13312\n"
"s_waitcnt vmcnt(10)\nv_sub_u32 v14, v14, v10\nMvaa 108,216,124\nDT 164,3,14336\nv_cmp_eq_u32 vcc, 16, v14\n"
"Mvaa 112,220,64\ns_bitcmp1_b64 vcc, s78\ns_cbranch_scc0 Lb00s_%=\n"
"Lb00r_%=:\nDT 166,7,14336\nMvaa 112,224,72\ns_bitcmp1_b64 vcc, s78\ns_cselect_b32 s42, 1179648, 0\n"
"DT 168,3,15360\nCV 26,108,109\nMvaa 112,228,80\nDT 170,7,15360\nCV 27,110,111\nMvaa 112,232,88\ns_barrier\n"
"buffer_store_dwordx4 v[24:27], v13, s[60:63], s88 offen sc1\nMvaa 112,172,96\nDT 192,3,16384\nDT 194,7,16384\n"
"Mvaa 112,176,104\ns_mov_b32 s88, 753664\nDT 196,0,18432\nMvaa 112,180,112\nDT 198,4,18432\nMvaa 112,184,120\n"
"LD 12,48,84,0\nMvaa 116,220,68\ns_add_u32 s84, s84, s85\nDT 200,0,19456\nDT 202,4,19456\nMvaa 116,224,76\n"
"DT 204,0,20480\nMvaa 116,228,84\nDT 206,4,20480\nCV 28,112,113\nMvaa 116,232,92\nLD 12,48,84,1024\n"
"Mvaa 116,172,100\ns_add_u32 s84, s84, s85\nDT 208,0,21504\nDT 210,4,21504\nMvaa 116,176,108\nDT 212,0,22528\n"
"CV 29,114,115\nMvaa 116,180,116\nWL 7\nDT 214,4,22528\nMvaa 116,184,124\n"
"buffer_load_dwordx4 v[224:227], v11, s[36:39], s4 offen\nMvaa 120,144,64\nDT 216,0,23552\nDT 218,4,23552\n"
"Mvaa 120,148,72\nDT 220,0,24576\nMvaa 120,152,80\nDT 222,4,24576\nCV 20,116,117\nMvaa 120,156,88\n"
"LD 12,48,84,2048\nMvaa 120,160,96\ns_add_u32 s84, s84, s85\nDT 224,0,25600\nDT 226,4,25600\nMvaa 120,164,104\n"
"DT 228,1,18432\nCV 21,118,119\nMvaa 120,168,112\nDT 230,5,18432\nMvaa 120,192,120\n"
"buffer_load_dwordx4 v[160:163], v13, s[40:43], s74 offen offset:0 sc1\nMvaa 124,144,68\nDT 232,1,19456\n"
"DT 234,5,19456\nMvaa 124,148,76\nDT 172,1,20480\nMvaa 124,152,84\nDT 174,5,20480\nCV 30,120,121\n"
"Mvaa 124,156,92\nLD 12,48,84,3072\nMvaa 124,160,100\ns_mov_b32 s84, 148352\nDT 176,1,21504\nDT 178,5,21504\n"
"Mvaa 124,164,108\ns_addk_i32 m0, 4096\nDT 180,1,22528\nCV 31,122,123\nMvaa 124,168,116\nWL 7\nDT 182,5,22528\n"
"Mvaa 124,192,124\nbuffer_store_dwordx4 v[28:31], v13, s[60:63], s87 offen sc1\nMvavz 128,196,32\n"
"DT 184,1,23552\nDT 186,5,23552\nMvav 128,200,40\ns_mov_b32 s87, 786432\nDT 144,1,24576\nMvav 128,204,48\n"
"DT 146,5,24576\nCV 22,124,125\nMvav 128,208,56\nLD 12,48,84,0\nMvav 128,212,64\ns_add_u32 s84, s84, s85\n"
"DT 148,1,25600\nDT 150,5,25600\nMvav 128,216,72\nDT 152,2,18432\nCV 23,126,127\nMvav 128,220,80\n"
"DT 154,6,18432\nMvav 128,224,88\nLD 12,48,84,1024\nMvavz 132,196,36\ns_add_u32 s84, s84, s85\nDT 156,2,19456\n"
"DT 158,6,19456\nMvav 132,200,44\nDT 160,2,20480\nMvav 132,204,52\nDT 162,6,20480\nMvav 132,208,60\n"
"buffer_store_dwordx4 v[20:23], v13, s[60:63], s88 offen sc1\nMvav 132,212,68\nDT 164,2,21504\nDT 166,6,21504\n"
"Mvav 132,216,76\ns_mov_b32 s88, 819200\nDT 168,2,22528\nMvav 132,220,84\nWL 7\nDT 170,6,22528\nMvav 132,224,92\n"
"LD 12,48,84,2048\nMvavz 136,228,32\ns_add_u32 s84, s84, s85\nDT 192,2,23552\nDT 194,6,23552\nMvav 136,232,40\n"
"DT 196,2,24576\nMvav 136,172,48\nDT 198,6,24576\nMvav 136,176,56\n"
"buffer_load_dwordx4 v[164:167], v13, s[40:43], s74 offen offset:1024 sc1\nMvav 136,180,64\nDT 200,2,25600\n"
"DT 202,6,25600\nMvav 136,184,72\nDT 204,3,18432\nMvav 136,144,80\nDT 206,7,18432\nMvav 136,148,88\n"
"LD 12,48,84,3072\nMvavz 140,228,36\ns_mov_b32 s84, 295808\nDT 208,3,19456\nDT 210,7,19456\nMvav 140,232,44\n"
"s_addk_i32 m0, 5120\nDT 212,3,20480\nMvav 140,172,52\nDT 214,7,20480\nMvav 140,176,60\n"
"buffer_load_dwordx4 v[168:171], v13, s[40:43], s74 offen offset:2048 sc1\nMvav 140,180,68\nDT 216,3,21504\n"
"DT 218,7,21504\nMvav 140,184,76\nDT 220,3,22528\nMvav 140,144,84\nWL 7\nDT 222,7,22528\nMvav 140,148,92\n"
"DT 224,3,23552\nMvavz 144,152,32\nDT 226,7,23552\nMvav 144,156,40\nDT 228,3,24576\nMvav 144,160,48\n"
"DT 230,7,24576\nMvav 144,164,56\ns_waitcnt vmcnt(16)\nDT 232,3,25600\nMvav 144,168,64\ns_barrier\n"
"DT 234,7,25600\nMvav 144,192,72\nLD 12,48,84,0\nMvav 144,196,80\ns_add_u32 s84, s84, s85\nDT 172,0,31744\n"
"DT 174,4,31744\nMvav 144,200,88\nDT 176,0,32768\nMvavz 148,152,36\nDT 178,4,32768\nMvav 148,156,44\n"
"DT 180,0,33792\nMvav 148,160,52\nDT 182,4,33792\nMvav 148,164,60\n"
"buffer_load_dwordx4 v[172:175], v13, s[40:43], s74 offen offset:3072 sc1\nMvav 148,168,68\n"
"s_add_u32 s74, s82, 16384\nDT 184,0,34816\nDT 186,4,34816\nMvav 148,192,76\nDT 144,0,35840\nMvav 148,196,84\n"
"WL 6\nDT 146,4,35840\nMvav 148,200,92\nLD 12,48,84,1024\nMvavz 152,204,32\ns_add_u32 s84, s84, s85\n"
"DT 148,0,36864\nDT 150,4,36864\nMvav 152,208,40\nDT 152,0,37888\nMvav 152,212,48\nDT 154,4,37888\n"
"Mvav 152,216,56\nLD 12,48,84,2048\nMvav 152,220,64\ns_add_u32 s84, s84, s85\nDT 156,0,38912\nDT 158,4,38912\n"
"Mvav 152,224,72\nDT 160,1,31744\nMvav 152,228,80\nDT 162,5,31744\nMvav 152,232,88\nDT 164,1,32768\n"
"Mvavz 156,204,36\nDT 166,5,32768\nMvav 156,208,44\nDT 168,1,33792\nMvav 156,212,52\nLD 12,48,84,3072\n"
"Mvav 156,216,60\ns_mov_b32 s84, 443264\nDT 170,5,33792\nDT 192,1,34816\nMvav 156,220,68\ns_addk_i32 m0, 4096\n"
"DT 194,5,34816\nMvav 156,224,76\nDT 196,1,35840\nMvav 156,228,84\nWL 6\n"
"buffer_load_dwordx4 v[176:179], v13, s[40:43], s75 offen offset:0 sc1\nMvav 156,232,92\nDT 198,5,35840\n"
"DT 200,1,36864\nMvaa 128,172,64\nDT 202,5,36864\nMvaa 128,176,72\nDT 204,1,37888\nMvaa 128,180,80\n"
"LD 12,48,84,0\nMvaa 128,184,88\ns_add_u32 s84, s84, s85\nDT 206,5,37888\nDT 208,1,38912\nMvaa 128,144,96\n"
"DT 210,5,38912\nMvaa 128,148,104\nDT 212,2,31744\nMvaa 128,152,112\nDT 214,6,31744\nMvaa 128,156,120\n"
"DT 216,2,32768\nMvaa 132,172,68\nLD 12,48,84,1024\nMvaa 132,176,76\ns_add_u32 s84, s84, s85\nDT 218,6,32768\n"
"DT 220,2,33792\nMvaa 132,180,84\nDT 222,6,33792\nCV 24,128,129\nMvaa 132,184,92\nDT 224,2,34816\nCV 25,130,131\n"
"Mvaa 132,144,100\nDT 226,6,34816\nMvaa 132,148,108\nDT 228,2,35840\nMvaa 132,152,116\nWL 6\nDT 230,6,35840\n"
"Mvaa 132,156,124\nLD 12,48,84,2048\nMvaa 136,160,64\ns_add_u32 s84, s84, s85\nDT 232,2,36864\nDT 234,6,36864\n"
"Mvaa 136,164,72\nDT 172,2,37888\nMvaa 136,168,80\nDT 174,6,37888\nCV 28,132,133\nMvaa 136,192,88\n"
"s_waitcnt vmcnt(13)\ns_bfm_b64 exec, 1, 36\nglobal_atomic_add v13, v9, s[98:99]\ns_mov_b64 exec, -1\n"
"Mvaa 136,196,96\nDT 176,2,38912\nDT 178,6,38912\nMvaa 136,200,104\nDT 180,3,31744\nCV 29,134,135\n"
"Mvaa 136,204,112\nDT 182,7,31744\nMvaa 136,208,120\nLD 12,48,84,3072\nMvaa 140,160,68\ns_movk_i32 s84, 1024\n"
"DT 184,3,32768\nDT 186,7,32768\nMvaa 140,164,76\ns_addk_i32 m0, 5120\nDT 144,3,33792\nCV 26,136,137\n"
"Mvaa 140,168,84\nDT 146,7,33792\nCV 27,138,139\nMvaa 140,192,92\n"
"buffer_store_dwordx4 v[24:27], v13, s[60:63], s87 offen sc1\nMvaa 140,196,100\nDT 148,3,34816\nDT 150,7,34816\n"
"Mvaa 140,200,108\ns_mov_b32 s87, 851968\nDT 152,3,35840\nMvaa 140,204,116\nWL 7\nDT 154,7,35840\n"
"Mvaa 140,208,124\nbuffer_load_dwordx4 v[180:183], v13, s[40:43], s75 offen offset:1024 sc1\nMvaa 144,212,64\n"
"DT 156,3,36864\nDT 158,7,36864\nMvaa 144,216,72\ns_nop 0\n"
"Lw18_%=:\ns_cbranch_execz Lrl00_%=\nDT 160,3,37888\nMvaa 144,220,80\nDT 162,7,37888\nCV 30,140,141\n"
"Mvaa 144,224,88\ns_waitcnt vmcnt(14)\nDT 164,3,38912\nCV 31,142,143\nMvaa 144,228,96\ns_barrier\n"
"buffer_load_dwordx4 v[184:187], v13, s[40:43], s75 offen offset:2048 sc1\nMvaa 144,232,104\nDT 166,7,38912\n"
"DT 168,0,0\nMvaa 144,172,112\nDT 170,4,0\nMvaa 144,176,120\nDT 192,0,1024\nMvaa 148,212,68\nLD 12,48,84,0\n"
"Mvaa 148,216,76\ns_add_u32 s84, s84, s85\nDT 194,4,1024\nDT 196,0,2048\nMvaa 148,220,84\nDT 198,4,2048\n"
"CV 20,144,145\nMvaa 148,224,92\nDT 200,0,3072\nCV 21,146,147\nMvaa 148,228,100\nLD 12,48,84,1024\n"
"Mvaa 148,232,108\ns_add_u32 s84, s84, s85\nDT 202,4,3072\nDT 204,0,4096\nMvaa 148,172,116\nWL 6\nDT 206,4,4096\n"
"Mvaa 148,176,124\nDT 208,0,5120\nMvaa 152,180,64\nbuffer_store_dwordx4 v[28:31], v13, s[60:63], s88 offen sc1\n"
"Mvaa 152,184,72\nDT 210,4,5120\nDT 212,0,6144\nMvaa 152,144,80\ns_mov_b32 s88, 884736\nDT 214,4,6144\n"
"CV 24,148,149\nMvaa 152,148,88\nDT 216,0,7168\nCV 25,150,151\nMvaa 152,152,96\nLD 12,48,84,2048\n"
"Mvaa 152,156,104\ns_add_u32 s84, s84, s85\nDT 218,4,7168\nDT 220,1,0\nMvaa 152,160,112\nDT 222,5,0\n"
"Mvaa 152,164,120\nDT 224,1,1024\nMvaa 156,180,68\n"
"buffer_load_dwordx4 v[188:191], v13, s[40:43], s75 offen offset:3072 sc1\nMvaa 156,184,76\n"
"s_add_u32 s75, s82, 20480\nDT 226,5,1024\nDT 228,1,2048\nMvaa 156,144,84\nDT 230,5,2048\nCV 22,152,153\n"
"Mvaa 156,148,92\nDT 232,1,3072\nCV 23,154,155\nMvaa 156,152,100\nLD 12,48,84,3072\nMvaa 156,156,108\n"
"s_mov_b32 s84, 148480\nDT 234,5,3072\nDT 172,1,4096\nMvaa 156,160,116\nWL 6\nDT 174,5,4096\nMvaa 156,164,124\n"
"s_addk_i32 m0, 4096\nDT 176,1,5120\nMvavz 96,168,32\n"
"buffer_store_dwordx4 v[20:23], v13, s[60:63], s87 offen sc1\nMvav 96,192,40\nDT 178,5,5120\nDT 180,1,6144\n"
"Mvav 96,196,48\ns_mov_b32 s87, 917504\nDT 182,5,6144\nCV 26,156,157\nMvav 96,200,56\nDT 184,1,7168\n"
"CV 27,158,159\nMvav 96,204,64\nLD 12,48,84,0\nMvav 96,208,72\ns_add_u32 s84, s84, s85\nDT 186,5,7168\n"
"DT 144,2,0\nMvav 96,212,80\nDT 146,6,0\nMvav 96,216,88\nDT 148,2,1024\nMvavz 100,168,36\nLD 12,48,84,1024\n"
"Mvav 100,192,44\ns_add_u32 s84, s84, s85\nDT 150,6,1024\nDT 152,2,2048\nMvav 100,196,52\nDT 154,6,2048\n"
"Mvav 100,200,60\nDT 156,2,3072\nMvav 100,204,68\n"
"buffer_load_dwordx4 v[192:195], v13, s[40:43], s74 offen offset:0 sc1\nMvav 100,208,76\nDT 158,6,3072\n"
"DT 160,2,4096\nMvav 100,212,84\nWL 6\nDT 162,6,4096\nMvav 100,216,92\nDT 164,2,5120\nMvavz 104,220,32\n"
"LD 12,48,84,2048\nMvav 104,224,40\ns_add_u32 s84, s84, s85\nDT 166,6,5120\nDT 168,2,6144\nMvav 104,228,48\n"
"DT 170,6,6144\nMvav 104,232,56\nDT 192,2,7168\nMvav 104,172,64\n"
"buffer_store_dwordx4 v[24:27], v13, s[60:63], s88 offen sc1\nMvav 104,176,72\nDT 194,6,7168\nDT 196,3,0\n"
"Mvav 104,180,80\ns_mov_b32 s88, 950272\nDT 198,7,0\nMvav 104,184,88\nDT 200,3,1024\nMvavz 108,220,36\n"
"LD 12,48,84,3072\nMvav 108,224,44\ns_mov_b32 s84, 295936\nDT 202,7,1024\nDT 204,3,2048\nMvav 108,228,52\n"
"s_addk_i32 m0, 9216\nDT 206,7,2048\nMvav 108,232,60\nDT 208,3,3072\nMvav 108,172,68\n"
"buffer_load_dwordx4 v[196:199], v13, s[40:43], s74 offen offset:1024 sc1\nMvav 108,176,76\nDT 210,7,3072\n"
"DT 212,3,4096\nMvav 108,180,84\nWL 6\nDT 214,7,4096\nMvav 108,184,92\nDT 216,3,5120\nMvavz 112,144,32\n"
"DT 218,7,5120\nMvav 112,148,40\nDT 220,3,6144\nMvav 112,152,48\nDT 222,7,6144\nMvav 112,156,56\n"
"s_waitcnt vmcnt(17)\nDT 224,3,7168\nMvav 112,160,64\ns_barrier\nDT 226,7,7168\nMvav 112,164,72\nLD 12,48,84,0\n"
"Mvav 112,168,80\ns_add_u32 s84, s84, s85\nDT 228,0,9216\nDT 230,4,9216\nMvav 112,192,88\nDT 232,0,10240\n"
"Mvavz 116,144,36\nDT 234,4,10240\nMvav 116,148,44\n"
"buffer_load_dwordx4 v[200:203], v13, s[40:43], s74 offen offset:2048 sc1\nMvav 116,152,52\nDT 172,0,11264\n"
"DT 174,4,11264\nMvav 116,156,60\nDT 176,0,12288\nMvav 116,160,68\nDT 178,4,12288\nMvav 116,164,76\n"
"LD 12,48,84,1024\nMvav 116,168,84\nWL 5\nDT 180,0,13312\nDT 182,4,13312\nMvav 116,192,92\n"
"s_add_u32 s84, s84, s85\nDT 184,0,14336\nMvavz 120,196,32\nDT 186,4,14336\nMvav 120,200,40\nDT 144,0,15360\n"
"Mvav 120,204,48\nDT 146,4,15360\nMvav 120,208,56\nLD 12,48,84,2048\nMvav 120,212,64\ns_add_u32 s84, s84, s85\n"
"DT 148,0,16384\nDT 150,4,16384\nMvav 120,216,72\nDT 152,1,9216\nMvav 120,220,80\nDT 154,5,9216\n"
"Mvav 120,224,88\nDT 156,1,10240\nMvavz 124,196,36\nDT 158,5,10240\nMvav 124,200,44\nDT 160,1,11264\n"
"Mvav 124,204,52\nLD 12,48,84,3072\nMvav 124,208,60\ns_mov_b32 s84, 443392\nDT 162,5,11264\nDT 164,1,12288\n"
"Mvav 124,212,68\ns_addk_i32 m0, 4096\nDT 166,5,12288\nMvav 124,216,76\nDT 168,1,13312\nMvav 124,220,84\nWL 6\n"
"buffer_load_dwordx4 v[204:207], v13, s[40:43], s74 offen offset:3072 sc1\nMvav 124,224,92\nDT 170,5,13312\n"
"DT 192,1,14336\nMvaa 96,228,64\nDT 194,5,14336\nMvaa 96,232,72\nDT 196,1,15360\nMvaa 96,172,80\nLD 12,48,84,0\n"
"Mvaa 96,176,88\ns_add_u32 s84, s84, s85\nDT 198,5,15360\nDT 200,1,16384\nMvaa 96,180,96\nDT 202,5,16384\n"
"Mvaa 96,184,104\nDT 204,2,9216\nMvaa 96,144,112\nDT 206,6,9216\nMvaa 96,148,120\nDT 208,2,10240\n"
"Mvaa 100,228,68\nLD 12,48,84,1024\nMvaa 100,232,76\ns_add_u32 s84, s84, s85\nDT 210,6,10240\nDT 212,2,11264\n"
"Mvaa 100,172,84\nDT 214,6,11264\nCV 28,96,97\nMvaa 100,176,92\nDT 216,2,12288\nCV 29,98,99\nMvaa 100,180,100\n"
"DT 218,6,12288\nMvaa 100,184,108\nbuffer_load_dwordx4 v[208:211], v13, s[40:43], s75 offen offset:0 sc1\n"
"Mvaa 100,144,116\nWL 5\nDT 220,2,13312\nDT 222,6,13312\nMvaa 100,148,124\nDT 224,2,14336\nMvaa 104,152,64\n"
"DT 226,6,14336\nMvaa 104,156,72\nLD 12,48,84,2048\nMvaa 104,160,80\ns_add_u32 s84, s84, s85\nDT 228,2,15360\n"
"DT 230,6,15360\nMvaa 104,164,88\nDT 232,2,16384\nCV 20,100,101\nMvaa 104,168,96\nDT 234,6,16384\nCV 21,102,103\n"
"Mvaa 104,192,104\nDT 172,3,9216\nMvaa 104,196,112\nLD 12,48,84,3072\nMvaa 104,200,120\nDT 174,7,9216\n"
"DT 176,3,10240\nMvaa 108,152,68\nDT 178,7,10240\nMvaa 108,156,76\nDT 180,3,11264\nMvaa 108,160,84\n"
"s_waitcnt vmcnt(13)\ns_bfm_b64 exec, 1, 40\nglobal_atomic_add v13, v9, s[98:99]\ns_mov_b64 exec, -1\n"
"Mvaa 108,164,92\nDT 182,7,11264\nDT 184,3,12288\nMvaa 108,168,100\nDT 186,7,12288\nCV 30,104,105\n"
"Mvaa 108,192,108\nDT 144,3,13312\nCV 31,106,107\nMvaa 108,196,116\nWL 6\n"
"buffer_store_dwordx4 v[28:31], v13, s[60:63], s87 offen sc1\nMvaa 108,200,124\nDT 146,7,13312\nDT 148,3,14336\n"
"Mvaa 112,204,64\ns_mov_b32 s87, 983040\nDT 150,7,14336\nMvaa 112,208,72\nDT 152,3,15360\nMvaa 112,212,80\n"
"buffer_load_dwordx4 v[212:215], v13, s[40:43], s75 offen offset:1024 sc1\nMvaa 112,216,88\ns_waitcnt vmcnt(15)\n"
"DT 154,7,15360\nDT 156,3,16384\nMvaa 112,220,96\ns_barrier\nDT 158,7,16384\nCV 22,108,109\nMvaa 112,224,104\n"
"DT 160,0,18432\nCV 23,110,111\nMvaa 112,228,112\nbuffer_store_dwordx4 v[20:23], v13, s[60:63], s88 offen sc1\n"
"Mvaa 112,232,120\nDT 162,4,18432\nDT 164,0,19456\nMvaa 116,204,68\ns_mov_b32 s88, 1015808\nDT 166,4,19456\n"
"Mvaa 116,208,76\nDT 168,0,20480\nMvaa 116,212,84\nDT 170,4,20480\nCV 24,112,113\nMvaa 116,216,92\n"
"DT 192,0,21504\nCV 25,114,115\nMvaa 116,220,100\nDT 194,4,21504\nMvaa 116,224,108\nDT 196,0,22528\n"
"Mvaa 116,228,116\nWL 6\nDT 198,4,22528\nMvaa 116,232,124\nDT 200,0,23552\nMvaa 120,172,64\nDT 202,4,23552\n"
"Mvaa 120,176,72\nbuffer_load_dwordx4 v[216:219], v13, s[40:43], s75 offen offset:2048 sc1\nMvaa 120,180,80\n"
"DT 204,0,24576\nDT 206,4,24576\nMvaa 120,184,88\nDT 208,0,25600\nCV 28,116,117\nMvaa 120,144,96\n"
"DT 210,4,25600\nCV 29,118,119\nMvaa 120,148,104\nDT 212,1,18432\nMvaa 120,152,112\nDT 214,5,18432\n"
"Mvaa 120,156,120\nDT 216,1,19456\nMvaa 124,172,68\nDT 218,5,19456\nMvaa 124,176,76\nDT 220,1,20480\n"
"Mvaa 124,180,84\nDT 222,5,20480\nMvaa 124,184,92\nDT 224,1,21504\nCV 26,120,121\nMvaa 124,144,100\n"
"DT 226,5,21504\nCV 27,122,123\nMvaa 124,148,108\nbuffer_store_dwordx4 v[24:27], v13, s[60:63], s87 offen sc1\n"
"Mvaa 124,152,116\nWL 5\nDT 228,1,22528\nDT 230,5,22528\nMvaa 124,156,124\ns_mov_b32 s87, 1048576\n"
"DT 232,1,23552\nMvavz 128,160,32\nDT 234,5,23552\nMvav 128,164,40\nDT 172,1,24576\nMvav 128,168,48\n"
"DT 174,5,24576\nCV 30,124,125\nMvav 128,192,56\n"
"buffer_load_dwordx4 v[220:223], v13, s[40:43], s75 offen offset:3072 sc1\nMvav 128,196,64\nDT 176,1,25600\n"
"DT 178,5,25600\nMvav 128,200,72\nDT 180,2,18432\nCV 31,126,127\nMvav 128,204,80\nDT 182,6,18432\n"
"Mvav 128,208,88\nbuffer_store_dwordx4 v[28:31], v13, s[60:63], s88 offen sc1\nMvavz 132,160,36\nDT 184,2,19456\n"
"DT 186,6,19456\nMvav 132,164,44\ns_mov_b32 s88, 1081344\nDT 144,2,20480\nMvav 132,168,52\nDT 146,6,20480\n"
"Mvav 132,192,60\nDT 148,2,21504\nMvav 132,196,68\nDT 150,6,21504\nMvav 132,200,76\nDT 152,2,22528\n"
"Mvav 132,204,84\nWL 7\nDT 154,6,22528\nMvav 132,208,92\nDT 156,2,23552\nMvavz 136,212,32\nDT 158,6,23552\n"
"Mvav 136,216,40\nDT 160,2,24576\nMvav 136,220,48\nDT 162,6,24576\nMvav 136,224,56\nDT 164,2,25600\n"
"Mvav 136,228,64\nDT 166,6,25600\nMvav 136,232,72\nDT 168,3,18432\nMvav 136,172,80\nDT 170,7,18432\n"
"Mvav 136,176,88\nDT 192,3,19456\nMvavz 140,212,36\nDT 194,7,19456\nMvav 140,216,44\nDT 196,3,20480\n"
"Mvav 140,220,52\nDT 198,7,20480\nMvav 140,224,60\nDT 200,3,21504\nMvav 140,228,68\nDT 202,7,21504\n"
"Mvav 140,232,76\nDT 204,3,22528\nMvav 140,172,84\nWL 6\nDT 206,7,22528\nMvav 140,176,92\nDT 208,3,23552\n"
"Mvavz 144,180,32\nDT 210,7,23552\nMvav 144,184,40\nDT 212,3,24576\nMvav 144,144,48\nDT 214,7,24576\n"
"Mvav 144,148,56\ns_waitcnt vmcnt(8)\nDT 216,3,25600\nMvav 144,152,64\ns_barrier\nDT 218,7,25600\n"
"Mvav 144,156,72\nDT 220,0,31744\nMvav 144,160,80\nDT 222,4,31744\nMvav 144,164,88\nDT 224,0,32768\n"
"Mvavz 148,180,36\nDT 226,4,32768\nMvav 148,184,44\nDT 228,0,33792\nMvav 148,144,52\nDT 230,4,33792\n"
"Mvav 148,148,60\nDT 232,0,34816\nMvav 148,152,68\nDT 234,4,34816\nMvav 148,156,76\nDT 172,0,35840\n"
"Mvav 148,160,84\nWL 6\nDT 174,4,35840\nMvav 148,164,92\nDT 176,0,36864\nMvavz 152,168,32\nDT 178,4,36864\n"
"Mvav 152,192,40\nDT 180,0,37888\nMvav 152,196,48\nDT 182,4,37888\nMvav 152,200,56\nDT 184,0,38912\n"
"Lw19_%=:\ns_cbranch_execz Lrl00_%=\nMvav 152,204,64\nDT 186,4,38912\nMvav 152,208,72\nDT 144,1,31744\n"
"Mvav 152,212,80\nDT 146,5,31744\nMvav 152,216,88\nDT 148,1,32768\nMvavz 156,168,36\nDT 150,5,32768\n"
"Mvav 156,192,44\nDT 152,1,33792\nMvav 156,196,52\nDT 154,5,33792\nMvav 156,200,60\nDT 156,1,34816\n"
"Mvav 156,204,68\nDT 158,5,34816\nMvav 156,208,76\nDT 160,1,35840\nMvav 156,212,84\nWL 6\nDT 162,5,35840\n"
"Mvav 156,216,92\nDT 164,1,36864\nMvaa 128,220,64\nDT 166,5,36864\nMvaa 128,224,72\nDT 168,1,37888\n"
"Mvaa 128,228,80\nDT 170,5,37888\nMvaa 128,232,88\nDT 192,1,38912\nMvaa 128,172,96\nDT 194,5,38912\n"
"Mvaa 128,176,104\nDT 196,2,31744\nMvaa 128,180,112\nDT 198,6,31744\nMvaa 128,184,120\nDT 200,2,32768\n"
"Mvaa 132,220,68\nDT 202,6,32768\nMvaa 132,224,76\nDT 204,2,33792\nMvaa 132,228,84\nDT 206,6,33792\n"
"Mvaa 132,232,92\nDT 208,2,34816\nCV 20,128,129\nMvaa 132,172,100\nDT 210,6,34816\nCV 21,130,131\n"
"Mvaa 132,176,108\ns_waitcnt vmcnt(0)\ns_bfm_b64 exec, 1, 44\nglobal_atomic_add v13, v9, s[98:99]\n"
"s_mov_b64 exec, -1\nMvaa 132,180,116\nWL 5\nDT 212,2,35840\nDT 214,6,35840\nMvaa 132,184,124\nDT 216,2,36864\n"
"Mvaa 136,144,64\nDT 218,6,36864\nMvaa 136,148,72\nDT 220,2,37888\nMvaa 136,152,80\nDT 222,6,37888\n"
"Mvaa 136,156,88\nDT 224,2,38912\nCV 24,132,133\nMvaa 136,160,96\nDT 226,6,38912\nCV 25,134,135\n"
"Mvaa 136,164,104\nDT 228,3,31744\nMvaa 136,168,112\nDT 230,7,31744\nMvaa 136,192,120\nDT 232,3,32768\n"
"Mvaa 140,144,68\nDT 234,7,32768\nMvaa 140,148,76\nDT 172,3,33792\nMvaa 140,152,84\nDT 174,7,33792\n"
"Mvaa 140,156,92\nDT 176,3,34816\nCV 22,136,137\nMvaa 140,160,100\nDT 178,7,34816\nCV 23,138,139\n"
"Mvaa 140,164,108\nbuffer_store_dwordx4 v[20:23], v13, s[60:63], s87 offen sc1\nMvaa 140,168,116\nWL 5\n"
"DT 180,3,35840\nDT 182,7,35840\nMvaa 140,192,124\ns_mov_b32 s87, 1114112\nDT 184,3,36864\nMvaa 144,196,64\n"
"DT 186,7,36864\nMvaa 144,200,72\nDT 144,3,37888\nMvaa 144,204,80\nDT 146,7,37888\nCV 26,140,141\n"
"Mvaa 144,208,88\nDT 148,3,38912\nCV 27,142,143\nMvaa 144,212,96\n"
"buffer_store_dwordx4 v[24:27], v13, s[60:63], s88 offen sc1\nMvaa 144,216,104\nDT 150,7,38912\n"
"Mvaa 144,220,112\ns_mov_b32 s88, 1146880\nMvaa 144,224,120\nMvaa 148,196,68\nMvaa 148,200,76\nMvaa 148,204,84\n"
"Mvaa 148,208,92\nMvaa 148,212,100\nMvaa 148,216,108\nMvaa 148,220,116\nWL 0\nCV 28,144,145\nCV 29,146,147\n"
"Mvaa 148,224,124\ns_bfm_b64 exec, 1, 63\nglobal_atomic_add v16, v13, v8, s[98:99] sc0\ns_mov_b64 exec, -1\n"
"Mvaa 152,228,64\nMvaa 152,232,72\nMvaa 152,172,80\nMvaa 152,176,88\ns_mov_b32 exec_lo, 0x11110000\n"
"s_mov_b32 exec_hi, 0x11111\nglobal_load_dword v14, v13, s[98:99] sc1\ns_mov_b64 exec, -1\nMvaa 152,180,96\n"
"Mvaa 152,184,104\nCV 20,148,149\nCV 21,150,151\nMvaa 152,144,112\nMvaa 152,148,120\nMvaa 156,228,68\n"
"Mvaa 156,232,76\nMvaa 156,172,84\nMvaa 156,176,92\nMvaa 156,180,100\nMvaa 156,184,108\nMvaa 156,144,116\n"
"Mvaa 156,148,124\nCV 30,152,153\nCV 31,154,155\nbuffer_store_dwordx4 v[28:31], v13, s[60:63], s87 offen sc1\n"
"s_nop 4\nCV 22,156,157\nCV 23,158,159\nbuffer_store_dwordx4 v[20:23], v13, s[60:63], s88 offen sc1\n"
"s_waitcnt vmcnt(2)\ns_mov_b32 s84, s42\ns_cmp_eq_u32 s34, s34\ns_cselect_b32 s85, -1, 0\n"
"s_and_b32 s3, s84, s85\n"
"Lredx0_%=:\nWL 0\nv_sub_u32 v16, v16, v10\nv_sub_u32 v14, v14, v10\ns_mov_b32 s42, 1179648\n"
"v_cmp_eq_u32 s[6:7], 16, v14\ns_cmp_lg_u32 s3, 0\ns_cbranch_scc0 Lrslowx0_%=\n"
"Lrhx0_%=:\nv_readlane_b32 s91, v16, 63\ns_lshr_b32 s92, s81, 2\ns_add_u32 s92, s92, 32\n"
"s_mul_i32 s91, s91, s35\ns_cmp_lt_u32 s91, 16\ns_cselect_b32 s74, 1, 0\ns_min_u32 s91, s91, 15\n"
"s_add_u32 s91, s91, 16\ns_cmp_eq_u32 s35, 0\ns_cselect_b32 s91, s92, s91\ns_lshl_b32 s82, s91, 15\n"
"s_and_b32 s2, s91, 1\ns_mul_i32 s2, s2, 18400\ns_lshl_b32 s92, s91, 5\ns_add_u32 s2, s2, s92\n"
"s_and_b32 s91, s91, 60\ns_add_u32 s91, s91, 16\ns_cmp_lg_u32 s3, 0\ns_cbranch_scc0 Lrslow2x0_%=\n"
"Lrmx0_%=:\nMvavz 104,240,164\nv_lshlrev_b32 v120, 16, v224\nv_and_b32 v121, 0xffff0000, v224\n"
"Mvavz 108,244,164\nv_lshlrev_b32 v122, 16, v225\nv_and_b32 v123, 0xffff0000, v225\nMvav 104,240,172\n"
"v_lshlrev_b32 v124, 16, v226\nv_and_b32 v125, 0xffff0000, v226\nMvav 108,244,172\nv_lshlrev_b32 v126, 16, v227\n"
"v_and_b32 v127, 0xffff0000, v227\nMvav 104,240,180\nMvav 108,244,180\nMvav 104,240,188\nMvav 108,244,188\n"
"Mvav 104,240,196\nMvav 108,244,196\nMvav 104,240,204\nMvav 108,244,204\nMvav 104,240,212\nMvav 108,244,212\n"
"s_cmp_lg_u32 s3, 0\ns_cbranch_scc0 Lrnx0_%=\ns_waitcnt vmcnt(0)\ns_bfm_b64 exec, 1, 48\n"
"global_atomic_add v13, v9, s[98:99]\ns_mov_b64 exec, -1\ns_cmp_eq_u32 s74, 0\ns_cbranch_scc1 Lre0x0_%=\n"
"buffer_load_dwordx4 v[232:235], v11, s[36:39], s2 offen\ns_bitcmp1_b64 s[6:7], s91\ns_cbranch_scc0 Lre0x0_%=\n"
"s_add_u32 s92, s82, 0\nbuffer_load_dwordx4 a[68:71], v13, s[40:43], s92 offen offset:1024 sc1\n"
"buffer_load_dwordx4 a[76:79], v13, s[40:43], s92 offen offset:3072 sc1\ns_add_u32 s92, s82, 4096\n"
"buffer_load_dwordx4 a[84:87], v13, s[40:43], s92 offen offset:1024 sc1\n"
"buffer_load_dwordx4 a[92:95], v13, s[40:43], s92 offen offset:3072 sc1\ns_add_u32 s92, s82, 16384\n"
"buffer_load_dwordx4 a[100:103], v13, s[40:43], s92 offen offset:1024 sc1\n"
"buffer_load_dwordx4 a[108:111], v13, s[40:43], s92 offen offset:3072 sc1\ns_add_u32 s92, s82, 20480\n"
"buffer_load_dwordx4 a[116:119], v13, s[40:43], s92 offen offset:1024 sc1\n"
"buffer_load_dwordx4 a[124:127], v13, s[40:43], s92 offen offset:3072 sc1\ns_add_u32 s92, s82, 0\n"
"buffer_load_dwordx4 a[64:67], v13, s[40:43], s92 offen offset:0 sc1\n"
"buffer_load_dwordx4 a[72:75], v13, s[40:43], s92 offen offset:2048 sc1\ns_add_u32 s92, s82, 4096\n"
"buffer_load_dwordx4 a[80:83], v13, s[40:43], s92 offen offset:0 sc1\n"
"buffer_load_dwordx4 a[88:91], v13, s[40:43], s92 offen offset:2048 sc1\ns_add_u32 s92, s82, 16384\n"
"buffer_load_dwordx4 a[96:99], v13, s[40:43], s92 offen offset:0 sc1\n"
"buffer_load_dwordx4 a[104:107], v13, s[40:43], s92 offen offset:2048 sc1\ns_add_u32 s92, s82, 20480\n"
"buffer_load_dwordx4 a[112:115], v13, s[40:43], s92 offen offset:0 sc1\n"
"buffer_load_dwordx4 a[120:123], v13, s[40:43], s92 offen offset:2048 sc1\n"
"Lre0x0_%=:\ns_nop 0\n"
"Lrnx0_%=:\nMvav 104,240,220\nMvav 108,244,220\nMvavz 96,240,160\ns_nop 1\nMvavz 100,244,160\ns_nop 1\n"
"Mvav 96,240,168\nMvav 100,244,168\nMvav 96,240,176\nEX 112,104\nEX 113,105\nEX 114,106\nEX 115,107\n"
"Mvav 100,244,176\nEX 116,108\nEX 117,109\nEX 118,110\nEX 119,111\nMvav 96,240,184\nv_mul_f32 v104, s89, v104\n"
"v_mul_f32 v105, s89, v105\nv_mul_f32 v106, s89, v106\nv_mul_f32 v107, s89, v107\nMvav 100,244,184\n"
"v_mul_f32 v108, s89, v108\nv_mul_f32 v109, s89, v109\nv_mul_f32 v110, s89, v110\nv_mul_f32 v111, s89, v111\n"
"Mvav 96,240,192\nv_add_f32 v112, 1.0, v112\nv_add_f32 v113, 1.0, v113\nv_add_f32 v114, 1.0, v114\n"
"v_add_f32 v115, 1.0, v115\nMvav 100,244,192\nv_add_f32 v116, 1.0, v116\nv_add_f32 v117, 1.0, v117\n"
"v_add_f32 v118, 1.0, v118\nv_add_f32 v119, 1.0, v119\nMvav 96,240,200\nRC 112,112\nRC 113,113\nRC 114,114\n"
"RC 115,115\nMvav 100,244,200\nRC 116,116\nRC 117,117\nRC 118,118\nRC 119,119\nMvav 96,240,208\nML 104,104,112\n"
"ML 105,105,113\nML 106,106,114\nML 107,107,115\nMvav 100,244,208\nML 108,108,116\nML 109,109,117\n"
"ML 110,110,118\nML 111,111,119\nMvav 96,240,216\nMvav 100,244,216\ns_nop 7\n"
"v_pk_fma_f32 v[96:97], v[96:97], v[104:105], v[120:121]\n"
"v_pk_fma_f32 v[98:99], v[98:99], v[106:107], v[122:123]\n"
"v_pk_fma_f32 v[100:101], v[100:101], v[108:109], v[124:125]\n"
"v_pk_fma_f32 v[102:103], v[102:103], v[110:111], v[126:127]\nCV 128,96,97\nCV 129,98,99\nCV 130,100,101\n"
"CV 131,102,103\ns_cmp_eq_u32 s74, 0\ns_cbranch_scc1 Lrsx0_%=\ns_bitcmp1_b64 s[6:7], s91\n"
"s_cbranch_scc1 Lrsx0_%=\ns_mov_b32 s90, 262144\ns_bfm_b64 exec, 1, s91\n"
"Lrwbx0_%=:\nglobal_load_dword v14, v13, s[98:99] sc1\ns_waitcnt vmcnt(0)\nv_sub_u32 v14, v14, v10\n"
"v_cmp_eq_u32 vcc, 16, v14\ns_sub_u32 s90, s90, 1\ns_cbranch_scc1 Lrgbx0_%=\ns_sleep 1\n"
"s_cbranch_vccz Lrwbx0_%=\n"
"Lrgbx0_%=:\ns_mov_b64 exec, -1\ns_add_u32 s92, s82, 0\n"
"buffer_load_dwordx4 a[68:71], v13, s[40:43], s92 offen offset:1024 sc1\n"
"buffer_load_dwordx4 a[76:79], v13, s[40:43], s92 offen offset:3072 sc1\ns_add_u32 s92, s82, 4096\n"
"buffer_load_dwordx4 a[84:87], v13, s[40:43], s92 offen offset:1024 sc1\n"
"buffer_load_dwordx4 a[92:95], v13, s[40:43], s92 offen offset:3072 sc1\ns_add_u32 s92, s82, 16384\n"
"buffer_load_dwordx4 a[100:103], v13, s[40:43], s92 offen offset:1024 sc1\n"
"buffer_load_dwordx4 a[108:111], v13, s[40:43], s92 offen offset:3072 sc1\ns_add_u32 s92, s82, 20480\n"
"buffer_load_dwordx4 a[116:119], v13, s[40:43], s92 offen offset:1024 sc1\n"
"buffer_load_dwordx4 a[124:127], v13, s[40:43], s92 offen offset:3072 sc1\ns_add_u32 s92, s82, 0\n"
"buffer_load_dwordx4 a[64:67], v13, s[40:43], s92 offen offset:0 sc1\n"
"buffer_load_dwordx4 a[72:75], v13, s[40:43], s92 offen offset:2048 sc1\ns_add_u32 s92, s82, 4096\n"
"buffer_load_dwordx4 a[80:83], v13, s[40:43], s92 offen offset:0 sc1\n"
"buffer_load_dwordx4 a[88:91], v13, s[40:43], s92 offen offset:2048 sc1\ns_add_u32 s92, s82, 16384\n"
"buffer_load_dwordx4 a[96:99], v13, s[40:43], s92 offen offset:0 sc1\n"
"buffer_load_dwordx4 a[104:107], v13, s[40:43], s92 offen offset:2048 sc1\ns_add_u32 s92, s82, 20480\n"
"buffer_load_dwordx4 a[112:115], v13, s[40:43], s92 offen offset:0 sc1\n"
"buffer_load_dwordx4 a[120:123], v13, s[40:43], s92 offen offset:2048 sc1\n"
"Lrsx0_%=:\nbuffer_store_dwordx4 v[128:131], v11, s[44:47], s4 offen sc1\ns_cmp_eq_u32 s74, 0\n"
"s_cbranch_scc1 Lfinx0_%=\ns_cmp_eq_u32 s35, 0\ns_cbranch_scc1 Lrqx0_%=\n"
"Lrpx0_%=:\ns_bfm_b64 exec, 1, 63\nglobal_atomic_add v16, v13, v8, s[98:99] sc0\ns_mov_b32 exec_lo, 0x11110000\n"
"s_mov_b32 exec_hi, 0x11111\nglobal_load_dword v14, v13, s[98:99] sc1\ns_mov_b64 exec, -1\n"
"Lrqx0_%=:\ns_waitcnt vmcnt(16)\nMvaaz 104,240,68\nv_lshlrev_b32 v120, 16, v232\n"
"v_and_b32 v121, 0xffff0000, v232\nMvaaz 108,244,68\nv_lshlrev_b32 v122, 16, v233\n"
"v_and_b32 v123, 0xffff0000, v233\ns_waitcnt vmcnt(15)\nMvaa 104,240,76\nv_lshlrev_b32 v124, 16, v234\n"
"v_and_b32 v125, 0xffff0000, v234\nMvaa 108,244,76\nv_lshlrev_b32 v126, 16, v235\n"
"v_and_b32 v127, 0xffff0000, v235\ns_waitcnt vmcnt(14)\nMvaa 104,240,84\nMvaa 108,244,84\ns_waitcnt vmcnt(13)\n"
"Mvaa 104,240,92\nMvaa 108,244,92\ns_waitcnt vmcnt(12)\nMvaa 104,240,100\nMvaa 108,244,100\ns_waitcnt vmcnt(11)\n"
"Mvaa 104,240,108\nMvaa 108,244,108\ns_waitcnt vmcnt(10)\nMvaa 104,240,116\nMvaa 108,244,116\n"
"s_waitcnt vmcnt(9)\nMvaa 104,240,124\nMvaa 108,244,124\ns_waitcnt vmcnt(8)\nMvaaz 96,240,64\ns_nop 1\n"
"Mvaaz 100,244,64\ns_nop 1\ns_waitcnt vmcnt(7)\nMvaa 96,240,72\nMvaa 100,244,72\ns_waitcnt vmcnt(6)\n"
"Mvaa 96,240,80\nEX 112,104\nEX 113,105\nEX 114,106\nEX 115,107\nMvaa 100,244,80\nEX 116,108\nEX 117,109\n"
"EX 118,110\nEX 119,111\ns_waitcnt vmcnt(5)\nMvaa 96,240,88\nv_mul_f32 v104, s89, v104\n"
"v_mul_f32 v105, s89, v105\nv_mul_f32 v106, s89, v106\nv_mul_f32 v107, s89, v107\nMvaa 100,244,88\n"
"v_mul_f32 v108, s89, v108\nv_mul_f32 v109, s89, v109\nv_mul_f32 v110, s89, v110\nv_mul_f32 v111, s89, v111\n"
"s_waitcnt vmcnt(4)\nMvaa 96,240,96\nv_add_f32 v112, 1.0, v112\nv_add_f32 v113, 1.0, v113\n"
"v_add_f32 v114, 1.0, v114\nv_add_f32 v115, 1.0, v115\nMvaa 100,244,96\nv_add_f32 v116, 1.0, v116\n"
"v_add_f32 v117, 1.0, v117\nv_add_f32 v118, 1.0, v118\nv_add_f32 v119, 1.0, v119\ns_waitcnt vmcnt(3)\n"
"Mvaa 96,240,104\nRC 112,112\nRC 113,113\nRC 114,114\nRC 115,115\nMvaa 100,244,104\nRC 116,116\nRC 117,117\n"
"RC 118,118\nRC 119,119\ns_waitcnt vmcnt(2)\nMvaa 96,240,112\nML 104,104,112\nML 105,105,113\nML 106,106,114\n"
"ML 107,107,115\nMvaa 100,244,112\nML 108,108,116\nML 109,109,117\nML 110,110,118\nML 111,111,119\n"
"s_waitcnt vmcnt(1)\nMvaa 96,240,120\nMvaa 100,244,120\ns_nop 7\n"
"v_pk_fma_f32 v[96:97], v[96:97], v[104:105], v[120:121]\n"
"v_pk_fma_f32 v[98:99], v[98:99], v[106:107], v[122:123]\n"
"v_pk_fma_f32 v[100:101], v[100:101], v[108:109], v[124:125]\n"
"v_pk_fma_f32 v[102:103], v[102:103], v[110:111], v[126:127]\nCV 136,96,97\nCV 137,98,99\nCV 138,100,101\n"
"CV 139,102,103\nbuffer_store_dwordx4 v[136:139], v11, s[44:47], s2 offen sc1\ns_cmp_eq_u32 s35, 0\n"
"s_cbranch_scc1 Lfinx0_%=\ns_waitcnt vmcnt(1)\nv_sub_u32 v16, v16, v10\nv_sub_u32 v14, v14, v10\n"
"v_readlane_b32 s91, v16, 63\nv_cmp_eq_u32 s[6:7], 16, v14\ns_cmp_ge_u32 s91, 16\ns_cbranch_scc1 Lfinx0_%=\n"
"s_add_u32 s91, s91, 16\n"
"Lryx0_%=:\ns_lshl_b32 s82, s91, 15\ns_and_b32 s2, s91, 1\ns_mul_i32 s2, s2, 18400\ns_lshl_b32 s92, s91, 5\n"
"s_add_u32 s2, s2, s92\ns_and_b32 s91, s91, 60\ns_add_u32 s91, s91, 16\n"
"buffer_load_dwordx4 v[232:235], v11, s[36:39], s2 offen\ns_bitcmp1_b64 s[6:7], s91\ns_cbranch_scc1 Lrgcx0_%=\n"
"s_mov_b32 s90, 262144\ns_bfm_b64 exec, 1, s91\n"
"Lrwcx0_%=:\nglobal_load_dword v14, v13, s[98:99] sc1\ns_waitcnt vmcnt(0)\nv_sub_u32 v14, v14, v10\n"
"v_cmp_eq_u32 vcc, 16, v14\ns_sub_u32 s90, s90, 1\ns_cbranch_scc1 Lrgcx0_%=\ns_sleep 1\n"
"s_cbranch_vccz Lrwcx0_%=\n"
"Lrgcx0_%=:\ns_mov_b64 exec, -1\ns_add_u32 s92, s82, 0\n"
"buffer_load_dwordx4 a[68:71], v13, s[40:43], s92 offen offset:1024 sc1\n"
"buffer_load_dwordx4 a[76:79], v13, s[40:43], s92 offen offset:3072 sc1\ns_add_u32 s92, s82, 4096\n"
"buffer_load_dwordx4 a[84:87], v13, s[40:43], s92 offen offset:1024 sc1\n"
"buffer_load_dwordx4 a[92:95], v13, s[40:43], s92 offen offset:3072 sc1\ns_add_u32 s92, s82, 16384\n"
"buffer_load_dwordx4 a[100:103], v13, s[40:43], s92 offen offset:1024 sc1\n"
"buffer_load_dwordx4 a[108:111], v13, s[40:43], s92 offen offset:3072 sc1\ns_add_u32 s92, s82, 20480\n"
"buffer_load_dwordx4 a[116:119], v13, s[40:43], s92 offen offset:1024 sc1\n"
"buffer_load_dwordx4 a[124:127], v13, s[40:43], s92 offen offset:3072 sc1\ns_add_u32 s92, s82, 0\n"
"buffer_load_dwordx4 a[64:67], v13, s[40:43], s92 offen offset:0 sc1\n"
"buffer_load_dwordx4 a[72:75], v13, s[40:43], s92 offen offset:2048 sc1\ns_add_u32 s92, s82, 4096\n"
"buffer_load_dwordx4 a[80:83], v13, s[40:43], s92 offen offset:0 sc1\n"
"buffer_load_dwordx4 a[88:91], v13, s[40:43], s92 offen offset:2048 sc1\ns_add_u32 s92, s82, 16384\n"
"buffer_load_dwordx4 a[96:99], v13, s[40:43], s92 offen offset:0 sc1\n"
"buffer_load_dwordx4 a[104:107], v13, s[40:43], s92 offen offset:2048 sc1\ns_add_u32 s92, s82, 20480\n"
"buffer_load_dwordx4 a[112:115], v13, s[40:43], s92 offen offset:0 sc1\n"
"buffer_load_dwordx4 a[120:123], v13, s[40:43], s92 offen offset:2048 sc1\ns_branch Lrpx0_%=\n"
"Lfinx0_%=:\ns_endpgm\n"
"Lrslowx0_%=:\ns_waitcnt vmcnt(0)\ns_bfm_b64 exec, 1, 48\nglobal_atomic_add v13, v9, s[98:99]\n"
"s_mov_b64 exec, -1\ns_cmp_lg_u32 s84, 0\ns_cbranch_scc1 Lrf0x0_%=\ns_lshl_b32 s82, s91, 15\ns_mov_b32 s91, s78\n"
"s_bitcmp1_b64 s[6:7], s91\ns_cbranch_scc1 Lrg0x0_%=\ns_mov_b32 s90, 262144\ns_bfm_b64 exec, 1, s91\n"
"Lrw0x0_%=:\nglobal_load_dword v14, v13, s[98:99] sc1\ns_waitcnt vmcnt(0)\nv_sub_u32 v14, v14, v10\n"
"v_cmp_eq_u32 vcc, 16, v14\ns_sub_u32 s90, s90, 1\ns_cbranch_scc1 Lrg0x0_%=\ns_sleep 1\n"
"s_cbranch_vccz Lrw0x0_%=\n"
"Lrg0x0_%=:\ns_mov_b64 exec, -1\nbuffer_load_dwordx4 v[224:227], v11, s[36:39], s4 offen\ns_add_u32 s92, s82, 0\n"
"buffer_load_dwordx4 v[164:167], v13, s[40:43], s92 offen offset:1024 sc1\n"
"buffer_load_dwordx4 v[172:175], v13, s[40:43], s92 offen offset:3072 sc1\ns_add_u32 s92, s82, 4096\n"
"buffer_load_dwordx4 v[180:183], v13, s[40:43], s92 offen offset:1024 sc1\n"
"buffer_load_dwordx4 v[188:191], v13, s[40:43], s92 offen offset:3072 sc1\ns_add_u32 s92, s82, 16384\n"
"buffer_load_dwordx4 v[196:199], v13, s[40:43], s92 offen offset:1024 sc1\n"
"buffer_load_dwordx4 v[204:207], v13, s[40:43], s92 offen offset:3072 sc1\ns_add_u32 s92, s82, 20480\n"
"buffer_load_dwordx4 v[212:215], v13, s[40:43], s92 offen offset:1024 sc1\n"
"buffer_load_dwordx4 v[220:223], v13, s[40:43], s92 offen offset:3072 sc1\ns_add_u32 s92, s82, 0\n"
"buffer_load_dwordx4 v[160:163], v13, s[40:43], s92 offen offset:0 sc1\n"
"buffer_load_dwordx4 v[168:171], v13, s[40:43], s92 offen offset:2048 sc1\ns_add_u32 s92, s82, 4096\n"
"buffer_load_dwordx4 v[176:179], v13, s[40:43], s92 offen offset:0 sc1\n"
"buffer_load_dwordx4 v[184:187], v13, s[40:43], s92 offen offset:2048 sc1\ns_add_u32 s92, s82, 16384\n"
"buffer_load_dwordx4 v[192:195], v13, s[40:43], s92 offen offset:0 sc1\n"
"buffer_load_dwordx4 v[200:203], v13, s[40:43], s92 offen offset:2048 sc1\ns_add_u32 s92, s82, 20480\n"
"buffer_load_dwordx4 v[208:211], v13, s[40:43], s92 offen offset:0 sc1\n"
"buffer_load_dwordx4 v[216:219], v13, s[40:43], s92 offen offset:2048 sc1\n"
"Lrf0x0_%=:\ns_branch Lrhx0_%=\n"
"Lrslow2x0_%=:\ns_cmp_eq_u32 s74, 0\ns_cbranch_scc1 Lre1x0_%=\n"
"buffer_load_dwordx4 v[232:235], v11, s[36:39], s2 offen\ns_bitcmp1_b64 s[6:7], s91\ns_cbranch_scc0 Lre1x0_%=\n"
"s_add_u32 s92, s82, 0\nbuffer_load_dwordx4 a[68:71], v13, s[40:43], s92 offen offset:1024 sc1\n"
"buffer_load_dwordx4 a[76:79], v13, s[40:43], s92 offen offset:3072 sc1\ns_add_u32 s92, s82, 4096\n"
"buffer_load_dwordx4 a[84:87], v13, s[40:43], s92 offen offset:1024 sc1\n"
"buffer_load_dwordx4 a[92:95], v13, s[40:43], s92 offen offset:3072 sc1\ns_add_u32 s92, s82, 16384\n"
"buffer_load_dwordx4 a[100:103], v13, s[40:43], s92 offen offset:1024 sc1\n"
"buffer_load_dwordx4 a[108:111], v13, s[40:43], s92 offen offset:3072 sc1\ns_add_u32 s92, s82, 20480\n"
"buffer_load_dwordx4 a[116:119], v13, s[40:43], s92 offen offset:1024 sc1\n"
"buffer_load_dwordx4 a[124:127], v13, s[40:43], s92 offen offset:3072 sc1\ns_add_u32 s92, s82, 0\n"
"buffer_load_dwordx4 a[64:67], v13, s[40:43], s92 offen offset:0 sc1\n"
"buffer_load_dwordx4 a[72:75], v13, s[40:43], s92 offen offset:2048 sc1\ns_add_u32 s92, s82, 4096\n"
"buffer_load_dwordx4 a[80:83], v13, s[40:43], s92 offen offset:0 sc1\n"
"buffer_load_dwordx4 a[88:91], v13, s[40:43], s92 offen offset:2048 sc1\ns_add_u32 s92, s82, 16384\n"
"buffer_load_dwordx4 a[96:99], v13, s[40:43], s92 offen offset:0 sc1\n"
"buffer_load_dwordx4 a[104:107], v13, s[40:43], s92 offen offset:2048 sc1\ns_add_u32 s92, s82, 20480\n"
"buffer_load_dwordx4 a[112:115], v13, s[40:43], s92 offen offset:0 sc1\n"
"buffer_load_dwordx4 a[120:123], v13, s[40:43], s92 offen offset:2048 sc1\ns_waitcnt vmcnt(17)\n"
"s_branch Lrmx0_%=\n"
"Lre1x0_%=:\ns_waitcnt vmcnt(0)\ns_branch Lrmx0_%=\n"
"Lpr0s_%=:\ns_mov_b32 s90, 262144\n"
"Lpr0l_%=:\ns_mov_b64 exec, s[0:1]\nglobal_atomic_add v13, v174, s[98:99]\ns_mov_b64 exec, -1\ns_sleep 16\n"
"s_bfm_b64 exec, 16, 0\nglobal_load_dword v175, v13, s[98:99] sc1\ns_mov_b64 exec, -1\ns_waitcnt vmcnt(0)\n"
"v_cmp_eq_u32 vcc, v172, v175\ns_sub_u32 s90, s90, 1\ns_cbranch_scc1 Lpr0r_%=\ns_and_b32 s92, vcc_lo, 0xffff\n"
"s_cbranch_scc1 Lpr0l_%=\ns_branch Lpr0r_%=\n"
"Lpos_%=:\ns_mov_b32 s90, 262144\ns_mov_b64 exec, 1\n"
"Lpol_%=:\nglobal_load_dword v14, v13, s[96:97] sc1\ns_waitcnt vmcnt(0)\nv_cmp_eq_u32 vcc, s80, v14\n"
"s_sub_u32 s90, s90, 1\ns_cbranch_scc1 Lpox_%=\ns_cbranch_vccz Lpox_%=\ns_sleep 2\ns_branch Lpol_%=\n"
"Lpox_%=:\ns_mov_b64 exec, -1\ns_branch Lpor_%=\n"
"Lb00s_%=:\ns_mov_b32 s90, 6\ns_bfm_b64 exec, 1, s78\n"
"Lb00l_%=:\nglobal_load_dword v14, v13, s[98:99] sc1\ns_waitcnt vmcnt(0)\nv_sub_u32 v14, v14, v10\n"
"v_cmp_eq_u32 vcc, 16, v14\ns_sub_u32 s90, s90, 1\ns_cbranch_scc1 Lb00x_%=\ns_cbranch_vccz Lb00l_%=\n"
"Lb00x_%=:\ns_mov_b64 exec, -1\ns_branch Lb00r_%=\n.p2align 12\n"
"Lw21_%=:\n"
"Lhub_%=:\ns_setpc_b64 s[74:75]\n"
"Lodd_%=:\nMvvvc 208,28,148,232\nMvvvc 212,26,148,232\nMvvvc 216,28,152,232\nMvvvc 220,26,152,232\n"
"s_waitcnt vmcnt(9)\nds_read_b128 a[164:167], v23 offset:34816\nds_read_b128 a[168:171], v24 offset:34816\n"
"s_nop 0\nWL 2\nMvaa 180,236,156\nCV 148,208,209\nCV 149,210,211\nMvaa 180,236,160\nCV 150,212,213\n"
"CV 151,214,215\nMvaa 188,156,156\nCV 152,216,217\nCV 153,218,219\nMvaa 188,160,160\nCV 154,220,221\n"
"CV 155,222,223\nglobal_load_dword v14, v17, s[30:31]\ns_xor_b32 s90, s81, 1\ns_lshl_b32 s91, s81, 7\n"
"v_readlane_b32 s80, v173, s90\ns_add_u32 s74, s98, 16384\ns_add_u32 s74, s74, s91\ns_mov_b32 s75, s99\n"
"s_mov_b64 exec, 0xffff\nglobal_store_dwordx4 v109, a[236:239], s[74:75] sc1\ns_mov_b64 exec, -1\n"
"Mvvvc 192,28,156,232\nMvvvc 196,26,156,232\nMvvvc 200,28,160,232\nMvvvc 204,26,160,232\nWL 0\ns_nop 3\n"
"Mvaa 180,236,164\nCV 156,192,193\nCV 157,194,195\nMvaa 180,236,168\nCV 158,196,197\nCV 159,198,199\n"
"Mvaa 188,164,164\nCV 160,200,201\nCV 161,202,203\nMvaa 188,168,168\nCV 162,204,205\nCV 163,206,207\n"
"v_mul_f32 v251, 0xbae38e39, v180\nCV 236,251,251\nv_lshlrev_b32 v237, 16, v236\nv_lshlrev_b32 v238, 16, v236\n"
"v_lshlrev_b32 v239, 16, v236\nv_lshlrev_b32 v236, 16, v236\ns_nop 1\nMvvvc 192,28,36,236\n"
"s_add_u32 m0, s72, 13312\nv_cndmask_b32 v252, v184, v185, s[0:1]\nv_cndmask_b32 v252, v252, v186, s[2:3]\n"
"s_mov_b32 s84, 2654208\ns_mov_b32 s85, 293888\nMvvvc 196,26,36,236\ns_waitcnt vmcnt(2)\nLD 245,44,84,0\n"
"s_add_u32 s84, s84, s85\nMvvvc 200,28,44,236\ns_barrier\nv_cndmask_b32 v253, v188, v189, s[0:1]\n"
"v_cndmask_b32 v252, v252, v187, s[4:5]\nMvvvc 204,26,44,236\ns_mov_b32 s86, 261120\n"
"ds_bpermute_b32 v174, v25, v252\nv_cndmask_b32 v253, v253, v190, s[2:3]\nMvvvc 208,28,52,236\n"
"s_add_u32 s93, s76, 0\nLD 245,44,84,1024\nMvvvc 212,26,52,236\ns_add_u32 s84, s84, s85\n"
"v_cndmask_b32 v253, v253, v191, s[4:5]\nCV 36,192,193\nMvvvc 216,28,60,236\ns_mov_b32 s4, 1.0\n"
"ds_bpermute_b32 v175, v25, v253\nDT 172,0,0\nLD 245,44,84,2048\nMvvvc 220,26,60,236\ns_add_u32 s84, s84, s85\n"
"DT 174,4,0\nCV 37,194,195\nMvvvc 224,28,68,236\ns_mov_b32 s5, 1.0\nDT 176,0,1024\nCV 38,196,197\n"
"Mvvvc 228,26,68,236\ns_movk_i32 s87, 1024\nLD 245,44,84,3072\nMvvvc 192,28,76,236\ns_mov_b32 s84, 3833856\n"
"DT 178,4,1024\nCV 39,198,199\nMvvvc 196,26,76,236\ns_addk_i32 m0, 4096\nDT 180,0,2048\nCV 44,200,201\n"
"CV 45,202,203\nMvvvc 200,28,84,236\ns_movk_i32 s88, 0\nLD 245,44,84,0\nCV 46,204,205\nCV 47,206,207\n"
"Mvvvc 204,26,84,236\ns_add_u32 s84, s84, s85\nDT 182,4,2048\nCV 52,208,209\nCV 53,210,211\nMvvvc 208,28,92,236\n"
"DT 184,0,3072\nCV 54,212,213\nCV 55,214,215\nMvvvc 212,26,92,236\nLD 245,44,84,1024\nCV 60,216,217\n"
"CV 61,218,219\nMvvvc 216,28,100,236\ns_add_u32 s84, s84, s85\nDT 186,4,3072\nDT 188,0,4096\nCV 62,220,221\n"
"CV 63,222,223\nMvvvc 220,26,100,236\nDT 190,4,4096\nCV 68,224,225\nCV 69,226,227\nMvvac 224,28,136,236\n"
"LD 245,44,84,2048\nCV 70,228,229\nCV 71,230,231\nMvvac 228,26,136,236\ns_add_u32 s84, s84, s85\nDT 192,0,5120\n"
"DT 194,4,5120\nCV 76,192,193\nCV 77,194,195\nMvvac 192,28,140,236\nDT 196,0,6144\nCV 78,196,197\nCV 79,198,199\n"
"Mvvac 196,26,140,236\nLD 245,44,84,3072\nCV 84,200,201\nCV 85,202,203\nMvvac 200,28,144,236\n"
"s_mov_b32 s84, 5013504\nDT 198,4,6144\nDT 200,0,7168\nCV 86,204,205\nCV 87,206,207\nMvvac 204,26,144,236\n"
"s_addk_i32 m0, 4096\nDT 202,4,7168\nCV 92,208,209\nCV 93,210,211\nMvvac 208,28,148,236\nLD 245,44,84,0\n"
"CV 94,212,213\nCV 95,214,215\nMvvac 212,26,148,236\nWL 6\nDT 204,0,8192\nDT 206,4,8192\nCV 100,216,217\n"
"CV 101,218,219\nMvvac 216,28,152,236\ns_addk_i32 m0, 1024\nDT 208,1,0\nCV 102,220,221\nCV 103,222,223\n"
"Mvvac 220,26,152,236\ns_movk_i32 s84, 0\nDT 210,5,0\nCV 224,224,225\nMvavz 104,172,32\nDT 212,1,1024\n"
"CV 226,226,227\nMvav 104,176,40\nDT 214,5,1024\nAW 136,224\nMvav 104,180,48\nWL 6\nDT 216,1,2048\nAW 137,226\n"
"Mvav 104,184,56\nDT 218,5,2048\nCV 228,228,229\nMvav 104,188,64\nDT 220,1,3072\nCV 230,230,231\n"
"Mvav 104,192,72\nWL 6\nDT 222,5,3072\nAW 138,228\nMvav 104,196,80\nDT 224,1,4096\nAW 139,230\nMvav 104,200,88\n"
"DT 226,5,4096\nCV 192,192,193\nMvav 104,204,96\nDT 228,1,5120\nCV 194,194,195\nMvavz 108,172,36\nDT 230,5,5120\n"
"AW 140,192\nMvav 108,176,44\nDT 232,1,6144\nAW 141,194\nMvav 108,180,52\nDT 234,5,6144\nCV 196,196,197\n"
"Mvav 108,184,60\nLD 244,40,84,0\nMvav 108,188,68\ns_add_u32 s84, s84, s86\nDT 240,1,7168\nDT 242,5,7168\n"
"Mvav 108,192,76\nDT 244,1,8192\nCV 198,198,199\nMvav 108,196,84\nDT 246,5,8192\nAW 142,196\nMvav 108,200,92\n"
"DT 248,2,0\nAW 143,198\nMvav 108,204,100\nLD 244,40,84,1024\nMvvac 224,28,156,236\ns_add_u32 s84, s84, s86\n"
"DT 250,6,0\nDT 252,2,1024\nMvvac 228,26,156,236\nWL 7\nDT 254,6,1024\nCV 200,200,201\nMvvac 192,28,160,236\n"
"DT 172,2,2048\nCV 202,202,203\nMvvac 196,26,160,236\nDT 174,6,2048\nAW 144,200\nMvavz 112,208,32\n"
"LD 244,40,84,2048\nMvav 112,212,40\ns_add_u32 s84, s84, s86\nDT 176,2,3072\nDT 178,6,3072\nMvav 112,216,48\n"
"DT 180,2,4096\nAW 145,202\nMvav 112,220,56\nDT 182,6,4096\nCV 204,204,205\nMvav 112,224,64\nWL 6\n"
"DT 184,2,5120\nCV 206,206,207\nMvav 112,228,72\nLD 244,40,84,3072\nMvav 112,232,80\ns_mov_b32 s84, 1048576\n"
"DT 186,6,5120\nDT 188,2,6144\nMvav 112,240,88\ns_addk_i32 m0, 4096\nDT 190,6,6144\nAW 146,204\nMvav 112,244,96\n"
"DT 192,2,7168\nAW 147,206\nMvavz 116,208,36\nDT 194,6,7168\nCV 208,208,209\nMvav 116,212,44\nLD 244,40,84,0\n"
"Mvav 116,216,52\ns_add_u32 s84, s84, s86\nDT 196,2,8192\nDT 198,6,8192\nMvav 116,220,60\nDT 200,3,0\n"
"CV 210,210,211\nMvav 116,224,68\nDT 202,7,0\nAW 148,208\nMvav 116,228,76\nDT 204,3,1024\nAW 149,210\n"
"Mvav 116,232,84\nLD 244,40,84,1024\nMvav 116,240,92\ns_add_u32 s84, s84, s86\nDT 206,7,1024\nDT 208,3,2048\n"
"Mvav 116,244,100\nDT 210,7,2048\nCV 212,212,213\nMvvac 200,28,164,236\nDT 212,3,3072\nCV 214,214,215\n"
"Mvvac 204,26,164,236\nWL 6\nDT 214,7,3072\nAW 150,212\nMvavz 120,248,32\nLD 244,40,84,2048\nMvav 120,252,40\n"
"s_add_u32 s84, s84, s86\nDT 216,3,4096\nDT 218,7,4096\nMvav 120,172,48\nDT 220,3,5120\nAW 151,214\n"
"Mvav 120,176,56\nDT 222,7,5120\nCV 216,216,217\nMvav 120,180,64\nDT 224,3,6144\nCV 218,218,219\n"
"Mvav 120,184,72\nLD 244,40,84,3072\nMvav 120,188,80\ns_mov_b32 s84, 2097152\nDT 226,7,6144\nDT 228,3,7168\n"
"Mvav 120,192,88\ns_addk_i32 m0, 4096\nDT 230,7,7168\nCV 220,220,221\nMvav 120,196,96\ns_waitcnt vmcnt(8)\n"
"DT 232,3,8192\nCV 222,222,223\nMvavz 124,248,36\ns_barrier\nDT 234,7,8192\nAW 152,216\nMvav 124,252,44\n"
"LD 244,40,84,0\nMvav 124,172,52\ns_add_u32 s84, s84, s86\nDT 240,0,13312\nDT 242,4,13312\nMvav 124,176,60\n"
"DT 244,0,14336\nAW 153,218\nMvav 124,180,68\nDT 246,4,14336\nAW 154,220\nMvav 124,184,76\nDT 248,0,15360\n"
"AW 155,222\nMvav 124,188,84\nDT 250,4,15360\nCV 224,224,225\nMvav 124,192,92\nDT 252,0,16384\nCV 226,226,227\n"
"Mvav 124,196,100\nWL 7\nDT 254,4,16384\nCV 228,228,229\nMvvac 208,28,168,236\nDT 172,0,17408\nCV 230,230,231\n"
"Mvvac 212,26,168,236\nDT 174,4,17408\nAW 156,224\nMvavz 164,200,32\nDT 176,0,18432\nAW 157,226\n"
"Mvav 164,204,40\nDT 178,4,18432\nAW 158,228\nMvav 164,208,48\nDT 180,0,19456\nAW 159,230\nMvav 164,212,56\n"
"DT 182,4,19456\nCV 192,192,193\nMvav 164,216,64\nDT 184,0,20480\nCV 194,194,195\nMvav 164,220,72\n"
"DT 186,4,20480\nCV 196,196,197\nMvav 164,224,80\nDT 188,0,21504\nCV 198,198,199\nMvav 164,228,88\n"
"DT 190,4,21504\nAW 160,192\nMvav 164,232,96\nDT 192,1,13312\nAW 161,194\nMvavz 168,200,36\nDT 194,5,13312\n"
"AW 162,196\nMvav 168,204,44\nLD 244,40,84,1024\nMvav 168,208,52\ns_add_u32 s84, s84, s86\nDT 196,1,14336\n"
"DT 198,5,14336\nMvav 168,212,60\nDT 200,1,15360\nAW 163,198\nMvav 168,216,68\nDT 202,5,15360\nCV 200,200,201\n"
"Mvav 168,220,76\nDT 204,1,16384\nCV 202,202,203\nMvav 168,224,84\nWL 6\nLD 244,40,84,2048\nMvav 168,228,92\n"
"s_add_u32 s84, s84, s86\nDT 206,5,16384\nDT 208,1,17408\nMvav 168,232,100\nDT 210,5,17408\nCV 204,204,205\n"
"Mvav 104,240,128\nDT 212,1,18432\nCV 206,206,207\nMvav 104,244,132\nDT 214,5,18432\nAW 164,200\n"
"Mvav 104,248,136\nLD 244,40,84,3072\nMvav 104,252,140\ns_mov_b32 s84, 3145728\nDT 216,1,19456\nDT 218,5,19456\n"
"Mvav 104,172,144\ns_addk_i32 m0, 4096\nDT 220,1,20480\nAW 165,202\nMvav 104,176,148\nDT 222,5,20480\n"
"AW 166,204\nMvav 104,180,152\nDT 224,1,21504\nAW 167,206\nMvav 104,184,156\nDT 226,5,21504\nCV 208,208,209\n"
"Mvav 104,188,160\nDT 228,2,13312\nCV 210,210,211\nMvaa 108,240,136\nDT 230,6,13312\nCV 212,212,213\n"
"Mvaa 108,244,140\nDT 232,2,14336\nCV 214,214,215\nMvaa 108,248,144\nDT 234,6,14336\nAW 168,208\n"
"Mvaa 108,252,148\nDT 240,2,15360\nAW 169,210\nMvaa 108,172,152\nDT 242,6,15360\nAW 170,212\nMvaa 108,176,156\n"
"DT 244,2,16384\nAW 171,214\nMvaa 108,180,160\nWL 6\nLD 244,40,84,0\nMvaa 108,184,164\ns_add_u32 s84, s84, s86\n"
"DT 246,6,16384\nDT 248,2,17408\nMvaa 108,188,168\nDT 250,6,17408\nv_mul_f32 v174, 0x3ae38e39, v174\n"
"Mvav 112,192,128\nDT 252,2,18432\nv_fma_f32 v174, -v250, v250, v174\nMvav 112,196,132\nLD 244,40,84,1024\n"
"Mvav 112,200,136\ns_add_u32 s84, s84, s86\nDT 254,6,18432\nDT 172,2,19456\nMvav 112,204,140\nDT 174,6,19456\n"
"v_max_f32 v174, 0, v174\nMvav 112,208,144\nDT 176,2,20480\nCV 174,174,174\nMvav 112,212,148\nLD 244,40,84,2048\n"
"Mvav 112,216,152\ns_add_u32 s84, s84, s86\nDT 178,6,20480\nDT 180,2,21504\nMvav 112,220,156\nDT 182,6,21504\n"
"v_lshlrev_b32 v174, 16, v174\nMvav 112,224,160\nDT 184,3,13312\nv_add_f32 v174, 0x3727c5ac, v174\n"
"Mvaa 116,192,136\nLD 244,40,84,3072\nMvaa 116,196,140\ns_mov_b32 s84, 4194304\nDT 186,7,13312\nDT 188,3,14336\n"
"Mvaa 116,200,144\ns_addk_i32 m0, 4096\nDT 190,7,14336\nCV 174,174,174\nMvaa 116,204,148\nDT 192,3,15360\n"
"v_lshlrev_b32 v174, 16, v174\nMvaa 116,208,152\nLD 244,40,84,0\nMvaa 116,212,156\ns_add_u32 s84, s84, s86\n"
"DT 194,7,15360\nDT 196,3,16384\nMvaa 116,216,160\nWL 6\nv_sqrt_f32 v174, v174\nMvaa 116,220,164\n"
"DT 198,7,16384\nDT 200,3,17408\nMvaa 116,224,168\nLD 244,40,84,1024\nMvav 120,228,128\n"
"Lw22_%=:\ns_cbranch_execz Lhub_%=\ns_movk_i32 s84, 128\nDT 202,7,17408\nDT 204,3,18432\nMvav 120,232,132\n"
"s_add_u32 m0, s72, 0\nDT 206,7,18432\nCV 174,174,174\nMvav 120,240,136\nDT 208,3,19456\n"
"v_lshlrev_b32 v174, 16, v174\nMvav 120,244,140\nRC 174,174\nMvav 120,248,144\nDT 210,7,19456\nDT 212,3,20480\n"
"Mvav 120,252,148\nDT 214,7,20480\nv_mul_f32 v16, 0x3e0293ee, v174\nMvav 120,172,152\ns_waitcnt vmcnt(9)\n"
"DT 216,3,21504\nv_mul_f32 v17, 0x3e0293ee, v174\nMvav 120,176,156\ns_barrier\nDT 218,7,21504\n"
"v_mul_f32 v175, 0x3ae38e39, v175\nMvav 120,180,160\ns_load_dword s83, s[74:75], 0x800\nLD 245,44,84,0\n"
"Mvaa 124,228,136\ns_add_u32 s84, s84, s85\nDT 220,0,22528\nDT 222,4,22528\nMvaa 124,232,140\n"
"s_mov_b64 s[6:7], 0\nDT 224,0,23552\nv_fma_f32 v175, -v251, v251, v175\nMvaa 124,240,144\ns_mov_b32 s78, 0\n"
"DT 226,4,23552\nv_max_f32 v175, 0, v175\nMvaa 124,244,148\ns_bfe_u32 s3, s74, 0x10015\nDT 228,0,24576\n"
"CV 175,175,175\nMvaa 124,248,152\ns_lshl_b32 s82, s3, 13\nDT 230,4,24576\nv_lshlrev_b32 v175, 16, v175\n"
"Mvaa 124,252,156\ns_lshl_b64 s[0:1], 1, s81\nDT 232,0,25600\nv_add_f32 v175, 0x3727c5ac, v175\n"
"Mvaa 124,172,160\nWL 7\nLD 245,44,84,1024\nMvaa 124,176,164\ns_add_u32 s84, s84, s85\nDT 234,4,25600\n"
"DT 240,0,26624\nMvaa 124,180,168\nDT 242,4,26624\nCV 175,175,175\nMvav 164,184,128\nDT 244,0,27648\n"
"v_lshlrev_b32 v175, 16, v175\nMvav 164,188,132\nv_sqrt_f32 v175, v175\nMvav 164,192,136\nDT 246,4,27648\n"
"DT 248,0,28672\nMvav 164,196,140\nDT 250,4,28672\nCV 175,175,175\nMvav 164,200,144\nLD 245,44,84,2048\n"
"Mvav 164,204,148\ns_add_u32 s84, s84, s85\nDT 252,0,29696\nDT 254,4,29696\nMvav 164,208,152\nDT 172,0,30720\n"
"v_lshlrev_b32 v175, 16, v175\nMvav 164,212,156\nRC 175,175\nMvav 164,216,160\nDT 174,4,30720\nDT 176,1,22528\n"
"Mvaa 168,184,136\nDT 178,5,22528\nv_mul_f32 v18, 0x3e0293ee, v175\nMvaa 168,188,140\nDT 180,1,23552\n"
"v_mul_f32 v19, 0x3e0293ee, v175\nMvaa 168,192,144\nLD 245,44,84,3072\nMvaa 168,196,148\ns_mov_b32 s84, 1179776\n"
"DT 182,5,23552\nDT 184,1,24576\nMvaa 168,200,152\ns_addk_i32 m0, 4096\nDT 186,5,24576\n"
"v_mul_f32 v240, 0xbfb8aa3b, v174\nMvaa 168,204,156\nDT 188,1,25600\nv_mul_f32 v241, 0xbfb8aa3b, v174\n"
"v_mul_f32 v242, 0xbfb8aa3b, v175\nv_mul_f32 v243, 0xbfb8aa3b, v175\n"
"v_pk_mul_f32 v[104:105], v[104:105], v[240:241]\nv_pk_mul_f32 v[106:107], v[106:107], v[240:241]\n"
"v_pk_mul_f32 v[108:109], v[108:109], v[242:243]\nv_pk_mul_f32 v[110:111], v[110:111], v[242:243]\n"
"v_pk_mul_f32 v[112:113], v[112:113], v[240:241]\nv_pk_mul_f32 v[114:115], v[114:115], v[240:241]\n"
"v_pk_mul_f32 v[116:117], v[116:117], v[242:243]\nv_pk_mul_f32 v[118:119], v[118:119], v[242:243]\n"
"v_pk_mul_f32 v[120:121], v[120:121], v[240:241]\nv_pk_mul_f32 v[122:123], v[122:123], v[240:241]\n"
"v_pk_mul_f32 v[124:125], v[124:125], v[242:243]\nv_pk_mul_f32 v[126:127], v[126:127], v[242:243]\n"
"v_pk_mul_f32 v[164:165], v[164:165], v[240:241]\nv_pk_mul_f32 v[166:167], v[166:167], v[240:241]\ns_nop 0\n"
"Mvaa 168,208,160\nWL 6\nEX 246,104\nMvaa 168,212,164\nDT 190,5,25600\nDT 192,1,26624\nMvaa 168,216,168\n"
"EX 247,105\nMvavz 176,220,32\nDT 194,5,26624\nDT 196,1,27648\nMvav 176,224,40\nLD 245,44,84,0\nMvav 176,228,48\n"
"s_add_u32 s84, s84, s85\nDT 198,5,27648\nDT 200,1,28672\nMvav 176,232,56\nEX 248,106\nMvav 176,240,64\n"
"DT 202,5,28672\nDT 204,1,29696\nMvav 176,244,72\nEX 249,107\nMvav 176,248,80\nDT 206,5,29696\nDT 208,1,30720\n"
"Mvav 176,252,88\nLD 245,44,84,1024\nMvav 176,172,96\ns_add_u32 s84, s84, s85\nDT 210,5,30720\nDT 212,2,22528\n"
"Mvavz 180,220,36\nDT 214,6,22528\nv_add_f32 v246, 1.0, v246\nMvav 180,224,44\nDT 216,2,23552\n"
"v_add_f32 v247, 1.0, v247\nMvav 180,228,52\nDT 218,6,23552\nv_add_f32 v248, 1.0, v248\nMvav 180,232,60\n"
"DT 220,2,24576\nv_add_f32 v249, 1.0, v249\nMvav 180,240,68\nRC 246,246\nMvav 180,244,76\nDT 222,6,24576\n"
"DT 224,2,25600\nMvav 180,248,84\nWL 6\nLD 245,44,84,2048\nMvav 180,252,92\ns_add_u32 s84, s84, s85\n"
"DT 226,6,25600\nDT 228,2,26624\nMvav 180,172,100\nRC 247,247\nMvavz 184,176,32\nDT 230,6,26624\nDT 232,2,27648\n"
"Mvav 184,180,40\nRC 248,248\nMvav 184,184,48\nDT 234,6,27648\nDT 240,2,28672\nMvav 184,188,56\n"
"LD 245,44,84,3072\nMvav 184,192,64\ns_mov_b32 s84, 2359424\nDT 242,6,28672\nDT 244,2,29696\nMvav 184,196,72\n"
"s_addk_i32 m0, 4096\nRC 249,249\nMvav 184,200,80\nDT 246,6,29696\nDT 248,2,30720\nMvav 184,204,88\n"
"DT 250,6,30720\nML 104,104,246\nMvav 184,208,96\nDT 252,3,22528\nML 105,105,247\nMvavz 188,176,36\n"
"DT 254,7,22528\nML 106,106,248\nMvav 188,180,44\nLD 245,44,84,0\nMvav 188,184,52\ns_mov_b32 s84, 2654336\n"
"DT 172,3,23552\nDT 174,7,23552\nMvav 188,188,60\ns_addk_i32 m0, 5120\nDT 176,3,24576\nML 107,107,249\n"
"Mvav 188,192,68\nDT 178,7,24576\nCV 104,104,105\nMvav 188,196,76\nDT 180,3,25600\nCV 106,106,107\n"
"Mvav 188,200,84\nWL 6\nDT 182,7,25600\nAW 64,104\nMvav 188,204,92\nDT 184,3,26624\nAW 65,106\nMvav 188,208,100\n"
"EX 250,108\nMvavz 192,212,32\nDT 186,7,26624\nDT 188,3,27648\nMvav 192,216,40\nEX 251,109\nMvav 192,220,48\n"
"DT 190,7,27648\nDT 192,3,28672\nMvav 192,224,56\nEX 252,110\nMvav 192,228,64\nDT 194,7,28672\nDT 196,3,29696\n"
"Mvav 192,232,72\nEX 253,111\nMvav 192,240,80\ns_waitcnt vmcnt(9)\nDT 198,7,29696\nDT 200,3,30720\n"
"Mvav 192,244,88\ns_barrier\nDT 202,7,30720\nv_add_f32 v250, 1.0, v250\nMvav 192,248,96\nLD 245,44,84,0\n"
"Mvavz 196,212,36\ns_load_dword s6, s[74:75], s82\nDT 204,0,31744\nDT 206,4,31744\nMvav 196,216,44\n"
"s_add_u32 s84, s84, s85\nDT 208,0,32768\nv_add_f32 v251, 1.0, v251\nMvav 196,220,52\nDT 210,4,32768\n"
"v_add_f32 v252, 1.0, v252\nMvav 196,224,60\nDT 212,0,33792\nv_add_f32 v253, 1.0, v253\nMvav 196,228,68\n"
"RC 250,250\nMvav 196,232,76\nDT 214,4,33792\nDT 216,0,34816\nMvav 196,240,84\nWL 7\nLD 245,44,84,1024\n"
"Mvav 196,244,92\ns_add_u32 s84, s84, s85\nDT 218,4,34816\nDT 220,0,35840\nMvav 196,248,100\nRC 251,251\n"
"Mvavz 200,252,32\nDT 222,4,35840\nDT 224,0,36864\nMvav 200,172,40\ns_add_u32 s78, s78, s6\nRC 252,252\n"
"Mvav 200,176,48\nDT 226,4,36864\nDT 228,0,37888\nMvav 200,180,56\nRC 253,253\nMvav 200,184,64\n"
"s_add_u32 s78, s78, s6\nDT 230,4,37888\nDT 232,0,38912\nMvav 200,188,72\nLD 245,44,84,2048\nMvav 200,192,80\n"
"s_add_u32 s84, s84, s85\nDT 234,4,38912\nDT 240,0,39936\nMvav 200,196,88\ns_add_u32 s78, s78, s6\n"
"DT 242,4,39936\nML 108,108,250\nMvav 200,200,96\nDT 244,1,31744\nML 109,109,251\nMvavz 204,252,36\n"
"DT 246,5,31744\nML 110,110,252\nMvav 204,172,44\ns_add_u32 s78, s78, s6\nDT 248,1,32768\nML 111,111,253\n"
"Mvav 204,176,52\nLD 245,44,84,3072\nMvav 204,180,60\ns_mov_b32 s84, 3833984\nDT 250,5,32768\nDT 252,1,33792\n"
"Mvav 204,184,68\ns_add_u32 s78, s78, s6\nDT 254,5,33792\nCV 108,108,109\nMvav 204,188,76\ns_addk_i32 m0, 4096\n"
"DT 172,1,34816\nCV 110,110,111\nMvav 204,192,84\nWL 6\nDT 174,5,34816\nAW 68,108\nMvav 204,196,92\n"
"s_add_u32 s78, s78, s6\nDT 176,1,35840\nAW 69,110\nMvav 204,200,100\nEX 254,112\nMvav 176,204,128\n"
"DT 178,5,35840\nDT 180,1,36864\nMvav 176,208,132\ns_add_u32 s78, s78, s6\nLD 245,44,84,0\nMvav 176,212,136\n"
"s_add_u32 s84, s84, s85\nDT 182,5,36864\nDT 184,1,37888\nMvav 176,216,140\nEX 255,113\nMvav 176,220,144\n"
"s_add_u32 s78, s78, s6\nDT 186,5,37888\nDT 188,1,38912\nMvav 176,224,148\nEX 20,114\nMvav 176,228,152\n"
"DT 190,5,38912\nDT 192,1,39936\nMvav 176,232,156\ns_add_u32 s78, s78, s6\nLD 245,44,84,1024\nMvav 176,240,160\n"
"s_add_u32 s84, s84, s85\nDT 194,5,39936\nDT 196,2,31744\nMvaa 180,204,136\nEX 21,115\nMvaa 180,208,140\n"
"s_add_u32 s78, s78, s6\nDT 198,6,31744\nDT 200,2,32768\nMvaa 180,212,144\nDT 202,6,32768\n"
"v_add_f32 v254, 1.0, v254\nMvaa 180,216,148\nDT 204,2,33792\nv_add_f32 v255, 1.0, v255\nMvaa 180,220,152\n"
"s_add_u32 s78, s78, s6\nDT 206,6,33792\nv_add_f32 v20, 1.0, v20\nMvaa 180,224,156\nLD 245,44,84,2048\n"
"Mvaa 180,228,160\nWL 5\nDT 208,2,34816\nDT 210,6,34816\nMvaa 180,232,164\ns_add_u32 s78, s78, s6\n"
"DT 212,2,35840\nv_add_f32 v21, 1.0, v21\nMvaa 180,240,168\ns_add_u32 s84, s84, s85\nRC 254,254\n"
"Mvav 184,244,128\nDT 214,6,35840\nDT 216,2,36864\nMvav 184,248,132\ns_add_u32 s78, s78, s6\nRC 255,255\n"
"Mvav 184,252,136\nDT 218,6,36864\nDT 220,2,37888\nMvav 184,172,140\nLD 245,44,84,3072\nMvav 184,176,144\n"
"s_add_u32 s78, s78, s6\nDT 222,6,37888\nDT 224,2,38912\nMvav 184,180,148\ns_addk_i32 m0, 4096\nRC 20,20\n"
"Mvav 184,184,152\ns_mov_b32 s84, 5013632\nDT 226,6,38912\nDT 228,2,39936\nMvav 184,188,156\n"
"s_add_u32 s78, s78, s6\nRC 21,21\nMvav 184,192,160\nDT 230,6,39936\nDT 232,3,31744\nMvaa 188,244,136\n"
"DT 234,7,31744\nML 112,112,254\nMvaa 188,248,140\ns_add_u32 s78, s78, s6\nLD 245,44,84,0\nMvaa 188,252,144\n"
"s_movk_i32 s84, 128\nDT 240,3,32768\nDT 242,7,32768\nMvaa 188,172,148\ns_addk_i32 m0, 1024\nDT 244,3,33792\n"
"ML 113,113,255\nMvaa 188,176,152\ns_add_u32 s78, s78, s6\nDT 246,7,33792\nML 114,114,20\nMvaa 188,180,156\n"
"DT 248,3,34816\nML 115,115,21\nMvaa 188,184,160\nWL 6\nDT 250,7,34816\nCV 112,112,113\nMvaa 188,188,164\n"
"s_add_u32 s78, s78, s6\nDT 252,3,35840\nCV 114,114,115\ns_xor_b32 s82, s82, 0x2000\nMvaa 188,192,168\n"
"DT 254,7,35840\nAW 66,112\nMvav 192,196,128\nDT 172,3,36864\nAW 67,114\nMvav 192,200,132\nEX 22,116\n"
"Mvav 192,204,136\nDT 174,7,36864\nDT 176,3,37888\nMvav 192,208,140\nEX 23,117\nMvav 192,212,144\n"
"DT 178,7,37888\nDT 180,3,38912\nMvav 192,216,148\nEX 24,118\nMvav 192,220,152\ns_waitcnt vmcnt(9)\n"
"DT 182,7,38912\nDT 184,3,39936\nMvav 192,224,156\ns_barrier\nEX 25,119\nMvav 192,228,160\nDT 186,7,39936\n"
"DT 188,0,0\nMvaa 196,196,136\ns_load_dword s7, s[74:75], s82\nLD 244,40,84,0\nMvaa 196,200,140\n"
"s_add_u32 s84, s84, s86\nDT 190,4,0\nDT 192,0,1024\nMvaa 196,204,144\nDT 194,4,1024\nv_add_f32 v22, 1.0, v22\n"
"Mvaa 196,208,148\nDT 196,0,2048\nv_add_f32 v23, 1.0, v23\nMvaa 196,212,152\nDT 198,4,2048\n"
"v_add_f32 v24, 1.0, v24\nMvaa 196,216,156\nDT 200,0,3072\nv_add_f32 v25, 1.0, v25\nMvaa 196,220,160\nWL 6\n"
"LD 244,40,84,1024\nMvaa 196,224,164\ns_add_u32 s84, s84, s86\nDT 202,4,3072\nDT 204,0,4096\nMvaa 196,228,168\n"
"RC 22,22\nMvav 200,232,128\nDT 206,4,4096\nDT 208,0,5120\nMvav 200,240,132\ns_sub_u32 s78, s78, s7\nRC 23,23\n"
"Mvav 200,244,136\nDT 210,4,5120\nDT 212,0,6144\nMvav 200,248,140\nRC 24,24\nMvav 200,252,144\n"
"s_sub_u32 s78, s78, s7\nDT 214,4,6144\nDT 216,0,7168\nMvav 200,172,148\nLD 244,40,84,2048\nMvav 200,176,152\n"
"s_add_u32 s84, s84, s86\nDT 218,4,7168\nDT 220,0,8192\nMvav 200,180,156\ns_sub_u32 s78, s78, s7\nRC 25,25\n"
"Mvav 200,184,160\nDT 222,4,8192\nDT 224,1,0\nMvaa 204,232,136\nDT 226,5,0\nML 116,116,22\nMvaa 204,240,140\n"
"s_sub_u32 s78, s78, s7\nDT 228,1,1024\nML 117,117,23\nMvaa 204,244,144\nLD 244,40,84,3072\nMvaa 204,248,148\n"
"s_mov_b32 s84, 1048704\nDT 230,5,1024\nDT 232,1,2048\nMvaa 204,252,152\ns_sub_u32 s78, s78, s7\nDT 234,5,2048\n"
"ML 118,118,24\nMvaa 204,172,156\ns_addk_i32 m0, 4096\nDT 240,1,3072\nML 119,119,25\nMvaa 204,176,160\nWL 6\n"
"DT 242,5,3072\nCV 116,116,117\nMvaa 204,180,164\ns_sub_u32 s78, s78, s7\nDT 244,1,4096\nCV 118,118,119\n"
"Mvaa 204,184,168\nDT 246,5,4096\nAW 70,116\nMvavz 208,188,32\nLD 244,40,84,0\nMvav 208,192,40\n"
"s_sub_u32 s78, s78, s7\n"
"Lw23_%=:\ns_cbranch_execz Lhub_%=\nDT 248,1,5120\nDT 250,5,5120\nMvav 208,196,48\ns_add_u32 s84, s84, s86\n"
"DT 252,1,6144\nAW 71,118\nMvav 208,200,56\nEX 28,120\nMvav 208,204,64\ns_sub_u32 s78, s78, s7\nDT 254,5,6144\n"
"DT 172,1,7168\nMvav 208,208,72\nEX 29,121\nMvav 208,212,80\nDT 174,5,7168\nDT 176,1,8192\nMvav 208,216,88\n"
"s_sub_u32 s78, s78, s7\nLD 244,40,84,1024\nMvav 208,220,96\ns_add_u32 s84, s84, s86\nDT 178,5,8192\nDT 180,2,0\n"
"Mvavz 212,188,36\nEX 30,122\nMvav 212,192,44\ns_sub_u32 s78, s78, s7\nDT 182,6,0\nDT 184,2,1024\n"
"Mvav 212,196,52\nEX 31,123\nMvav 212,200,60\nDT 186,6,1024\nDT 188,2,2048\nMvav 212,204,68\n"
"s_sub_u32 s78, s78, s7\nDT 190,6,2048\nv_add_f32 v28, 1.0, v28\nMvav 212,208,76\nLD 244,40,84,2048\n"
"Mvav 212,212,84\nWL 5\nDT 192,2,3072\nDT 194,6,3072\nMvav 212,216,92\ns_sub_u32 s78, s78, s7\nDT 196,2,4096\n"
"v_add_f32 v29, 1.0, v29\nMvav 212,220,100\ns_add_u32 s84, s84, s86\nDT 198,6,4096\nv_add_f32 v30, 1.0, v30\n"
"Mvavz 216,224,32\nDT 200,2,5120\nv_add_f32 v31, 1.0, v31\nMvav 216,228,40\ns_sub_u32 s78, s78, s7\nRC 28,28\n"
"Mvav 216,232,48\nDT 202,6,5120\nDT 204,2,6144\nMvav 216,240,56\nLD 244,40,84,3072\nMvav 216,244,64\n"
"s_sub_u32 s78, s78, s7\nDT 206,6,6144\nDT 208,2,7168\nMvav 216,248,72\ns_addk_i32 m0, 4096\nRC 29,29\n"
"Mvav 216,252,80\ns_mov_b32 s84, 2097280\nDT 210,6,7168\nDT 212,2,8192\nMvav 216,172,88\ns_sub_u32 s78, s78, s7\n"
"RC 30,30\nMvav 216,176,96\nDT 214,6,8192\nDT 216,3,0\nMvavz 220,224,36\nRC 31,31\nMvav 220,228,44\n"
"s_sub_u32 s78, s78, s7\nDT 218,7,0\nDT 220,3,1024\nMvav 220,232,52\nLD 244,40,84,0\nMvav 220,240,60\n"
"s_add_u32 s84, s84, s86\nDT 222,7,1024\nDT 224,3,2048\nMvav 220,244,68\ns_sub_u32 s78, s78, s7\nDT 226,7,2048\n"
"ML 120,120,28\nMvav 220,248,76\nDT 228,3,3072\nML 121,121,29\nMvav 220,252,84\nWL 6\nDT 230,7,3072\n"
"ML 122,122,30\nMvav 220,172,92\ns_sub_u32 s78, s78, s7\nDT 232,3,4096\nML 123,123,31\nMvav 220,176,100\n"
"s_bfe_u32 s78, s78, 0x10016\nDT 234,7,4096\nCV 120,120,121\nMvavz 224,180,32\ns_xor_b32 s78, s78, s3\n"
"DT 240,3,5120\nv_xor_b32 v174, s78, v172\nMvav 224,184,40\ns_movk_i32 s74, 1024\nDT 242,7,5120\n"
"v_and_or_b32 v174, v174, 1, 2\nMvav 224,188,48\ns_movk_i32 s75, 0\ns_mov_b64 exec, s[0:1]\n"
"global_atomic_add v13, v174, s[98:99]\ns_mov_b64 exec, -1\nMvav 224,192,56\nDT 244,3,6144\nDT 246,7,6144\n"
"Mvav 224,196,64\nDT 248,3,7168\nv_mov_b32 v174, 2\nMvav 224,200,72\nDT 250,7,7168\nCV 122,122,123\n"
"Mvav 224,204,80\ns_waitcnt vmcnt(10)\nDT 252,3,8192\nAW 72,120\nMvav 224,208,88\ns_barrier\nDT 254,7,8192\n"
"AW 73,122\nMvav 224,212,96\nLD 244,40,84,1024\nMvavz 228,180,36\ns_add_u32 s84, s84, s86\nDT 172,0,13312\n"
"DT 174,4,13312\nMvav 228,184,44\nEX 26,124\nMvav 228,188,52\nDT 176,0,14336\nDT 178,4,14336\nMvav 228,192,60\n"
"EX 27,125\nMvav 228,196,68\nDT 180,0,15360\nDT 182,4,15360\nMvav 228,200,76\nEX 246,126\nMvav 228,204,84\nWL 6\n"
"DT 184,0,16384\nDT 186,4,16384\nMvav 228,208,92\nLD 244,40,84,2048\nMvav 228,212,100\ns_add_u32 s84, s84, s86\n"
"DT 188,0,17408\nDT 190,4,17408\nMvavz 232,216,32\nEX 247,127\nMvav 232,220,40\nDT 192,0,18432\nDT 194,4,18432\n"
"Mvav 232,224,48\nDT 196,0,19456\nv_add_f32 v26, 1.0, v26\nMvav 232,228,56\nDT 198,4,19456\n"
"v_add_f32 v27, 1.0, v27\nMvav 232,232,64\nLD 244,40,84,3072\nMvav 232,240,72\ns_mov_b32 s84, 3145856\n"
"DT 200,0,20480\nDT 202,4,20480\nMvav 232,244,80\ns_addk_i32 m0, 4096\nDT 204,0,21504\n"
"v_add_f32 v246, 1.0, v246\nMvav 232,248,88\nDT 206,4,21504\nv_add_f32 v247, 1.0, v247\nMvav 232,252,96\n"
"RC 26,26\nMvavz 236,216,36\nDT 208,1,13312\nDT 210,5,13312\nMvav 236,220,44\nRC 27,27\nMvav 236,224,52\n"
"DT 212,1,14336\nDT 214,5,14336\nMvav 236,228,60\nLD 244,40,84,0\nMvav 236,232,68\ns_add_u32 s84, s84, s86\n"
"DT 216,1,15360\nDT 218,5,15360\nMvav 236,240,76\nRC 246,246\nMvav 236,244,84\nWL 6\nDT 220,1,16384\n"
"DT 222,5,16384\nMvav 236,248,92\nRC 247,247\nMvav 236,252,100\nDT 224,1,17408\nDT 226,5,17408\n"
"Mvav 208,172,128\nLD 244,40,84,1024\nMvav 208,176,132\ns_add_u32 s84, s84, s86\nDT 228,1,18432\nDT 230,5,18432\n"
"Mvav 208,180,136\nDT 232,1,19456\nML 124,124,26\nMvav 208,184,140\nDT 234,5,19456\nML 125,125,27\n"
"Mvav 208,188,144\nDT 240,1,20480\nML 126,126,246\nMvav 208,192,148\nDT 242,5,20480\nML 127,127,247\n"
"Mvav 208,196,152\nDT 244,1,21504\nCV 124,124,125\nMvav 208,200,156\nLD 244,40,84,2048\nMvav 208,204,160\n"
"s_add_u32 s84, s84, s86\nDT 246,5,21504\nDT 248,2,13312\nMvaa 212,172,136\nDT 250,6,13312\nCV 126,126,127\n"
"Mvaa 212,176,140\nDT 252,2,14336\nAW 76,124\nMvaa 212,180,144\nDT 254,6,14336\nAW 77,126\nMvaa 212,184,148\n"
"EX 248,164\nMvaa 212,188,152\nDT 172,2,15360\nDT 174,6,15360\nMvaa 212,192,156\nLD 244,40,84,3072\n"
"Mvaa 212,196,160\nWL 5\nDT 176,2,16384\nDT 178,6,16384\nMvaa 212,200,164\ns_addk_i32 m0, 4096\nEX 249,165\n"
"EX 250,166\nv_add_f32 v248, 1.0, v248\nv_pk_mul_f32 v[176:177], v[176:177], v[16:17]\n"
"v_pk_mul_f32 v[178:179], v[178:179], v[16:17]\nv_pk_mul_f32 v[180:181], v[180:181], v[18:19]\n"
"v_pk_mul_f32 v[182:183], v[182:183], v[18:19]\nv_pk_mul_f32 v[184:185], v[184:185], v[16:17]\n"
"v_pk_mul_f32 v[186:187], v[186:187], v[16:17]\nv_pk_mul_f32 v[188:189], v[188:189], v[18:19]\n"
"v_pk_mul_f32 v[190:191], v[190:191], v[18:19]\nv_pk_mul_f32 v[192:193], v[192:193], v[16:17]\n"
"v_pk_mul_f32 v[194:195], v[194:195], v[16:17]\nv_pk_mul_f32 v[196:197], v[196:197], v[18:19]\n"
"v_pk_mul_f32 v[198:199], v[198:199], v[18:19]\nv_pk_mul_f32 v[200:201], v[200:201], v[16:17]\n"
"v_pk_mul_f32 v[202:203], v[202:203], v[16:17]\nv_pk_mul_f32 v[204:205], v[204:205], v[18:19]\n"
"v_pk_mul_f32 v[206:207], v[206:207], v[18:19]\nv_pk_mul_f32 v[168:169], v[168:169], v[242:243]\n"
"v_pk_mul_f32 v[170:171], v[170:171], v[242:243]\nv_pk_mul_f32 v[208:209], v[208:209], v[240:241]\n"
"v_pk_mul_f32 v[210:211], v[210:211], v[240:241]\nCV 176,176,177\nMvaa 212,204,168\ns_mov_b32 s84, 4194432\n"
"DT 180,2,17408\nDT 182,6,17408\nMvav 216,208,128\nDT 184,2,18432\nCV 178,178,179\nMvav 216,212,132\n"
"DT 186,6,18432\nAW 0,176\nMvav 216,216,136\nDT 188,2,19456\nAW 1,178\nMvav 216,220,140\nLD 244,40,84,0\n"
"Mvav 216,224,144\ns_add_u32 s84, s84, s86\nDT 190,6,19456\nDT 192,2,20480\nMvav 216,228,148\nDT 194,6,20480\n"
"CV 180,180,181\nMvav 216,232,152\nDT 196,2,21504\nCV 182,182,183\nMvav 216,240,156\nDT 198,6,21504\nAW 16,180\n"
"Mvav 216,244,160\nDT 200,3,13312\nAW 17,182\nMvaa 220,208,136\nDT 202,7,13312\nCV 184,184,185\n"
"Mvaa 220,212,140\nLD 244,40,84,1024\nMvaa 220,216,144\ns_movk_i32 s84, 256\nDT 204,3,14336\nDT 206,7,14336\n"
"Mvaa 220,220,148\ns_add_u32 m0, s72, 0\nDT 208,3,15360\nCV 186,186,187\nMvaa 220,224,152\nDT 210,7,15360\n"
"AW 2,184\nMvaa 220,228,156\nDT 212,3,16384\nAW 3,186\nMvaa 220,232,160\nWL 6\nDT 214,7,16384\nCV 188,188,189\n"
"Mvaa 220,240,164\nDT 216,3,17408\nCV 190,190,191\nMvaa 220,244,168\nDT 218,7,17408\nAW 18,188\n"
"Mvav 224,248,128\nDT 220,3,18432\nAW 19,190\nMvav 224,252,132\nDT 222,7,18432\nCV 192,192,193\n"
"Mvav 224,172,136\nDT 224,3,19456\nCV 194,194,195\nMvav 224,176,140\nDT 226,7,19456\nAW 4,192\nMvav 224,180,144\n"
"DT 228,3,20480\nAW 5,194\nMvav 224,184,148\nDT 230,7,20480\nCV 196,196,197\nMvav 224,188,152\n"
"s_waitcnt vmcnt(10)\nDT 232,3,21504\nCV 198,198,199\nMvav 224,192,156\ns_barrier\nDT 234,7,21504\nAW 20,196\n"
"Mvav 224,196,160\nLD 245,44,84,0\nMvaa 228,248,136\ns_add_u32 s84, s84, s85\nDT 240,0,22528\nDT 242,4,22528\n"
"Mvaa 228,252,140\nDT 244,0,23552\nAW 21,198\nMvaa 228,172,144\nDT 246,4,23552\nCV 200,200,201\n"
"Mvaa 228,176,148\nDT 248,0,24576\nCV 202,202,203\nMvaa 228,180,152\nDT 250,4,24576\nAW 6,200\nMvaa 228,184,156\n"
"DT 252,0,25600\nAW 7,202\nMvaa 228,188,160\nWL 7\nLD 245,44,84,1024\nMvaa 228,192,164\ns_add_u32 s84, s84, s85\n"
"DT 254,4,25600\nDT 172,0,26624\nMvaa 228,196,168\nDT 174,4,26624\nCV 204,204,205\nMvav 232,200,128\n"
"DT 176,0,27648\nCV 206,206,207\nMvav 232,204,132\nDT 178,4,27648\nAW 22,204\nMvav 232,208,136\nDT 180,0,28672\n"
"AW 23,206\nMvav 232,212,140\nEX 251,167\nMvav 232,216,144\nDT 182,4,28672\nDT 184,0,29696\nMvav 232,220,148\n"
"LD 245,44,84,2048\nMvav 232,224,152\ns_add_u32 s84, s84, s85\nDT 186,4,29696\nDT 188,0,30720\nMvav 232,228,156\n"
"DT 190,4,30720\nv_add_f32 v249, 1.0, v249\nMvav 232,232,160\nDT 192,1,22528\nv_add_f32 v250, 1.0, v250\n"
"Mvaa 236,200,136\nDT 194,5,22528\nv_add_f32 v251, 1.0, v251\nMvaa 236,204,140\nRC 248,248\nMvaa 236,208,144\n"
"DT 196,1,23552\nDT 198,5,23552\nMvaa 236,212,148\nLD 245,44,84,3072\nMvaa 236,216,152\ns_mov_b32 s84, 1179904\n"
"DT 200,1,24576\nDT 202,5,24576\nMvaa 236,220,156\ns_addk_i32 m0, 4096\nRC 249,249\nMvaa 236,224,160\nWL 5\n"
"DT 204,1,25600\nDT 206,5,25600\nMvaa 236,228,164\nRC 250,250\nMvaa 236,232,168\nDT 208,1,26624\nDT 210,5,26624\n"
"Mvavz 176,240,32\nLD 245,44,84,0\nMvav 176,244,40\ns_add_u32 s84, s84, s85\nDT 212,1,27648\nDT 214,5,27648\n"
"Mvav 176,248,48\nRC 251,251\nMvav 176,252,56\nDT 216,1,28672\nDT 218,5,28672\nMvav 176,172,64\nDT 220,1,29696\n"
"ML 164,164,248\nMvav 176,176,72\nDT 222,5,29696\nML 165,165,249\nMvav 176,180,80\nDT 224,1,30720\n"
"ML 166,166,250\nMvav 176,184,88\nLD 245,44,84,1024\nMvav 176,188,96\ns_add_u32 s84, s84, s85\nDT 226,5,30720\n"
"DT 228,2,22528\nMvavz 180,240,36\nDT 230,6,22528\nML 167,167,251\nMvav 180,244,44\nDT 232,2,23552\n"
"CV 164,164,165\nMvav 180,248,52\nDT 234,6,23552\nCV 166,166,167\nMvav 180,252,60\nDT 240,2,24576\nAW 74,164\n"
"Mvav 180,172,68\nDT 242,6,24576\nAW 75,166\nMvav 180,176,76\nLD 245,44,84,2048\nMvav 180,180,84\nWL 5\n"
"DT 244,2,25600\nDT 246,6,25600\nMvav 180,184,92\ns_add_u32 s84, s84, s85\nEX 252,168\nMvav 180,188,100\n"
"DT 248,2,26624\nDT 250,6,26624\nMvavz 184,192,32\nEX 253,169\nMvav 184,196,40\nDT 252,2,27648\nDT 254,6,27648\n"
"Mvav 184,200,48\nEX 254,170\nMvav 184,204,56\nDT 172,2,28672\nDT 174,6,28672\nMvav 184,208,64\n"
"LD 245,44,84,3072\nMvav 184,212,72\ns_mov_b32 s84, 2359552\nDT 176,2,29696\nDT 178,6,29696\nMvav 184,216,80\n"
"s_addk_i32 m0, 4096\nEX 255,171\nMvav 184,220,88\nDT 180,2,30720\nDT 182,6,30720\nMvav 184,224,96\n"
"DT 184,3,22528\nv_add_f32 v252, 1.0, v252\nMvavz 188,192,36\nDT 186,7,22528\nv_add_f32 v253, 1.0, v253\n"
"Mvav 188,196,44\nLD 245,44,84,0\nMvav 188,200,52\ns_mov_b32 s84, 2654464\nDT 188,3,23552\nDT 190,7,23552\n"
"Mvav 188,204,60\ns_addk_i32 m0, 5120\nDT 192,3,24576\nv_add_f32 v254, 1.0, v254\nMvav 188,208,68\n"
"DT 194,7,24576\nv_add_f32 v255, 1.0, v255\nMvav 188,212,76\nRC 252,252\nMvav 188,216,84\nWL 5\nDT 196,3,25600\n"
"DT 198,7,25600\nMvav 188,220,92\nRC 253,253\nMvav 188,224,100\nDT 200,3,26624\nDT 202,7,26624\ns_nop 0\n"
"Lw24_%=:\ns_cbranch_execz Lhub_%=\nMvavz 192,228,32\nRC 254,254\nMvav 192,232,40\nDT 204,3,27648\n"
"DT 206,7,27648\nMvav 192,240,48\nRC 255,255\nMvav 192,244,56\nDT 208,3,28672\nDT 210,7,28672\nMvav 192,248,64\n"
"DT 212,3,29696\nML 168,168,252\nMvav 192,252,72\nDT 214,7,29696\nML 169,169,253\nMvav 192,172,80\n"
"s_waitcnt vmcnt(9)\nDT 216,3,30720\nML 170,170,254\nMvav 192,176,88\ns_barrier\nDT 218,7,30720\nML 171,171,255\n"
"Mvav 192,180,96\nLD 245,44,84,0\nMvavz 196,228,36\ns_add_u32 s84, s84, s85\nDT 220,0,31744\nDT 222,4,31744\n"
"Mvav 196,232,44\nDT 224,0,32768\nCV 168,168,169\nMvav 196,240,52\nDT 226,4,32768\nCV 170,170,171\n"
"Mvav 196,244,60\nDT 228,0,33792\nAW 78,168\nMvav 196,248,68\nDT 230,4,33792\nAW 79,170\nMvav 196,252,76\n"
"EX 20,208\nMvav 196,172,84\nWL 6\nDT 232,0,34816\nDT 234,4,34816\nMvav 196,176,92\nLD 245,44,84,1024\n"
"Mvav 196,180,100\ns_add_u32 s84, s84, s85\nDT 240,0,35840\nDT 242,4,35840\nMvavz 200,184,32\nEX 21,209\n"
"EX 22,210\nv_add_f32 v20, 1.0, v20\nv_pk_mul_f32 v[212:213], v[212:213], v[242:243]\n"
"v_pk_mul_f32 v[214:215], v[214:215], v[242:243]\nv_pk_mul_f32 v[216:217], v[216:217], v[240:241]\n"
"v_pk_mul_f32 v[218:219], v[218:219], v[240:241]\nv_pk_mul_f32 v[220:221], v[220:221], v[242:243]\n"
"v_pk_mul_f32 v[222:223], v[222:223], v[242:243]\nv_pk_mul_f32 v[224:225], v[224:225], v[240:241]\n"
"v_pk_mul_f32 v[226:227], v[226:227], v[240:241]\nv_pk_mul_f32 v[228:229], v[228:229], v[242:243]\n"
"v_pk_mul_f32 v[230:231], v[230:231], v[242:243]\nv_pk_mul_f32 v[232:233], v[232:233], v[240:241]\n"
"v_pk_mul_f32 v[234:235], v[234:235], v[240:241]\nv_pk_mul_f32 v[236:237], v[236:237], v[242:243]\n"
"v_pk_mul_f32 v[238:239], v[238:239], v[242:243]\nv_add_f32 v21, 1.0, v21\nMvav 200,188,40\nDT 244,0,36864\n"
"DT 246,4,36864\nMvav 200,192,48\nEX 23,211\nMvav 200,196,56\nDT 248,0,37888\nDT 250,4,37888\nMvav 200,200,64\n"
"LD 245,44,84,2048\nMvav 200,204,72\ns_add_u32 s84, s84, s85\nDT 252,0,38912\nDT 254,4,38912\nMvav 200,208,80\n"
"DT 172,0,39936\nv_add_f32 v22, 1.0, v22\nMvav 200,212,88\nDT 174,4,39936\nv_add_f32 v23, 1.0, v23\n"
"Mvav 200,216,96\nRC 20,20\nMvavz 204,184,36\nDT 176,1,31744\nDT 178,5,31744\nMvav 204,188,44\nRC 21,21\n"
"Mvav 204,192,52\nDT 180,1,32768\nDT 182,5,32768\nMvav 204,196,60\nLD 245,44,84,3072\nMvav 204,200,68\n"
"s_mov_b32 s84, 3834112\nDT 184,1,33792\nDT 186,5,33792\nMvav 204,204,76\ns_addk_i32 m0, 4096\nRC 22,22\n"
"Mvav 204,208,84\nWL 6\nDT 188,1,34816\nDT 190,5,34816\nMvav 204,212,92\nRC 23,23\nMvav 204,216,100\n"
"DT 192,1,35840\nDT 194,5,35840\nMvav 176,220,128\nLD 245,44,84,0\nMvav 176,224,132\ns_add_u32 s84, s84, s85\n"
"DT 196,1,36864\nDT 198,5,36864\nMvav 176,228,136\nEX 24,212\nMvav 176,232,140\nDT 200,1,37888\nDT 202,5,37888\n"
"Mvav 176,240,144\nEX 25,213\nMvav 176,244,148\nDT 204,1,38912\nDT 206,5,38912\nMvav 176,248,152\nEX 28,214\n"
"Mvav 176,252,156\nDT 208,1,39936\nDT 210,5,39936\nMvav 176,172,160\nLD 245,44,84,1024\nMvaa 180,220,136\n"
"s_add_u32 s84, s84, s85\nDT 212,2,31744\nDT 214,6,31744\nMvaa 180,224,140\nEX 29,215\nMvaa 180,228,144\n"
"DT 216,2,32768\nDT 218,6,32768\nMvaa 180,232,148\nDT 220,2,33792\nv_add_f32 v24, 1.0, v24\nMvaa 180,240,152\n"
"DT 222,6,33792\nv_add_f32 v25, 1.0, v25\nMvaa 180,244,156\nLD 245,44,84,2048\nMvaa 180,248,160\nWL 6\n"
"DT 224,2,34816\nDT 226,6,34816\nMvaa 180,252,164\ns_add_u32 s84, s84, s85\nDT 228,2,35840\n"
"v_add_f32 v28, 1.0, v28\nMvaa 180,172,168\nDT 230,6,35840\nv_add_f32 v29, 1.0, v29\nMvav 184,176,128\nRC 24,24\n"
"Mvav 184,180,132\nDT 232,2,36864\nDT 234,6,36864\nMvav 184,184,136\nRC 25,25\nMvav 184,188,140\nDT 240,2,37888\n"
"DT 242,6,37888\nMvav 184,192,144\nLD 245,44,84,3072\nMvav 184,196,148\ns_mov_b32 s84, 5013760\nDT 244,2,38912\n"
"DT 246,6,38912\nMvav 184,200,152\ns_addk_i32 m0, 4096\nRC 28,28\nMvav 184,204,156\nDT 248,2,39936\n"
"DT 250,6,39936\nMvav 184,208,160\nRC 29,29\nMvaa 188,176,136\nDT 252,3,31744\nDT 254,7,31744\nMvaa 188,180,140\n"
"LD 245,44,84,0\nMvaa 188,184,144\ns_movk_i32 s84, 256\nDT 172,3,32768\nDT 174,7,32768\nMvaa 188,188,148\n"
"s_addk_i32 m0, 1024\nEX 30,216\nMvaa 188,192,152\nDT 176,3,33792\nDT 178,7,33792\nMvaa 188,196,156\nEX 31,217\n"
"Mvaa 188,200,160\nWL 6\nDT 180,3,34816\nDT 182,7,34816\nMvaa 188,204,164\nEX 26,218\nMvaa 188,208,168\n"
"DT 184,3,35840\nDT 186,7,35840\nMvav 192,212,128\nEX 27,219\nMvav 192,216,132\nDT 188,3,36864\nDT 190,7,36864\n"
"Mvav 192,220,136\nDT 192,3,37888\nv_add_f32 v30, 1.0, v30\nMvav 192,224,140\nDT 194,7,37888\n"
"v_add_f32 v31, 1.0, v31\nMvav 192,228,144\nDT 196,3,38912\nv_add_f32 v26, 1.0, v26\nMvav 192,232,148\n"
"DT 198,7,38912\nv_add_f32 v27, 1.0, v27\nMvav 192,240,152\ns_waitcnt vmcnt(9)\nRC 30,30\nMvav 192,244,156\n"
"s_barrier\nDT 200,3,39936\nDT 202,7,39936\nMvav 192,248,160\nLD 244,40,84,0\nMvaa 196,212,136\n"
"s_add_u32 s84, s84, s86\nDT 204,0,0\nDT 206,4,0\nMvaa 196,216,140\nRC 31,31\nMvaa 196,220,144\nDT 208,0,1024\n"
"DT 210,4,1024\nMvaa 196,224,148\nRC 26,26\nMvaa 196,228,152\nDT 212,0,2048\nDT 214,4,2048\nMvaa 196,232,156\n"
"RC 27,27\nMvaa 196,240,160\nWL 6\nDT 216,0,3072\nDT 218,4,3072\nMvaa 196,244,164\nLD 244,40,84,1024\n"
"Mvaa 196,248,168\ns_add_u32 s84, s84, s86\nDT 220,0,4096\nDT 222,4,4096\nMvav 200,252,128\nEX 246,220\n"
"Mvav 200,172,132\nDT 224,0,5120\nDT 226,4,5120\nMvav 200,176,136\nEX 247,221\nMvav 200,180,140\nDT 228,0,6144\n"
"DT 230,4,6144\nMvav 200,184,144\nLD 244,40,84,2048\nMvav 200,188,148\ns_add_u32 s84, s84, s86\nDT 232,0,7168\n"
"DT 234,4,7168\nMvav 200,192,152\nEX 248,222\nMvav 200,196,156\nDT 240,0,8192\nDT 242,4,8192\nMvav 200,200,160\n"
"EX 249,223\nMvaa 204,252,136\nDT 244,1,0\nDT 246,5,0\nMvaa 204,172,140\nDT 248,1,1024\n"
"v_add_f32 v246, 1.0, v246\nMvaa 204,176,144\nLD 244,40,84,3072\nMvaa 204,180,148\ns_mov_b32 s84, 1048832\n"
"DT 250,5,1024\nDT 252,1,2048\nMvaa 204,184,152\ns_addk_i32 m0, 4096\nDT 254,5,2048\nv_add_f32 v247, 1.0, v247\n"
"Mvaa 204,188,156\nDT 172,1,3072\nv_add_f32 v248, 1.0, v248\nMvaa 204,192,160\nWL 7\nDT 174,5,3072\n"
"v_add_f32 v249, 1.0, v249\nMvaa 204,196,164\nRC 246,246\nMvaa 204,200,168\nDT 176,1,4096\nDT 178,5,4096\n"
"Mvavz 104,204,32\nLD 244,40,84,0\nMvav 104,208,40\ns_add_u32 s84, s84, s86\nDT 180,1,5120\nDT 182,5,5120\n"
"Mvav 104,212,48\nRC 247,247\nMvav 104,216,56\nDT 184,1,6144\nDT 186,5,6144\nMvav 104,220,64\nRC 248,248\n"
"Mvav 104,224,72\nDT 188,1,7168\nDT 190,5,7168\nMvav 104,228,80\nRC 249,249\nMvav 104,232,88\nDT 192,1,8192\n"
"DT 194,5,8192\nMvav 104,240,96\nLD 244,40,84,1024\nMvavz 108,204,36\ns_add_u32 s84, s84, s86\nDT 196,2,0\n"
"DT 198,6,0\nMvav 108,208,44\nEX 250,224\nMvav 108,212,52\nDT 200,2,1024\nDT 202,6,1024\nMvav 108,216,60\n"
"EX 251,225\nMvav 108,220,68\nDT 204,2,2048\nDT 206,6,2048\nMvav 108,224,76\nLD 244,40,84,2048\nMvav 108,228,84\n"
"WL 6\nDT 208,2,3072\nDT 210,6,3072\nMvav 108,232,92\ns_add_u32 s84, s84, s86\nEX 252,226\nMvav 108,240,100\n"
"DT 212,2,4096\nDT 214,6,4096\nMvavz 112,244,32\nEX 253,227\nMvav 112,248,40\nDT 216,2,5120\nDT 218,6,5120\n"
"Mvav 112,252,48\nDT 220,2,6144\nv_add_f32 v250, 1.0, v250\nMvav 112,172,56\nLD 244,40,84,3072\nMvav 112,176,64\n"
"s_mov_b32 s84, 2097408\nDT 222,6,6144\nDT 224,2,7168\nMvav 112,180,72\ns_addk_i32 m0, 4096\nDT 226,6,7168\n"
"v_add_f32 v251, 1.0, v251\nMvav 112,184,80\nDT 228,2,8192\nv_add_f32 v252, 1.0, v252\nMvav 112,188,88\n"
"DT 230,6,8192\nv_add_f32 v253, 1.0, v253\nMvav 112,192,96\nRC 250,250\nMvavz 116,244,36\nDT 232,3,0\n"
"DT 234,7,0\nMvav 116,248,44\nLD 244,40,84,0\nMvav 116,252,52\ns_add_u32 s84, s84, s86\nDT 240,3,1024\n"
"DT 242,7,1024\nMvav 116,172,60\nRC 251,251\nMvav 116,176,68\nDT 244,3,2048\nDT 246,7,2048\nMvav 116,180,76\n"
"RC 252,252\nMvav 116,184,84\nWL 6\nDT 248,3,3072\nDT 250,7,3072\nMvav 116,188,92\nRC 253,253\nEX 254,228\n"
"v_pk_mul_f32 v[176:177], v[176:177], v[16:17]\nv_pk_mul_f32 v[178:179], v[178:179], v[16:17]\n"
"v_pk_mul_f32 v[180:181], v[180:181], v[18:19]\nv_pk_mul_f32 v[182:183], v[182:183], v[18:19]\n"
"v_pk_mul_f32 v[184:185], v[184:185], v[16:17]\nv_pk_mul_f32 v[186:187], v[186:187], v[16:17]\n"
"v_pk_mul_f32 v[188:189], v[188:189], v[18:19]\nv_pk_mul_f32 v[190:191], v[190:191], v[18:19]\n"
"v_pk_mul_f32 v[192:193], v[192:193], v[16:17]\nv_pk_mul_f32 v[194:195], v[194:195], v[16:17]\n"
"v_pk_mul_f32 v[196:197], v[196:197], v[18:19]\nv_pk_mul_f32 v[198:199], v[198:199], v[18:19]\n"
"v_pk_mul_f32 v[200:201], v[200:201], v[16:17]\nv_pk_mul_f32 v[202:203], v[202:203], v[16:17]\n"
"v_pk_mul_f32 v[204:205], v[204:205], v[18:19]\nv_pk_mul_f32 v[206:207], v[206:207], v[18:19]\n"
"v_pk_mul_f32 v[208:209], v[208:209], v[20:21]\nv_pk_mul_f32 v[210:211], v[210:211], v[22:23]\n"
"v_pk_mul_f32 v[212:213], v[212:213], v[24:25]\nv_pk_mul_f32 v[214:215], v[214:215], v[28:29]\n"
"v_pk_mul_f32 v[216:217], v[216:217], v[30:31]\nv_pk_mul_f32 v[218:219], v[218:219], v[26:27]\n"
"v_pk_mul_f32 v[220:221], v[220:221], v[246:247]\nv_pk_mul_f32 v[222:223], v[222:223], v[248:249]\n"
"v_pk_mul_f32 v[224:225], v[224:225], v[250:251]\nv_pk_mul_f32 v[226:227], v[226:227], v[252:253]\n"
"CV 176,176,177\nMvav 116,192,100\nDT 252,3,4096\nDT 254,7,4096\nMvavz 120,196,32\nDT 172,3,5120\n"
"CV 178,178,179\nMvav 120,200,40\nDT 174,7,5120\nAW 8,176\nMvav 120,204,48\nDT 176,3,6144\nAW 9,178\n"
"Mvav 120,208,56\nDT 178,7,6144\nCV 180,180,181\nMvav 120,212,64\nDT 180,3,7168\nCV 182,182,183\n"
"Mvav 120,216,72\nDT 182,7,7168\nAW 24,180\nMvav 120,220,80\ns_waitcnt vmcnt(9)\nDT 184,3,8192\nAW 25,182\n"
"Mvav 120,224,88\ns_barrier\nDT 186,7,8192\nCV 184,184,185\nMvav 120,228,96\nLD 244,40,84,1024\n"
"Mvavz 124,196,36\ns_add_u32 s84, s84, s86\nDT 188,0,13312\nDT 190,4,13312\nMvav 124,200,44\nDT 192,0,14336\n"
"CV 186,186,187\nMvav 124,204,52\nDT 194,4,14336\nAW 10,184\nMvav 124,208,60\nDT 196,0,15360\nAW 11,186\n"
"Mvav 124,212,68\nDT 198,4,15360\nCV 188,188,189\nMvav 124,216,76\nDT 200,0,16384\nCV 190,190,191\n"
"Mvav 124,220,84\nWL 7\nLD 244,40,84,2048\nMvav 124,224,92\ns_add_u32 s84, s84, s86\nDT 202,4,16384\n"
"DT 204,0,17408\nMvav 124,228,100\nDT 206,4,17408\nAW 26,188\nMvavz 164,232,32\nDT 208,0,18432\nAW 27,190\n"
"Mvav 164,240,40\nDT 210,4,18432\nCV 192,192,193\nMvav 164,244,48\nDT 212,0,19456\nCV 194,194,195\n"
"Mvav 164,248,56\nDT 214,4,19456\nAW 12,192\nMvav 164,252,64\nLD 244,40,84,3072\nMvav 164,172,72\n"
"s_mov_b32 s84, 3145984\nDT 216,0,20480\nDT 218,4,20480\nMvav 164,176,80\ns_addk_i32 m0, 4096\nDT 220,0,21504\n"
"AW 13,194\nMvav 164,180,88\nDT 222,4,21504\nCV 196,196,197\nMvav 164,184,96\nDT 224,1,13312\nCV 198,198,199\n"
"Mvavz 168,232,36\nDT 226,5,13312\nAW 28,196\nMvav 168,240,44\nDT 228,1,14336\nAW 29,198\nMvav 168,244,52\n"
"LD 244,40,84,0\nMvav 168,248,60\ns_add_u32 s84, s84, s86\nDT 230,5,14336\nDT 232,1,15360\nMvav 168,252,68\n"
"DT 234,5,15360\nCV 200,200,201\nMvav 168,172,76\nDT 240,1,16384\nCV 202,202,203\nMvav 168,176,84\nWL 6\n"
"DT 242,5,16384\nAW 14,200\nMvav 168,180,92\nDT 244,1,17408\nAW 15,202\nMvav 168,184,100\ns_nop 0\n"
"Lw25_%=:\ns_cbranch_execz Lhub_%=\nDT 246,5,17408\nCV 204,204,205\nMvav 104,188,128\nLD 244,40,84,1024\n"
"Mvav 104,192,132\ns_add_u32 s84, s84, s86\nDT 248,1,18432\nDT 250,5,18432\nMvav 104,196,136\nDT 252,1,19456\n"
"CV 206,206,207\nMvav 104,200,140\nDT 254,5,19456\nAW 30,204\nMvav 104,204,144\nDT 172,1,20480\nAW 31,206\n"
"Mvav 104,208,148\nDT 174,5,20480\nCV 208,208,209\nMvav 104,212,152\nDT 176,1,21504\nCV 210,210,211\n"
"Mvav 104,216,156\nLD 244,40,84,2048\nMvav 104,220,160\ns_add_u32 s84, s84, s86\nDT 178,5,21504\nDT 180,2,13312\n"
"Mvaa 108,188,136\nDT 182,6,13312\nAW 80,208\nMvaa 108,192,140\nDT 184,2,14336\nAW 81,210\nMvaa 108,196,144\n"
"DT 186,6,14336\nCV 212,212,213\nMvaa 108,200,148\nDT 188,2,15360\nCV 214,214,215\nMvaa 108,204,152\n"
"DT 190,6,15360\nAW 84,212\nMvaa 108,208,156\nLD 244,40,84,3072\nMvaa 108,212,160\nWL 5\nDT 192,2,16384\n"
"DT 194,6,16384\nMvaa 108,216,164\ns_addk_i32 m0, 4096\nDT 196,2,17408\nAW 85,214\nMvaa 108,220,168\n"
"s_mov_b32 s84, 4194560\nDT 198,6,17408\nCV 216,216,217\nMvav 112,224,128\nDT 200,2,18432\nCV 218,218,219\n"
"Mvav 112,228,132\nDT 202,6,18432\nAW 82,216\nMvav 112,232,136\nDT 204,2,19456\nAW 83,218\nMvav 112,240,140\n"
"LD 244,40,84,0\nMvav 112,244,144\ns_add_u32 s84, s84, s86\nDT 206,6,19456\nDT 208,2,20480\nMvav 112,248,148\n"
"DT 210,6,20480\nCV 220,220,221\nMvav 112,252,152\nDT 212,2,21504\nCV 222,222,223\nMvav 112,172,156\n"
"DT 214,6,21504\nAW 86,220\nMvav 112,176,160\nDT 216,3,13312\nAW 87,222\nMvaa 116,224,136\nDT 218,7,13312\n"
"CV 224,224,225\nMvaa 116,228,140\nLD 244,40,84,1024\nMvaa 116,232,144\ns_movk_i32 s84, 384\nDT 220,3,14336\n"
"DT 222,7,14336\nMvaa 116,240,148\ns_add_u32 m0, s72, 0\nDT 224,3,15360\nCV 226,226,227\nMvaa 116,244,152\n"
"DT 226,7,15360\nAW 88,224\nMvaa 116,248,156\nDT 228,3,16384\nAW 89,226\nMvaa 116,252,160\nWL 6\nEX 255,229\n"
"Mvaa 116,172,164\nDT 230,7,16384\nDT 232,3,17408\nMvaa 116,176,168\nEX 20,230\nMvav 120,180,128\n"
"DT 234,7,17408\nDT 240,3,18432\nMvav 120,184,132\nEX 21,231\nMvav 120,188,136\nDT 242,7,18432\nDT 244,3,19456\n"
"Mvav 120,192,140\nDT 246,7,19456\nv_add_f32 v254, 1.0, v254\nMvav 120,196,144\nDT 248,3,20480\n"
"v_add_f32 v255, 1.0, v255\nMvav 120,200,148\nDT 250,7,20480\nv_add_f32 v20, 1.0, v20\nMvav 120,204,152\n"
"s_waitcnt vmcnt(9)\nDT 252,3,21504\nv_add_f32 v21, 1.0, v21\nMvav 120,208,156\ns_barrier\nRC 254,254\n"
"Mvav 120,212,160\nDT 254,7,21504\nDT 172,0,22528\nMvaa 124,180,136\nLD 244,40,84,0\nMvaa 124,184,140\n"
"s_add_u32 s84, s84, s86\nDT 174,4,22528\nDT 176,0,23552\nMvaa 124,188,144\nRC 255,255\nMvaa 124,192,148\n"
"DT 178,4,23552\nDT 180,0,24576\nMvaa 124,196,152\nRC 20,20\nMvaa 124,200,156\nDT 182,4,24576\nDT 184,0,25600\n"
"Mvaa 124,204,160\nWL 6\nLD 244,40,84,1024\nMvaa 124,208,164\ns_add_u32 s84, s84, s86\nDT 186,4,25600\n"
"DT 188,0,26624\nMvaa 124,212,168\nRC 21,21\nMvav 164,216,128\nDT 190,4,26624\nDT 192,0,27648\nMvav 164,220,132\n"
"EX 22,232\nMvav 164,224,136\nDT 194,4,27648\nDT 196,0,28672\nMvav 164,228,140\nEX 23,233\nMvav 164,232,144\n"
"DT 198,4,28672\nDT 200,0,29696\nMvav 164,240,148\nLD 244,40,84,2048\nMvav 164,244,152\ns_add_u32 s84, s84, s86\n"
"DT 202,4,29696\nDT 204,0,30720\nMvav 164,248,156\nEX 24,234\nMvav 164,252,160\nDT 206,4,30720\nDT 208,1,22528\n"
"Mvaa 168,216,136\nEX 25,235\nMvaa 168,220,140\nDT 210,5,22528\nDT 212,1,23552\nMvaa 168,224,144\n"
"LD 244,40,84,3072\nMvaa 168,228,148\ns_mov_b32 s84, 1048960\nDT 214,5,23552\nDT 216,1,24576\nMvaa 168,232,152\n"
"s_addk_i32 m0, 4096\nDT 218,5,24576\nv_add_f32 v22, 1.0, v22\nMvaa 168,240,156\nDT 220,1,25600\n"
"v_add_f32 v23, 1.0, v23\nMvaa 168,244,160\nWL 6\nDT 222,5,25600\nv_add_f32 v24, 1.0, v24\nMvaa 168,248,164\n"
"DT 224,1,26624\nv_add_f32 v25, 1.0, v25\nMvaa 168,252,168\nRC 22,22\nMvavz 176,172,32\nDT 226,5,26624\n"
"DT 228,1,27648\nMvav 176,176,40\nLD 244,40,84,0\nMvav 176,180,48\ns_add_u32 s84, s84, s86\nDT 230,5,27648\n"
"DT 232,1,28672\nMvav 176,184,56\nRC 23,23\nMvav 176,188,64\nDT 234,5,28672\nDT 240,1,29696\nMvav 176,192,72\n"
"RC 24,24\nMvav 176,196,80\nDT 242,5,29696\nDT 244,1,30720\nMvav 176,200,88\nLD 244,40,84,1024\nMvav 176,204,96\n"
"s_add_u32 s84, s84, s86\nDT 246,5,30720\nDT 248,2,22528\nMvavz 180,172,36\nRC 25,25\nMvav 180,176,44\n"
"DT 250,6,22528\nDT 252,2,23552\nMvav 180,180,52\nEX 28,236\nMvav 180,184,60\nDT 254,6,23552\nDT 172,2,24576\n"
"Mvav 180,188,68\nEX 29,237\nMvav 180,192,76\nDT 174,6,24576\nDT 176,2,25600\nMvav 180,196,84\nWL 6\n"
"LD 244,40,84,2048\nMvav 180,200,92\ns_add_u32 s84, s84, s86\nDT 178,6,25600\nDT 180,2,26624\nMvav 180,204,100\n"
"EX 30,238\nMvavz 184,208,32\nDT 182,6,26624\nDT 184,2,27648\nMvav 184,212,40\nEX 31,239\nMvav 184,216,48\n"
"DT 186,6,27648\nDT 188,2,28672\nMvav 184,220,56\nLD 244,40,84,3072\nMvav 184,224,64\ns_mov_b32 s84, 2097536\n"
"DT 190,6,28672\nDT 192,2,29696\nMvav 184,228,72\ns_addk_i32 m0, 4096\nDT 194,6,29696\nv_add_f32 v28, 1.0, v28\n"
"Mvav 184,232,80\nDT 196,2,30720\nv_add_f32 v29, 1.0, v29\nMvav 184,240,88\nDT 198,6,30720\n"
"v_add_f32 v30, 1.0, v30\nMvav 184,244,96\nDT 200,3,22528\nv_add_f32 v31, 1.0, v31\nMvavz 188,208,36\nRC 28,28\n"
"Mvav 188,212,44\nDT 202,7,22528\nDT 204,3,23552\nMvav 188,216,52\nLD 244,40,84,0\nMvav 188,220,60\n"
"s_mov_b32 s84, 2359680\nDT 206,7,23552\nDT 208,3,24576\nMvav 188,224,68\ns_addk_i32 m0, 5120\nRC 29,29\n"
"Mvav 188,228,76\nDT 210,7,24576\nDT 212,3,25600\nMvav 188,232,84\nWL 6\nRC 30,30\nMvav 188,240,92\n"
"DT 214,7,25600\nDT 216,3,26624\nMvav 188,244,100\nRC 31,31\nMvavz 192,248,32\nDT 218,7,26624\nDT 220,3,27648\n"
"Mvav 192,252,40\nDT 222,7,27648\nML 116,116,242\nMvav 192,172,48\nDT 224,3,28672\nML 117,117,243\n"
"Mvav 192,176,56\nDT 226,7,28672\nML 104,104,240\nMvav 192,180,64\nDT 228,3,29696\nML 105,105,241\n"
"Mvav 192,184,72\nDT 230,7,29696\nML 118,118,242\nMvav 192,188,80\ns_waitcnt vmcnt(9)\nDT 232,3,30720\n"
"ML 119,119,243\nMvav 192,192,88\ns_barrier\nEX 26,104\nMvav 192,196,96\nDT 234,7,30720\nDT 240,0,31744\n"
"Mvavz 196,248,36\nLD 244,40,84,0\nMvav 196,252,44\ns_add_u32 s84, s84, s86\nDT 242,4,31744\nDT 244,0,32768\n"
"Mvav 196,172,52\nEX 27,105\nMvav 196,176,60\nDT 246,4,32768\nDT 248,0,33792\nMvav 196,180,68\nDT 250,4,33792\n"
"ML 120,120,240\nMvav 196,184,76\nDT 252,0,34816\nML 121,121,241\nMvav 196,188,84\nWL 6\nLD 244,40,84,1024\n"
"Mvav 196,192,92\ns_add_u32 s84, s84, s86\nDT 254,4,34816\nDT 172,0,35840\nMvav 196,196,100\nDT 174,4,35840\n"
"v_add_f32 v26, 1.0, v26\nMvavz 200,200,32\nDT 176,0,36864\nv_add_f32 v27, 1.0, v27\nMvav 200,204,40\nRC 26,26\n"
"Mvav 200,208,48\nDT 178,4,36864\nDT 180,0,37888\nMvav 200,212,56\nRC 27,27\nMvav 200,216,64\nDT 182,4,37888\n"
"DT 184,0,38912\nMvav 200,220,72\nLD 244,40,84,2048\nMvav 200,224,80\ns_add_u32 s84, s84, s86\nDT 186,4,38912\n"
"DT 188,0,39936\nMvav 200,228,88\nDT 190,4,39936\nML 126,126,242\nMvav 200,232,96\nDT 192,1,31744\n"
"ML 127,127,243\nMvavz 204,200,36\nDT 194,5,31744\nML 122,122,240\nMvav 204,204,44\nDT 196,1,32768\n"
"ML 123,123,241\nMvav 204,208,52\nLD 244,40,84,3072\nMvav 204,212,60\ns_mov_b32 s84, 3408256\nDT 198,5,32768\n"
"DT 200,1,33792\nMvav 204,216,68\ns_addk_i32 m0, 4096\nDT 202,5,33792\nML 124,124,242\nMvav 204,220,76\n"
"DT 204,1,34816\nML 125,125,243\nMvav 204,224,84\nWL 6\nDT 206,5,34816\nML 106,106,240\nMvav 204,228,92\n"
"DT 208,1,35840\nML 107,107,241\nMvav 204,232,100\nDT 210,5,35840\nML 108,108,242\nMvav 176,240,128\n"
"LD 244,40,84,0\nMvav 176,244,132\ns_add_u32 s84, s84, s86\nDT 212,1,36864\nDT 214,5,36864\nMvav 176,248,136\n"
"DT 216,1,37888\nML 109,109,243\nMvav 176,252,140\nEX 246,106\nMvav 176,172,144\nDT 218,5,37888\nDT 220,1,38912\n"
"Mvav 176,176,148\nEX 247,107\nMvav 176,180,152\nDT 222,5,38912\nDT 224,1,39936\nMvav 176,184,156\n"
"LD 244,40,84,1024\nMvav 176,188,160\ns_add_u32 s84, s84, s86\nDT 226,5,39936\nDT 228,2,31744\nMvaa 180,240,136\n"
"EX 248,108\nMvaa 180,244,140\nDT 230,6,31744\nDT 232,2,32768\nMvaa 180,248,144\nEX 249,109\nMvaa 180,252,148\n"
"DT 234,6,32768\nDT 240,2,33792\nMvaa 180,172,152\nDT 242,6,33792\nML 110,110,242\nMvaa 180,176,156\n"
"LD 244,40,84,2048\nMvaa 180,180,160\nWL 5\nDT 244,2,34816\nDT 246,6,34816\nMvaa 180,184,164\n"
"s_add_u32 s84, s84, s86\nDT 248,2,35840\nML 111,111,243\nMvaa 180,188,168\nDT 250,6,35840\nML 112,112,240\n"
"Mvav 184,192,128\nDT 252,2,36864\nML 113,113,241\nMvav 184,196,132\nDT 254,6,36864\nv_add_f32 v246, 1.0, v246\n"
"Mvav 184,200,136\nDT 172,2,37888\nv_add_f32 v247, 1.0, v247\nMvav 184,204,140\nLD 244,40,84,3072\n"
"Mvav 184,208,144\ns_mov_b32 s84, 4456832\nDT 174,6,37888\nDT 176,2,38912\nMvav 184,212,148\n"
"s_addk_i32 m0, 4096\nDT 178,6,38912\nv_add_f32 v248, 1.0, v248\nMvav 184,216,152\nDT 180,2,39936\n"
"v_add_f32 v249, 1.0, v249\nMvav 184,220,156\nEX 250,110\nMvav 184,224,160\nDT 182,6,39936\nDT 184,3,31744\n"
"Mvaa 188,192,136\nEX 251,111\nMvaa 188,196,140\nDT 186,7,31744\nDT 188,3,32768\nMvaa 188,200,144\n"
"LD 244,40,84,0\nMvaa 188,204,148\ns_movk_i32 s84, 384\nDT 190,7,32768\nDT 192,3,33792\nMvaa 188,208,152\n"
"s_addk_i32 m0, 1024\nEX 252,112\nMvaa 188,212,156\nDT 194,7,33792\nDT 196,3,34816\nMvaa 188,216,160\nWL 6\n"
"EX 253,113\nMvaa 188,220,164\nDT 198,7,34816\nDT 200,3,35840\nMvaa 188,224,168\nRC 246,246\nMvav 192,228,128\n"
"DT 202,7,35840\nDT 204,3,36864\nMvav 192,232,132\nRC 247,247\nMvav 192,240,136\nDT 206,7,36864\nDT 208,3,37888\n"
"Mvav 192,244,140\nRC 248,248\nMvav 192,248,144\nDT 210,7,37888\nDT 212,3,38912\nMvav 192,252,148\nRC 249,249\n"
"Mvav 192,172,152\ns_waitcnt vmcnt(9)\nDT 214,7,38912\nDT 216,3,39936\nMvav 192,176,156\ns_barrier\n"
"s_bfm_b64 exec, 16, 0\nglobal_load_dword v175, v13, s[98:99] sc1\ns_mov_b64 exec, -1\nMvav 192,180,160\n"
"DT 218,7,39936\nDT 220,0,0\nMvaa 196,228,136\nDT 222,4,0\nv_add_f32 v250, 1.0, v250\nMvaa 196,232,140\n"
"DT 224,0,1024\nv_add_f32 v251, 1.0, v251\nMvaa 196,240,144\nLD 245,44,84,0\nMvaa 196,244,148\n"
"s_add_u32 s84, s84, s85\nDT 226,4,1024\nDT 228,0,2048\nMvaa 196,248,152\nDT 230,4,2048\n"
"v_add_f32 v252, 1.0, v252\nMvaa 196,252,156\nDT 232,0,3072\nv_add_f32 v253, 1.0, v253\nMvaa 196,172,160\nWL 6\n"
"s_nop 0\n"
"Lw26_%=:\ns_cbranch_execz Lhub_%=\nLD 245,44,84,1024\nMvaa 196,176,164\ns_add_u32 s84, s84, s85\nDT 234,4,3072\n"
"DT 240,0,4096\nMvaa 196,180,168\nDT 242,4,4096\nML 164,164,240\nMvav 200,184,128\nDT 244,0,5120\n"
"ML 165,165,241\nMvav 200,188,132\nDT 246,4,5120\nML 166,166,240\nMvav 200,192,136\nDT 248,0,6144\n"
"ML 167,167,241\nMvav 200,196,140\nRC 250,250\nMvav 200,200,144\nDT 250,4,6144\nDT 252,0,7168\nMvav 200,204,148\n"
"LD 245,44,84,2048\nMvav 200,208,152\ns_add_u32 s84, s84, s85\nDT 254,4,7168\nDT 172,0,8192\nMvav 200,212,156\n"
"RC 251,251\nMvav 200,216,160\nDT 174,4,8192\nDT 176,1,0\nMvaa 204,184,136\nRC 252,252\nMvaa 204,188,140\n"
"DT 178,5,0\nDT 180,1,1024\nMvaa 204,192,144\nLD 245,44,84,3072\nMvaa 204,196,148\ns_mov_b32 s84, 1180032\n"
"DT 182,5,1024\nDT 184,1,2048\nMvaa 204,200,152\ns_addk_i32 m0, 4096\nRC 253,253\nMvaa 204,204,156\n"
"DT 186,5,2048\nDT 188,1,3072\nMvaa 204,208,160\nWL 6\nDT 190,5,3072\nML 168,168,242\nML 169,169,243\n"
"ML 170,170,242\nML 171,171,243\nv_pk_mul_f32 v[228:229], v[228:229], v[254:255]\n"
"v_pk_mul_f32 v[230:231], v[230:231], v[20:21]\nv_pk_mul_f32 v[232:233], v[232:233], v[22:23]\n"
"v_pk_mul_f32 v[234:235], v[234:235], v[24:25]\nv_pk_mul_f32 v[236:237], v[236:237], v[28:29]\n"
"v_pk_mul_f32 v[238:239], v[238:239], v[30:31]\nv_pk_mul_f32 v[176:177], v[176:177], v[16:17]\n"
"v_pk_mul_f32 v[178:179], v[178:179], v[16:17]\nv_pk_mul_f32 v[180:181], v[180:181], v[18:19]\n"
"v_pk_mul_f32 v[182:183], v[182:183], v[18:19]\nv_pk_mul_f32 v[184:185], v[184:185], v[16:17]\n"
"v_pk_mul_f32 v[186:187], v[186:187], v[16:17]\nv_pk_mul_f32 v[188:189], v[188:189], v[18:19]\n"
"v_pk_mul_f32 v[190:191], v[190:191], v[18:19]\nv_pk_mul_f32 v[192:193], v[192:193], v[16:17]\n"
"v_pk_mul_f32 v[194:195], v[194:195], v[16:17]\nv_pk_mul_f32 v[196:197], v[196:197], v[18:19]\n"
"v_pk_mul_f32 v[198:199], v[198:199], v[18:19]\nv_pk_mul_f32 v[200:201], v[200:201], v[16:17]\n"
"v_pk_mul_f32 v[202:203], v[202:203], v[16:17]\nv_pk_mul_f32 v[104:105], v[104:105], v[26:27]\n"
"v_pk_mul_f32 v[106:107], v[106:107], v[246:247]\nv_pk_mul_f32 v[108:109], v[108:109], v[248:249]\n"
"v_pk_mul_f32 v[110:111], v[110:111], v[250:251]\nv_pk_mul_f32 v[112:113], v[112:113], v[252:253]\n"
"CV 228,228,229\nMvaa 204,212,164\nDT 192,1,4096\nCV 230,230,231\nMvaa 204,216,168\nDT 194,5,4096\nAW 92,228\n"
"Mvavz 208,220,32\nLD 245,44,84,0\nMvav 208,224,40\ns_add_u32 s84, s84, s85\nDT 196,1,5120\nDT 198,5,5120\n"
"Mvav 208,228,48\nDT 200,1,6144\nAW 93,230\nMvav 208,232,56\nDT 202,5,6144\nCV 232,232,233\nMvav 208,240,64\n"
"DT 204,1,7168\nCV 234,234,235\nMvav 208,244,72\nDT 206,5,7168\nAW 90,232\nMvav 208,248,80\nDT 208,1,8192\n"
"AW 91,234\nMvav 208,252,88\nLD 245,44,84,1024\nMvav 208,172,96\ns_add_u32 s84, s84, s85\nDT 210,5,8192\n"
"DT 212,2,0\nMvavz 212,220,36\nDT 214,6,0\nCV 236,236,237\nMvav 212,224,44\nDT 216,2,1024\nCV 238,238,239\n"
"Mvav 212,228,52\nDT 218,6,1024\nAW 94,236\nMvav 212,232,60\nDT 220,2,2048\nAW 95,238\nMvav 212,240,68\n"
"DT 222,6,2048\nCV 176,176,177\nMvav 212,244,76\nLD 245,44,84,2048\nMvav 212,248,84\nWL 5\nDT 224,2,3072\n"
"DT 226,6,3072\nMvav 212,252,92\ns_add_u32 s84, s84, s85\nDT 228,2,4096\nCV 178,178,179\nMvav 212,172,100\n"
"DT 230,6,4096\nAW 32,176\nMvavz 216,176,32\nDT 232,2,5120\nAW 33,178\nMvav 216,180,40\nDT 234,6,5120\n"
"CV 180,180,181\nMvav 216,184,48\nDT 240,2,6144\nCV 182,182,183\nMvav 216,188,56\nLD 245,44,84,3072\n"
"Mvav 216,192,64\ns_mov_b32 s84, 2359680\nDT 242,6,6144\nDT 244,2,7168\nMvav 216,196,72\ns_addk_i32 m0, 4096\n"
"DT 246,6,7168\nAW 48,180\nMvav 216,200,80\nDT 248,2,8192\nAW 49,182\nMvav 216,204,88\nDT 250,6,8192\n"
"CV 184,184,185\nMvav 216,208,96\nDT 252,3,0\nCV 186,186,187\nMvavz 220,176,36\nDT 254,7,0\nAW 34,184\n"
"Mvav 220,180,44\nLD 245,44,84,0\nMvav 220,184,52\ns_add_u32 s84, s84, s85\nDT 172,3,1024\nDT 174,7,1024\n"
"Mvav 220,188,60\nDT 176,3,2048\nAW 35,186\nMvav 220,192,68\nDT 178,7,2048\ns_waitcnt vmcnt(9)\n"
"v_cmp_eq_u32 vcc, v172, v175\nMvav 220,196,76\ns_and_b32 s92, vcc_lo, 0xffff\ns_cbranch_scc1 Lpr1s_%=\n"
"Lpr1r_%=:\nDT 180,3,3072\nv_and_b32 v175, 1, v175\nMvav 220,200,84\nWL 6\nDT 182,7,3072\n"
"v_cmp_eq_u32 vcc, 1, v175\nMvav 220,204,92\ns_and_b32 s78, vcc_lo, 0xffff\ns_mov_b64 exec, s[0:1]\n"
"global_atomic_add v13, v174, s[98:99]\ns_mov_b64 exec, -1\nMvav 220,208,100\ns_bcnt1_i32_b32 s78, s78\n"
"DT 184,3,4096\nDT 186,7,4096\nMvavz 224,212,32\ns_cmp_ge_u32 s78, 9\ns_cselect_b64 vcc, -1, 0\nDT 188,3,5120\n"
"CV 188,188,189\nMvav 224,216,40\ns_and_b32 s78, vcc_lo, 0x2000\nDT 190,7,5120\nCV 190,190,191\nMvav 224,220,48\n"
"s_or_b32 s60, s60, s78\nDT 192,3,6144\nAW 50,188\nMvav 224,224,56\ns_or_b32 s64, s64, s78\nDT 194,7,6144\n"
"AW 51,190\nMvav 224,228,64\ns_or_b32 s68, s68, s78\nDT 196,3,7168\nCV 192,192,193\nMvav 224,232,72\n"
"s_or_b32 s98, s98, s78\nDT 198,7,7168\nCV 194,194,195\nMvav 224,240,80\ns_barrier\nDT 200,3,8192\nAW 36,192\n"
"Mvav 224,244,88\nLD 245,44,84,1024\nMvav 224,248,96\ns_add_u32 s84, s84, s85\nDT 202,7,8192\nDT 204,0,13312\n"
"Mvavz 228,212,36\nDT 206,4,13312\nAW 37,194\nMvav 228,216,44\nDT 208,0,14336\nCV 196,196,197\nMvav 228,220,52\n"
"DT 210,4,14336\nCV 198,198,199\nMvav 228,224,60\nDT 212,0,15360\nAW 52,196\nMvav 228,228,68\nDT 214,4,15360\n"
"AW 53,198\nMvav 228,232,76\nLD 245,44,84,2048\nMvav 228,240,84\nWL 5\nDT 216,0,16384\nDT 218,4,16384\n"
"Mvav 228,244,92\ns_add_u32 s84, s84, s85\nDT 220,0,17408\nCV 200,200,201\nMvav 228,248,100\nDT 222,4,17408\n"
"CV 202,202,203\nMvavz 232,252,32\nDT 224,0,18432\nAW 38,200\nMvav 232,172,40\nDT 226,4,18432\nAW 39,202\n"
"Mvav 232,176,48\nEX 20,116\nMvav 232,180,56\nDT 228,0,19456\nDT 230,4,19456\nMvav 232,184,64\n"
"LD 245,44,84,3072\nMvav 232,188,72\ns_mov_b32 s84, 3539328\nDT 232,0,20480\nDT 234,4,20480\nMvav 232,192,80\n"
"s_addk_i32 m0, 4096\nEX 21,117\nMvav 232,196,88\nDT 240,0,21504\nDT 242,4,21504\nMvav 232,200,96\n"
"DT 244,1,13312\nv_add_f32 v20, 1.0, v20\nMvavz 236,252,36\nDT 246,5,13312\nv_add_f32 v21, 1.0, v21\n"
"Mvav 236,172,44\nLD 245,44,84,0\nMvav 236,176,52\ns_add_u32 s84, s84, s85\nDT 248,1,14336\nDT 250,5,14336\n"
"Mvav 236,180,60\nRC 20,20\nMvav 236,184,68\nDT 252,1,15360\nDT 254,5,15360\nMvav 236,188,76\nRC 21,21\n"
"Mvav 236,192,84\nWL 5\nDT 172,1,16384\nDT 174,5,16384\nMvav 236,196,92\nEX 22,118\nMvav 236,200,100\n"
"DT 176,1,17408\nDT 178,5,17408\nMvav 208,204,128\nLD 245,44,84,1024\nMvav 208,208,132\ns_add_u32 s84, s84, s85\n"
"DT 180,1,18432\nDT 182,5,18432\nMvav 208,212,136\nEX 23,119\nMvav 208,216,140\nDT 184,1,19456\nDT 186,5,19456\n"
"Mvav 208,220,144\nEX 24,120\nMvav 208,224,148\nDT 188,1,20480\nDT 190,5,20480\nMvav 208,228,152\n"
"LD 245,44,84,2048\nMvav 208,232,156\ns_add_u32 s84, s84, s85\nDT 192,1,21504\nDT 194,5,21504\nMvav 208,240,160\n"
"EX 25,121\nMvaa 212,204,136\nDT 196,2,13312\nDT 198,6,13312\nMvaa 212,208,140\nDT 200,2,14336\n"
"v_add_f32 v22, 1.0, v22\nMvaa 212,212,144\nDT 202,6,14336\nv_add_f32 v23, 1.0, v23\nMvaa 212,216,148\nRC 22,22\n"
"Mvaa 212,220,152\nDT 204,2,15360\nDT 206,6,15360\nMvaa 212,224,156\nLD 245,44,84,3072\nMvaa 212,228,160\nWL 6\n"
"DT 208,2,16384\nDT 210,6,16384\nMvaa 212,232,164\ns_addk_i32 m0, 4096\nRC 23,23\nMvaa 212,240,168\n"
"s_mov_b32 s84, 4718976\nDT 212,2,17408\nDT 214,6,17408\nMvav 216,244,128\nDT 216,2,18432\n"
"v_add_f32 v24, 1.0, v24\nMvav 216,248,132\nDT 218,6,18432\nv_add_f32 v25, 1.0, v25\nMvav 216,252,136\n"
"LD 245,44,84,0\nMvav 216,172,140\ns_add_u32 s84, s84, s85\nDT 220,2,19456\nDT 222,6,19456\nMvav 216,176,144\n"
"s_mov_b32 s85, 35840\nRC 24,24\nMvav 216,180,148\nDT 224,2,20480\nDT 226,6,20480\nMvav 216,184,152\nRC 25,25\n"
"Mvav 216,188,156\nDT 228,2,21504\nDT 230,6,21504\nMvav 216,192,160\nEX 26,126\nMvaa 220,244,136\n"
"DT 232,3,13312\nDT 234,7,13312\nMvaa 220,248,140\nLD 245,44,84,1024\nMvaa 220,252,144\ns_movk_i32 s84, 0\n"
"DT 240,3,14336\nDT 242,7,14336\nMvaa 220,172,148\ns_add_u32 m0, s73, 9024\nEX 27,127\nMvaa 220,176,152\n"
"DT 244,3,15360\nDT 246,7,15360\nMvaa 220,180,156\nEX 28,122\nMvaa 220,184,160\nWL 6\nDT 248,3,16384\n"
"DT 250,7,16384\nMvaa 220,188,164\nEX 29,123\nMvaa 220,192,168\nDT 252,3,17408\nDT 254,7,17408\n"
"Mvav 224,196,128\nDT 172,3,18432\nv_add_f32 v26, 1.0, v26\nMvav 224,200,132\nDT 174,7,18432\n"
"v_add_f32 v27, 1.0, v27\nMvav 224,204,136\nRC 26,26\nMvav 224,208,140\nDT 176,3,19456\nDT 178,7,19456\n"
"Mvav 224,212,144\nRC 27,27\nMvav 224,216,148\nDT 180,3,20480\nDT 182,7,20480\nMvav 224,220,152\n"
"s_waitcnt vmcnt(10)\nDT 184,3,21504\nv_add_f32 v28, 1.0, v28\nMvav 224,224,156\ns_barrier\nDT 186,7,21504\n"
"v_add_f32 v29, 1.0, v29\nMvav 224,228,160\nLD 10,52,84,0\nMvaa 228,196,136\ns_movk_i32 s84, 3072\n"
"DT 188,0,22528\nDT 190,4,22528\nMvaa 228,200,140\nRC 28,28\nMvaa 228,204,144\nDT 192,0,23552\nDT 194,4,23552\n"
"Mvaa 228,208,148\nRC 29,29\nMvaa 228,212,152\nDT 196,0,24576\nDT 198,4,24576\nMvaa 228,216,156\nEX 30,124\n"
"Mvaa 228,220,160\nWL 6\nDT 200,0,25600\nDT 202,4,25600\nMvaa 228,224,164\nLD 10,52,84,1024\nMvaa 228,228,168\n"
"s_movk_i32 s84, 6144\nDT 204,0,26624\nDT 206,4,26624\nMvav 232,232,128\nEX 31,125\nMvav 232,240,132\n"
"DT 208,0,27648\nDT 210,4,27648\nMvav 232,244,136\nDT 212,0,28672\nv_add_f32 v30, 1.0, v30\nMvav 232,248,140\n"
"DT 214,4,28672\nv_add_f32 v31, 1.0, v31\nMvav 232,252,144\nLD 10,52,84,2048\nMvav 232,172,148\n"
"s_movk_i32 s84, 9216\nDT 216,0,29696\nDT 218,4,29696\nMvav 232,176,152\nRC 30,30\nMvav 232,180,156\n"
"DT 220,0,30720\nDT 222,4,30720\nMvav 232,184,160\nRC 31,31\nMvaa 236,232,136\nDT 224,1,22528\nDT 226,5,22528\n"
"Mvaa 236,240,140\nEX 246,164\nMvaa 236,244,144\nDT 228,1,23552\nDT 230,5,23552\nMvaa 236,248,148\n"
"LD 10,52,84,3072\nMvaa 236,252,152\ns_movk_i32 s84, 16384\nDT 232,1,24576\nDT 234,5,24576\nMvaa 236,172,156\n"
"s_addk_i32 m0, -4928\nEX 247,165\nMvaa 236,176,160\nWL 6\nDT 240,1,25600\nDT 242,5,25600\nMvaa 236,180,164\n"
"EX 248,166\nMvaa 236,184,168\nDT 244,1,26624\nDT 246,5,26624\nMvavz 176,188,32\nLD 10,52,84,0\nMvav 176,192,40\n"
"s_movk_i32 s84, 19456\nDT 248,1,27648\nDT 250,5,27648\nMvav 176,196,48\nEX 249,167\nMvav 176,200,56\n"
"DT 252,1,28672\nDT 254,5,28672\nMvav 176,204,64\nDT 172,1,29696\nv_cndmask_b32 v175, v172, v173, vcc\n"
"Mvav 176,208,72\nDT 174,5,29696\nv_add_f32 v246, 1.0, v246\nMvav 176,212,80\nDT 176,1,30720\n"
"v_add_f32 v247, 1.0, v247\nMvav 176,216,88\nLD 10,52,84,1024\nMvav 176,220,96\ns_movk_i32 s84, 22528\n"
"DT 178,5,30720\nDT 180,2,22528\nMvavz 180,188,36\nDT 182,6,22528\nv_add_f32 v248, 1.0, v248\nMvav 180,192,44\n"
"DT 184,2,23552\nv_add_f32 v249, 1.0, v249\nMvav 180,196,52\nEX 250,168\nMvav 180,200,60\nDT 186,6,23552\n"
"DT 188,2,24576\nMvav 180,204,68\nEX 251,169\nMvav 180,208,76\nDT 190,6,24576\nDT 192,2,25600\n"
"Lw27_%=:\ns_cbranch_execz Lhub_%=\nMvav 180,212,84\nWL 6\nLD 10,52,84,2048\nMvav 180,216,92\n"
"s_movk_i32 s84, 25600\nDT 194,6,25600\nDT 196,2,26624\nMvav 180,220,100\nEX 252,170\nMvavz 184,224,32\n"
"DT 198,6,26624\nDT 200,2,27648\nMvav 184,228,40\nEX 253,171\nMvav 184,232,48\nDT 202,6,27648\nDT 204,2,28672\n"
"Mvav 184,240,56\nLD 10,52,84,3072\nMvav 184,244,64\ns_movk_i32 s84, 0\nDT 206,6,28672\nDT 208,2,29696\n"
"Mvav 184,248,72\ns_add_u32 m0, s72, 0\nRC 246,246\nMvav 184,252,80\nDT 210,6,29696\nDT 212,2,30720\n"
"Mvav 184,172,88\nRC 247,247\nMvav 184,176,96\nDT 214,6,30720\nDT 216,3,22528\nMvavz 188,224,36\nRC 248,248\n"
"Mvav 188,228,44\nDT 218,7,22528\nDT 220,3,23552\nMvav 188,232,52\nLD 11,56,84,0\nMvav 188,240,60\n"
"s_movk_i32 s84, 7168\nDT 222,7,23552\nDT 224,3,24576\nMvav 188,244,68\nRC 249,249\nMvav 188,248,76\n"
"DT 226,7,24576\nDT 228,3,25600\nMvav 188,252,84\nWL 6\nDT 230,7,25600\nv_add_f32 v250, 1.0, v250\n"
"Mvav 188,172,92\nDT 232,3,26624\nv_add_f32 v251, 1.0, v251\nMvav 188,176,100\nLD 11,56,84,1024\n"
"Mvavz 192,180,32\ns_movk_i32 s84, 128\nDT 234,7,26624\nDT 240,3,27648\nMvav 192,184,40\ns_addk_i32 m0, 2048\n"
"DT 242,7,27648\nv_add_f32 v252, 1.0, v252\nMvav 192,188,48\nDT 244,3,28672\nv_add_f32 v253, 1.0, v253\n"
"Mvav 192,192,56\nRC 250,250\nMvav 192,196,64\nDT 246,7,28672\nDT 248,3,29696\nMvav 192,200,72\nRC 251,251\n"
"Mvav 192,204,80\nDT 250,7,29696\nDT 252,3,30720\nMvav 192,208,88\nLD 11,56,84,0\nMvav 192,212,96\n"
"s_movk_i32 s84, 7296\nDT 254,7,30720\nML 114,114,240\nMvavz 196,180,36\nRC 252,252\nMvav 196,184,44\n"
"RC 253,253\nMvav 196,188,52\ns_waitcnt vmcnt(7)\nML 115,115,241\nCV 104,104,105\nMvav 196,192,60\ns_barrier\n"
"EX 254,114\nMvav 196,196,68\nDK 172,8,9024\nv_add_f32 v254, 1.0, v254\nMvav 196,200,76\nLD 11,56,84,1024\n"
"Mvav 196,204,84\nWL 1\nDK 174,9,9024\nDK 176,8,9088\nMvav 196,208,92\ns_add_u32 m0, s73, 17408\nEX 255,115\n"
"Mvav 196,212,100\ns_addk_i32 s84, 25472\nDK 178,9,9088\nDK 180,8,9152\nMvavz 200,216,32\nLD 10,52,84,0\n"
"Mvav 200,220,40\ns_addk_i32 s84, 3072\nDK 182,9,9152\nDK 184,8,9216\nMvav 200,224,48\nDK 186,9,9216\n"
"v_add_f32 v255, 1.0, v255\nMvav 200,228,56\nRC 254,254\nMvav 200,232,64\nLD 10,52,84,1024\nMvav 200,240,72\n"
"s_addk_i32 s84, 3072\nRC 255,255\nAW 96,104\nCV 112,112,113\nAW 98,112\n"
"v_pk_mul_f32 v[204:205], v[204:205], v[18:19]\nv_pk_mul_f32 v[206:207], v[206:207], v[18:19]\n"
"v_pk_mul_f32 v[208:209], v[208:209], v[16:17]\nv_pk_mul_f32 v[210:211], v[210:211], v[16:17]\n"
"v_pk_mul_f32 v[216:217], v[216:217], v[16:17]\nv_pk_mul_f32 v[218:219], v[218:219], v[16:17]\n"
"v_pk_mul_f32 v[224:225], v[224:225], v[16:17]\nv_pk_mul_f32 v[226:227], v[226:227], v[16:17]\n"
"v_pk_mul_f32 v[232:233], v[232:233], v[16:17]\nv_pk_mul_f32 v[234:235], v[234:235], v[16:17]\n"
"v_pk_mul_f32 v[212:213], v[212:213], v[18:19]\nv_pk_mul_f32 v[214:215], v[214:215], v[18:19]\n"
"v_pk_mul_f32 v[220:221], v[220:221], v[18:19]\nv_pk_mul_f32 v[222:223], v[222:223], v[18:19]\n"
"v_pk_mul_f32 v[228:229], v[228:229], v[18:19]\nv_pk_mul_f32 v[230:231], v[230:231], v[18:19]\n"
"v_pk_mul_f32 v[236:237], v[236:237], v[18:19]\nv_pk_mul_f32 v[238:239], v[238:239], v[18:19]\n"
"v_pk_mul_f32 v[116:117], v[116:117], v[20:21]\nv_pk_mul_f32 v[118:119], v[118:119], v[22:23]\n"
"v_pk_mul_f32 v[120:121], v[120:121], v[24:25]\nv_pk_mul_f32 v[126:127], v[126:127], v[26:27]\n"
"v_pk_mul_f32 v[122:123], v[122:123], v[28:29]\nv_pk_mul_f32 v[124:125], v[124:125], v[30:31]\n"
"v_pk_mul_f32 v[164:165], v[164:165], v[246:247]\nv_pk_mul_f32 v[166:167], v[166:167], v[248:249]\n"
"v_pk_mul_f32 v[168:169], v[168:169], v[250:251]\nv_pk_mul_f32 v[170:171], v[170:171], v[252:253]\n"
"v_pk_mul_f32 v[114:115], v[114:115], v[254:255]\nCV 204,204,205\nMvav 200,244,80\nDT 188,0,31744\n"
"CV 206,206,207\nMvav 200,248,88\nDT 190,4,31744\nAW 54,204\nMvav 200,252,96\nDT 192,0,32768\nAW 55,206\n"
"Mvavz 204,216,36\nDT 194,4,32768\nCV 208,208,209\nMvav 204,220,44\nDT 196,0,33792\nCV 210,210,211\n"
"Mvav 204,224,52\nDT 198,4,33792\nCV 216,216,217\nMvav 204,228,60\nDT 200,0,34816\nCV 218,218,219\n"
"Mvav 204,232,68\nDT 202,4,34816\nAW 40,208\nMvav 204,240,76\nDT 204,0,35840\nAW 41,210\nMvav 204,244,84\nWL 6\n"
"DT 206,4,35840\nAW 42,216\nMvav 204,248,92\nDT 208,0,36864\nAW 43,218\nMvav 204,252,100\nDT 210,4,36864\n"
"CV 224,224,225\nMvaaz 32,172,0\nDT 212,0,37888\nCV 226,226,227\nMvaa 32,176,4\nDT 214,4,37888\nCV 232,232,233\n"
"Mvaa 32,180,8\nDT 216,0,38912\nCV 234,234,235\nMvaa 32,184,12\nDT 218,4,38912\nAW 44,224\nMvaaz 36,172,16\n"
"DT 220,0,39936\nAW 45,226\nMvaa 36,176,20\nDT 222,4,39936\nAW 46,232\nMvaa 36,180,24\nDT 224,1,31744\n"
"AW 47,234\nMvaa 36,184,28\nDT 226,5,31744\nCV 212,212,213\nMvaaz 40,172,32\nDT 228,1,32768\nCV 214,214,215\n"
"Mvaa 40,176,36\nDT 230,5,32768\nCV 220,220,221\nMvaa 40,180,40\nDT 232,1,33792\nCV 222,222,223\nMvaa 40,184,44\n"
"DT 234,5,33792\nAW 56,212\nMvaaz 44,172,48\nDT 240,1,34816\nAW 57,214\nMvaa 44,176,52\nDT 242,5,34816\n"
"AW 58,220\nAW 59,222\ns_nop 1\nMvaa 44,180,56\nWL 6\nDT 244,1,35840\nCV 228,228,229\nCV 230,230,231\n"
"CV 236,236,237\nCV 238,238,239\nAW 60,228\nAW 61,230\nAW 62,236\nAW 63,238\ns_nop 1\nMvaa 44,184,60\n"
"DT 246,5,35840\nv_max3_f32 v16, v32, v33, v34\nMvav 176,188,128\nDT 248,1,36864\nv_max_f32 v17, v16, v35\n"
"Mvav 176,192,132\nDT 250,5,36864\nv_max_f32 v16, v16, v35\nMvav 176,196,136\nDT 252,1,37888\n"
"v_max3_f32 v20, v36, v37, v38\nMvav 176,200,140\nv_permlane32_swap_b32 v16, v17\nMvav 176,204,144\n"
"DT 254,5,37888\nDT 172,1,38912\nMvav 176,208,148\nDT 174,5,38912\nv_max_f32 v18, v16, v17\nMvav 176,212,152\n"
"DT 176,1,39936\nv_max_f32 v19, v16, v17\nMvav 176,216,156\nDT 178,5,39936\nv_max_f32 v21, v20, v39\n"
"Mvav 176,220,160\nv_permlane16_swap_b32 v18, v19\nMvaa 180,188,136\nv_max_f32 v20, v20, v39\n"
"v_min_f32 v16, -v18, -v19\nMvaa 180,192,140\nv_min_f32 v17, -v18, -v19\nv_mov_b32 v18, v16\nMvaa 180,196,144\n"
"v_mov_b32 v19, v16\nv_max3_f32 v24, v40, v41, v42\nMvaa 180,200,148\nv_permlane32_swap_b32 v20, v21\n"
"Mvaa 180,204,152\nDK 180,8,10048\nv_max_f32 v22, v20, v21\nMvaa 180,208,156\nDK 182,9,10048\n"
"v_max_f32 v23, v20, v21\nMvaa 180,212,160\nDK 184,8,10112\nv_max_f32 v25, v24, v43\nMvaa 180,216,164\nWL 3\n"
"v_permlane16_swap_b32 v22, v23\nMvaa 180,220,168\nDK 186,9,10112\nDK 188,8,10176\nMvav 184,224,128\n"
"DK 190,9,10176\nv_max_f32 v24, v24, v43\nMvav 184,228,132\nDK 192,8,10240\nv_min_f32 v20, -v22, -v23\n"
"Mvav 184,232,136\nDK 194,9,10240\nv_min_f32 v21, -v22, -v23\nMvav 184,240,140\nv_mov_b32 v22, v20\n"
"v_mov_b32 v23, v20\nMvav 184,244,144\nv_permlane32_swap_b32 v24, v25\nMvav 184,248,148\n"
"v_max_f32 v26, v24, v25\nv_max_f32 v27, v24, v25\nMvav 184,252,152\nDT 196,2,31744\n"
"v_max3_f32 v28, v44, v45, v46\nMvav 184,172,156\nv_permlane16_swap_b32 v26, v27\nMvav 184,176,160\n"
"DT 198,6,31744\nDT 200,2,32768\nMvaa 188,224,136\nDT 202,6,32768\nv_max_f32 v29, v28, v47\nMvaa 188,228,140\n"
"DT 204,2,33792\nv_max_f32 v28, v28, v47\nMvaa 188,232,144\nDT 206,6,33792\nv_min_f32 v24, -v26, -v27\n"
"Mvaa 188,240,148\nDT 208,2,34816\nv_min_f32 v25, -v26, -v27\nMvaa 188,244,152\nDT 210,6,34816\n"
"v_mov_b32 v26, v24\nMvaa 188,248,156\nDT 212,2,35840\nv_mov_b32 v27, v24\nMvaa 188,252,160\nWL 6\n"
"v_permlane32_swap_b32 v28, v29\nMvaa 188,172,164\nDT 214,6,35840\nDT 216,2,36864\nMvaa 188,176,168\n"
"DT 218,6,36864\nv_max_f32 v30, v28, v29\nMvaac 48,180,0,16\nDT 220,2,37888\nv_max_f32 v31, v28, v29\n"
"Mvaa 48,184,4\nLD 10,52,84,2048\nMvaa 48,188,8\ns_addk_i32 s84, 3072\nDT 222,6,37888\nDT 224,2,38912\n"
"Mvaa 48,192,12\nv_permlane16_swap_b32 v30, v31\nMvaac 52,180,16,20\nDT 226,6,38912\nv_min_f32 v28, -v30, -v31\n"
"Mvaa 52,184,20\nDT 228,2,39936\nv_min_f32 v29, -v30, -v31\nMvaa 52,188,24\nDT 230,6,39936\nv_mov_b32 v30, v28\n"
"Mvaa 52,192,28\nv_mov_b32 v31, v28\nCV 164,164,165\nMvaac 56,180,32,24\nEX 48,48\nMvaa 56,184,36\nEX 49,49\n"
"Mvaa 56,188,40\nEX 50,50\nMvaa 56,192,44\nDK 232,8,11072\nCV 66,48,49\nMvaac 60,180,48,28\nEX 51,51\n"
"Mvaa 60,184,52\nDK 234,9,11072\nDK 240,8,11136\nMvaa 60,188,56\nWL 3\nDK 242,9,11136\nCV 67,50,51\n"
"Mvaa 60,192,60\nEX 52,52\nMvav 192,196,128\nDK 244,8,11200\nDK 246,9,11200\nMvav 192,200,132\nEX 53,53\n"
"Mvav 192,204,136\nDK 248,8,11264\nDK 250,9,11264\nMvav 192,208,140\nEX 54,54\nMvav 192,212,144\nEX 55,55\n"
"Mvav 192,216,148\nCV 74,52,53\nCV 75,54,55\nMvav 192,220,152\nDT 252,3,31744\nCV 166,166,167\nMvav 192,224,156\n"
"EX 56,56\nMvav 192,228,160\nDT 254,7,31744\nDT 172,3,32768\nMvaa 196,196,136\nEX 57,57\nMvaa 196,200,140\n"
"DT 174,7,32768\nDT 176,3,33792\nMvaa 196,204,144\nEX 58,58\nMvaa 196,208,148\nDT 178,7,33792\nDT 180,3,34816\n"
"Mvaa 196,212,152\nEX 59,59\nMvaa 196,216,156\nDT 182,7,34816\nDT 184,3,35840\nMvaa 196,220,160\nWL 6\n"
"DT 186,7,35840\nCV 82,56,57\nMvaa 196,224,164\nDT 188,3,36864\nCV 83,58,59\nEX 60,60\nAW 106,164\n"
"v_pk_add_f32 v[32:33], v[32:33], v[16:17]\nv_pk_add_f32 v[34:35], v[34:35], v[16:17]\n"
"v_pk_add_f32 v[36:37], v[36:37], v[20:21]\nv_pk_add_f32 v[38:39], v[38:39], v[20:21]\n"
"v_pk_add_f32 v[40:41], v[40:41], v[24:25]\nv_pk_add_f32 v[42:43], v[42:43], v[24:25]\n"
"v_pk_add_f32 v[44:45], v[44:45], v[28:29]\nv_pk_add_f32 v[46:47], v[46:47], v[28:29]\n"
"v_pk_mul_f32 v[186:187], v[186:187], v[240:241]\nv_pk_mul_f32 v[176:177], v[176:177], v[240:241]\n"
"v_pk_mul_f32 v[178:179], v[178:179], v[240:241]\nv_pk_mul_f32 v[180:181], v[180:181], v[242:243]\n"
"v_pk_mul_f32 v[182:183], v[182:183], v[242:243]\nv_pk_mul_f32 v[184:185], v[184:185], v[240:241]\n"
"v_pk_mul_f32 v[188:189], v[188:189], v[242:243]\nv_pk_mul_f32 v[190:191], v[190:191], v[242:243]\n"
"v_pk_mul_f32 v[192:193], v[192:193], v[240:241]\nv_pk_mul_f32 v[194:195], v[194:195], v[240:241]\nAW 107,166\n"
"Mvaa 196,228,168\nEX 32,32\nEX 33,33\nEX 34,34\nEX 35,35\nCV 64,32,33\nCV 65,34,35\nMvaac 32,232,0,16\n"
"DT 190,7,36864\nDT 192,3,37888\nMvaa 32,240,4\nEX 36,36\nMvaa 32,244,8\nDT 194,7,37888\nDT 196,3,38912\n"
"EX 37,37\nEX 38,38\nMvaa 32,248,12\nEX 39,39\nCV 72,36,37\nCV 73,38,39\nMvaac 36,232,16,20\nDT 198,7,38912\n"
"DT 200,3,39936\nMvaa 36,240,20\nEX 40,40\nMvaa 36,244,24\nDT 202,7,39936\nCV 168,168,169\nEX 41,41\nEX 42,42\n"
"Mvaa 36,248,28\nEX 43,43\nCV 80,40,41\nCV 81,42,43\nMvaac 40,232,32,24\nEX 44,44\nMvaa 40,240,36\nEX 45,45\n"
"Mvaa 40,244,40\nEX 46,46\nEX 47,47\nMvaa 40,248,44\nDK 204,8,12096\nCV 88,44,45\nCV 89,46,47\n"
"Mvaac 44,232,48,28\nEX 61,61\nMvaa 44,240,52\nDK 206,9,12096\nDK 208,8,12160\nMvaa 44,244,56\nWL 3\nEX 62,62\n"
"Mvaa 44,248,60\nDK 210,9,12160\nDK 212,8,12224\nMvav 200,252,128\nEX 63,63\nMvav 200,172,132\nDK 214,9,12224\n"
"DK 216,8,12288\nMvav 200,176,136\nDK 218,9,12288\nCV 90,60,61\nMvav 200,180,140\nCV 91,62,63\nCV 170,170,171\n"
"Mvav 200,184,144\nEX 32,32\nMvav 200,188,148\nEX 33,33\nMvav 200,192,152\nEX 34,34\nMvav 200,196,156\nEX 35,35\n"
"Mvav 200,200,160\nCV 68,32,33\nCV 69,34,35\nMvaa 204,252,136\ns_waitcnt vmcnt(3)\nEX 36,36\nMvaa 204,172,140\n"
"s_barrier\nEX 37,37\nMvaa 204,176,144\nDK 220,8,4096\nCV 76,36,37\nMvaa 204,180,148\nEX 38,38\n"
"Mvaa 204,184,152\nDK 222,9,4096\nDK 224,8,4160\nMvaa 204,188,156\nEX 39,39\nMvaa 204,192,160\nWL 3\n"
"DK 226,9,4160\nDK 228,8,4224\nMvaa 204,196,164\nDK 230,9,4224\nCV 77,38,39\ns_nop 0\n"
"Lw28_%=:\ns_cbranch_execz Lhub_%=\nMvaa 204,200,168\nEX 40,40\nMvaac 48,204,0,16\nDK 232,8,4288\nDK 234,9,4288\n"
"Mvaa 48,208,4\nEX 41,41\nMvaa 48,212,8\nEX 42,42\nMvaa 48,216,12\nEX 43,43\nMvaac 52,204,16,20\nCV 84,40,41\n"
"CV 85,42,43\nMvaa 52,208,20\nEX 44,44\nMvaa 52,212,24\nEX 45,45\nMvaa 52,216,28\nEX 46,46\nMvaac 56,204,32,24\n"
"EX 47,47\nMvaa 56,208,36\nCV 92,44,45\nCV 93,46,47\nMvaa 56,212,40\nEX 48,48\nMvaa 56,216,44\nDT 192,0,0\n"
"AW 110,168\nMvaac 60,204,48,28\nEX 49,49\nMvaa 60,208,52\nWL 1\nDT 194,4,0\nDT 196,0,1024\nMvaa 60,212,56\n"
"EX 50,50\nMvaa 60,216,60\nDT 198,4,1024\nCV 70,48,49\nMvaac 32,220,0,16\nEX 51,51\nMvaa 32,224,4\nCV 71,50,51\n"
"AW 111,170\nMvaa 32,228,8\nEX 52,52\nMvaa 32,232,12\nDT 200,1,0\nAW 190,175\nMvaac 36,220,16,20\nEX 53,53\n"
"Mvaa 36,224,20\nDT 202,5,0\nDT 204,1,1024\nMvaa 36,228,24\nEX 54,54\nMvaa 36,232,28\nDT 206,5,1024\n"
"CV 78,52,53\nMvaac 40,220,32,24\nEX 55,55\nMvaa 40,224,36\nDK 208,8,5120\nCV 79,54,55\nMvaa 40,228,40\n"
"EX 56,56\nMvaa 40,232,44\nDK 210,9,5120\nDK 212,8,5184\nMvaac 44,220,48,28\nEX 57,57\nMvaa 44,224,52\nWL 4\n"
"DK 214,9,5184\nDK 216,8,5248\nMvaa 44,228,56\nEX 58,58\nMvaa 44,232,60\nDK 218,9,5248\nDK 220,8,5312\n"
"Mvavz 128,192,64\nEX 59,59\nMvav 128,196,68\nDK 222,9,5312\nCV 86,56,57\nMvavz 132,192,72\nCV 87,58,59\n"
"CV 120,120,121\nMvav 132,196,76\nEX 60,60\nMvavz 136,192,80\nEX 61,61\nMvav 136,196,84\nEX 62,62\nEX 63,63\n"
"Mvavz 140,192,88\nWL 1\nLD 10,52,84,3072\nCV 94,60,61\nCV 95,62,63\ns_nop 1\nMvav 140,196,92\n"
"s_add_u32 m0, s72, 13312\nEX 172,186\nMvavz 144,200,64\ns_movk_i32 s84, 16384\nEX 173,187\nMvav 144,204,68\n"
"EX 246,176\nMvavz 148,200,72\nLD 11,56,84,0\nMvav 148,204,76\ns_movk_i32 s84, 23552\nDT 224,2,0\nAW 104,120\n"
"Mvavz 152,200,80\nEX 247,177\nMvav 152,204,84\nDT 226,6,0\nDT 228,2,1024\nMvavz 156,200,88\nLD 11,56,84,1024\n"
"Mvav 156,204,92\ns_movk_i32 s84, 14464\nDT 230,6,1024\nCV 106,106,107\nMvaac 48,208,0,16\nWL 4\nEX 248,178\n"
"Mvaa 48,212,4\nEX 249,179\nMvaa 48,216,8\nLD 11,56,84,2048\nMvaa 48,220,12\ns_movk_i32 s84, 21632\nDT 232,3,0\n"
"AW 97,106\nMvaac 52,208,16,20\nEX 250,180\nMvaa 52,212,20\nDT 234,7,0\nDT 192,3,1024\nMvaa 52,216,24\n"
"LD 11,56,84,3072\nMvaa 52,220,28\ns_addk_i32 s84, 27520\nDT 194,7,1024\nCV 114,114,115\nMvaac 56,208,32,24\n"
"s_add_u32 m0, s73, 26624\nEX 251,181\nMvaa 56,212,36\nDK 196,8,6144\nAW 99,114\nMvaa 56,216,40\nEX 252,182\n"
"Mvaa 56,220,44\nDK 198,9,6144\nDK 200,8,6208\nMvaac 60,208,48,28\nEX 253,183\nMvaa 60,212,52\nWL 4\n"
"DK 202,9,6208\nDK 204,8,6272\nMvaa 60,216,56\nEX 254,184\nMvaa 60,220,60\nDK 206,9,6272\nDK 208,8,6336\n"
"Mvavz 160,224,64\nEX 255,185\nEX 32,32\nCV 122,122,123\nv_pk_add_f32 v[172:173], v[172:173], s[4:5]\n"
"v_pk_mul_f32 v[198:199], v[198:199], v[242:243]\nv_pk_add_f32 v[246:247], v[246:247], s[4:5]\n"
"v_pk_add_f32 v[248:249], v[248:249], s[4:5]\nv_pk_add_f32 v[250:251], v[250:251], s[4:5]\n"
"v_pk_add_f32 v[252:253], v[252:253], s[4:5]\nv_pk_add_f32 v[254:255], v[254:255], s[4:5]\n"
"v_pk_mul_f32 v[196:197], v[196:197], v[242:243]\nv_pk_mul_f32 v[200:201], v[200:201], v[240:241]\n"
"v_pk_mul_f32 v[202:203], v[202:203], v[240:241]\nv_pk_mul_f32 v[204:205], v[204:205], v[242:243]\n"
"v_pk_mul_f32 v[206:207], v[206:207], v[242:243]\nAW 105,122\nMvav 160,228,68\nDK 210,9,6336\nCV 108,108,109\n"
"RC 172,172\nRC 173,173\nMvavz 164,224,72\nRC 246,246\nRC 247,247\nAW 100,108\n"
"v_pk_mul_f32 v[186:187], v[186:187], v[172:173]\nv_pk_mul_f32 v[176:177], v[176:177], v[246:247]\n"
"CV 176,176,177\nMvav 164,228,76\nEX 172,198\nEX 173,199\nMvavz 168,224,80\nRC 248,248\nRC 249,249\nAW 112,176\n"
"v_pk_add_f32 v[172:173], v[172:173], s[4:5]\nv_pk_mul_f32 v[178:179], v[178:179], v[248:249]\nCV 178,178,179\n"
"Mvav 168,228,84\nRC 172,172\nRC 173,173\ns_nop 0\nv_pk_mul_f32 v[198:199], v[198:199], v[172:173]\ns_nop 0\n"
"Mvavz 172,224,88\nWL 1\nRC 250,250\nMvav 172,228,92\nRC 251,251\nRC 252,252\nRC 253,253\nAW 113,178\n"
"Mvavz 176,232,64\nEX 246,188\nMvav 176,192,68\nEX 247,189\nEX 248,190\nCV 186,186,187\n"
"v_pk_mul_f32 v[180:181], v[180:181], v[250:251]\nv_pk_mul_f32 v[182:183], v[182:183], v[252:253]\n"
"v_pk_add_f32 v[246:247], v[246:247], s[4:5]\nCV 180,180,181\nCV 182,182,183\nAW 116,180\nAW 117,182\n"
"Mvavz 180,232,72\nEX 249,191\nRC 254,254\nRC 255,255\nMvav 180,192,76\nDT 212,0,2048\nAW 115,186\nRC 246,246\n"
"CV 198,198,199\nv_pk_add_f32 v[248:249], v[248:249], s[4:5]\nv_pk_mul_f32 v[184:185], v[184:185], v[254:255]\n"
"CV 184,184,185\nAW 114,184\ns_nop 0\nMvavz 184,232,80\nRC 247,247\nRC 248,248\nRC 249,249\nMvav 184,192,84\n"
"DT 214,4,2048\nDT 216,0,3072\nEX 33,33\nAW 125,198\nv_pk_mul_f32 v[188:189], v[188:189], v[246:247]\n"
"v_pk_mul_f32 v[190:191], v[190:191], v[248:249]\nCV 188,188,189\nCV 190,190,191\nAW 118,188\nAW 119,190\n"
"Mvavz 188,232,88\nEX 34,34\nEX 35,35\nMvav 188,192,92\nDT 218,4,3072\nCV 96,32,33\nCV 97,34,35\n"
"Mvaac 32,196,0,16\nWL 4\nEX 36,36\nMvaa 32,200,4\nEX 37,37\nMvaa 32,204,8\nEX 38,38\nEX 39,39\nMvaa 32,208,12\n"
"DT 220,1,2048\nCV 104,36,37\nCV 105,38,39\nMvaac 36,196,16,20\nEX 40,40\nMvaa 36,200,20\nDT 222,5,2048\n"
"DT 224,1,3072\nMvaa 36,204,24\nEX 41,41\nEX 42,42\nEX 43,43\nMvaa 36,208,28\nDT 226,5,3072\nCV 112,40,41\n"
"CV 113,42,43\nMvaac 40,196,32,24\nEX 44,44\nMvaa 40,200,36\nDK 228,8,7168\nCV 116,116,117\nMvaa 40,204,40\n"
"EX 45,45\nEX 46,46\nEX 47,47\nMvaa 40,208,44\nDK 230,9,7168\nDK 232,8,7232\nEX 250,192\nEX 251,193\n"
"CV 120,44,45\nCV 121,46,47\nMvaac 44,196,48,28\nEX 252,194\nEX 253,195\nMvaa 44,200,52\nWL 4\nDK 234,9,7232\n"
"DK 192,8,7296\nEX 254,196\nAW 102,116\nv_pk_add_f32 v[250:251], v[250:251], s[4:5]\n"
"v_pk_add_f32 v[252:253], v[252:253], s[4:5]\nCV 124,124,125\nMvaa 44,204,56\nEX 255,197\nRC 250,250\n"
"RC 251,251\nMvaa 44,208,60\nDK 194,9,7296\nDK 196,8,7360\nRC 252,252\nAW 108,124\n"
"v_pk_add_f32 v[254:255], v[254:255], s[4:5]\nv_pk_mul_f32 v[192:193], v[192:193], v[250:251]\nCV 192,192,193\n"
"RC 253,253\nAW 120,192\nv_pk_mul_f32 v[194:195], v[194:195], v[252:253]\nCV 194,194,195\nAW 121,194\ns_nop 0\n"
"Mvavz 192,212,64\nEX 246,200\nEX 247,201\nEX 248,202\nMvav 192,216,68\nDK 198,9,7360\nCV 110,110,111\n"
"EX 249,203\nAW 101,110\nv_pk_add_f32 v[246:247], v[246:247], s[4:5]\n"
"v_pk_add_f32 v[248:249], v[248:249], s[4:5]\nCV 118,118,119\nRC 254,254\nRC 255,255\ns_nop 0\n"
"v_pk_mul_f32 v[196:197], v[196:197], v[254:255]\nCV 196,196,197\nAW 124,196\ns_nop 0\nMvavz 196,212,72\n"
"EX 250,204\nEX 251,205\nEX 252,206\nMvav 196,216,76\nEX 253,207\nRC 246,246\nAW 103,118\n"
"v_pk_add_f32 v[250:251], v[250:251], s[4:5]\nv_pk_add_f32 v[252:253], v[252:253], s[4:5]\nCV 126,126,127\n"
"RC 247,247\nRC 248,248\nRC 249,249\nv_pk_mul_f32 v[200:201], v[200:201], v[246:247]\n"
"v_pk_mul_f32 v[202:203], v[202:203], v[248:249]\nCV 200,200,201\nCV 202,202,203\nAW 122,200\nAW 123,202\n"
"Mvavz 200,212,80\nRC 250,250\nRC 251,251\nRC 252,252\nMvav 200,216,84\nRC 253,253\nEX 48,48\nAW 109,126\n"
"v_pk_mul_f32 v[204:205], v[204:205], v[250:251]\nv_pk_mul_f32 v[206:207], v[206:207], v[252:253]\n"
"CV 204,204,205\nCV 206,206,207\nAW 126,204\nAW 127,206\nMvavz 204,212,88\nWL 1\nEX 49,49\nMvav 204,216,92\n"
"EX 50,50\nMvavz 208,220,64\nEX 51,51\nMvav 208,224,68\nCV 98,48,49\nCV 99,50,51\nMvavz 212,220,72\nEX 52,52\n"
"Mvav 212,224,76\nDT 200,2,2048\nMvavz 216,220,80\nEX 53,53\nMvav 216,224,84\nDT 202,6,2048\nDT 204,2,3072\n"
"Mvavz 220,220,88\nEX 54,54\nMvav 220,224,92\nDT 206,6,3072\nCV 106,52,53\nMvaac 48,228,0,16\nWL 4\nEX 55,55\n"
"Mvaa 48,232,4\nbuffer_load_dword a191, v15, s[52:55], s93 offen\nMvaa 48,192,8\ns_add_u32 s93, s76, 0\n"
"EX 56,56\nMvaa 48,196,12\nDT 208,3,2048\nCV 107,54,55\nMvaac 52,228,16,20\nEX 57,57\nMvaa 52,232,20\n"
"DT 210,7,2048\nDT 212,3,3072\nMvaa 52,192,24\nEX 58,58\nEX 59,59\nMvaa 52,196,28\nDT 214,7,3072\nCV 114,56,57\n"
"CV 115,58,59\nMvaac 56,228,32,24\nbuffer_load_dword a191, v15, s[56:59], s93 offen\nMvaa 56,232,36\n"
"s_add_u32 s93, s76, 65536\nEX 60,60\nMvaa 56,192,40\nEX 61,61\nEX 62,62\nEX 63,63\nMvaa 56,196,44\n"
"CV 122,60,61\nCV 123,62,63\nMvaac 60,228,48,28\nbuffer_load_dword a191, v15, s[52:55], s93 offen\n"
"Mvaa 60,232,52\nWL 1\nEX 32,32\nMvaa 60,192,56\ns_add_u32 s93, s76, 65536\nEX 33,33\nMvaa 60,196,60\nEX 34,34\n"
"Mvavz 224,200,64\nbuffer_load_dword a191, v15, s[56:59], s93 offen\nMvav 224,204,68\ns_add_u32 s93, s76, 0\n"
"EX 35,35\nMvavz 228,200,72\nCV 100,32,33\nCV 101,34,35\nMvav 228,204,76\nEX 36,36\nMvavz 232,200,80\n"
"buffer_load_dword a191, v15, s[48:51], s93 offen\nMvav 232,204,84\ns_add_u32 s93, s76, 65536\nEX 37,37\n"
"Mvavz 236,200,88\nWL 0\nEX 38,38\nMvav 236,204,92\nEX 39,39\nMvavz 240,208,64\n"
"buffer_load_dword a191, v15, s[48:51], s93 offen\nMvav 240,212,68\ns_waitcnt vmcnt(6)\nCV 108,36,37\n"
"CV 109,38,39\ns_barrier\nMvavz 244,208,72\ns_add_u32 s93, s76, 131072\nDK 216,8,17408\nDK 218,9,17408\n"
"Mvav 244,212,76\nEX 40,40\nMvavz 248,208,80\nDK 220,8,17472\nDK 222,9,17472\nMvav 248,212,84\nLD 10,52,84,0\n"
"Mvavz 252,208,88\ns_addk_i32 s84, 3072\nDK 224,8,17536\nDK 226,9,17536\nMvav 252,212,92\nEX 41,41\n"
"Maavz 240,236,64\nDK 228,8,17600\nDK 230,9,17600\nMaav 240,236,68\nEX 42,42\nMaavz 244,236,72\nEX 43,43\n"
"Maav 244,236,76\nDT 232,0,13312\nCV 116,40,41\nMaavz 248,236,80\nDT 234,4,13312\nCV 117,42,43\nMaav 248,236,84\n"
"WL 4\nLD 10,52,84,1024\nMaavz 252,236,88\ns_addk_i32 s84, 3072\nDT 192,0,14336\nDT 194,4,14336\n"
"Maav 252,236,92\nEX 44,44\nMvaac 32,216,0,16\nWL 4\nEX 45,45\nMvaa 32,220,4\nEX 46,46\nMvaa 32,224,8\nEX 47,47\n"
"Mvaa 32,228,12\nDT 196,1,13312\nCV 124,44,45\nMvaac 36,216,16,20\nDT 198,5,13312\nCV 125,46,47\nMvaa 36,220,20\n"
"EX 48,48\nMvaa 36,224,24\nDT 200,1,14336\nDT 202,5,14336\nMvaa 36,228,28\nEX 49,49\nMvaac 40,216,32,24\n"
"EX 50,50\nMvaa 40,220,36\nDK 204,8,18432\nCV 102,48,49\nMvaa 40,224,40\nEX 51,51\nMvaa 40,228,44\n"
"DK 206,9,18432\nDK 208,8,18496\nMvaac 44,216,48,28\nLD 10,52,84,2048\nMvaa 44,220,52\nWL 3\nDK 210,9,18496\n"
"DK 212,8,18560\nMvaa 44,224,56\ns_addk_i32 s84, 3072\nDK 214,9,18560\nCV 103,50,51\nMvaa 44,228,60\nEX 52,52\n"
"Mvav 128,232,96\nDK 216,8,18624\nDK 218,9,18624\nMvav 128,192,100\nEX 53,53\nEX 54,54\nEX 55,55\n"
"Mvav 132,232,104\nCV 110,52,53\nCV 111,54,55\ns_nop 1\nMvav 132,192,108\nEX 56,56\nEX 57,57\nEX 58,58\n"
"Mvav 136,232,112\nEX 59,59\nCV 118,56,57\nCV 119,58,59\ns_nop 1\nMvav 136,192,116\nEX 60,60\nEX 61,61\n"
"EX 62,62\nMvav 140,232,120\nEX 63,63\nCV 126,60,61\nCV 127,62,63\ns_nop 1\nMvav 140,192,124\nLD 10,52,84,3072\n"
"Lw29_%=:\ns_cbranch_execz Lhub_%=\nMvav 144,196,96\ns_add_u32 m0, s72, 22528\nEX 32,32\nMvav 144,200,100\n"
"s_addk_i32 s84, -25600\nEX 33,33\nMvav 148,196,104\nEX 34,34\nMvav 148,200,108\nDT 220,2,13312\nCV 64,32,33\n"
"Mvav 152,196,112\nLD 11,56,84,0\nMvav 152,200,116\nWL 1\nDT 222,6,13312\nDT 224,2,14336\nMvav 156,196,120\n"
"s_addk_i32 s84, 7168\nEX 35,35\nMvav 156,200,124\nDT 226,6,14336\nCV 65,34,35\nMvaac 48,204,0,16\n"
"LD 11,56,84,1024\nMvaa 48,208,4\ns_movk_i32 s84, 30848\nEX 36,36\nMvaa 48,212,8\nEX 37,37\nMvaa 48,216,12\n"
"DT 228,3,13312\nCV 72,36,37\nMvaac 52,204,16,20\nLD 11,56,84,2048\nMvaa 52,208,20\ns_addk_i32 s84, 7168\n"
"DT 230,7,13312\nDT 232,3,14336\nMvaa 52,212,24\nEX 38,38\nMvaa 52,216,28\nDT 234,7,14336\nMvaac 56,204,32,24\n"
"LD 11,56,84,3072\nMvaa 56,208,36\ns_addk_i32 s84, 27520\nDK 192,8,19456\nMvaa 56,212,40\n"
"s_add_u32 m0, s73, 35840\nEX 39,39\nMvaa 56,216,44\nDK 194,9,19456\nDK 196,8,19520\nMvaac 60,204,48,28\n"
"DK 198,9,19520\nCV 73,38,39\nMvaa 60,208,52\nWL 5\nEX 40,40\nMvaa 60,212,56\nDK 200,8,19584\nDK 202,9,19584\n"
"Mvaa 60,216,60\nEX 41,41\nMvav 160,220,96\nDK 204,8,19648\nDK 206,9,19648\nMvav 160,224,100\nEX 42,42\n"
"Mvav 164,220,104\nEX 43,43\nMvav 164,224,108\nCV 80,40,41\nCV 81,42,43\nMvav 168,220,112\nEX 44,44\n"
"Mvav 168,224,116\nEX 45,45\nMvav 172,220,120\nWL 2\nEX 46,46\nMvav 172,224,124\nEX 47,47\nMvav 176,228,96\n"
"CV 88,44,45\nCV 89,46,47\nMvav 176,232,100\nEX 48,48\nMvav 180,228,104\nEX 49,49\nMvav 180,232,108\n"
"DT 208,0,15360\nCV 66,48,49\nMvav 184,228,112\nEX 50,50\nMvav 184,232,116\nDT 210,4,15360\nDT 212,0,16384\n"
"Mvav 188,228,120\nEX 51,51\nMvav 188,232,124\nDT 214,4,16384\nCV 67,50,51\nMvaac 32,192,0,16\nWL 4\nEX 52,52\n"
"Mvaa 32,196,4\nEX 53,53\nMvaa 32,200,8\nEX 54,54\nMvaa 32,204,12\nDT 216,1,15360\nCV 74,52,53\n"
"Mvaac 36,192,16,20\nEX 55,55\nMvaa 36,196,20\nDT 218,5,15360\nDT 220,1,16384\nMvaa 36,200,24\nDT 222,5,16384\n"
"CV 75,54,55\nMvaa 36,204,28\nEX 56,56\nMvaac 40,192,32,24\nEX 57,57\nMvaa 40,196,36\nDK 224,8,20480\n"
"CV 82,56,57\nMvaa 40,200,40\nEX 58,58\nMvaa 40,204,44\nDK 226,9,20480\nDK 228,8,20544\nMvaac 44,192,48,28\n"
"EX 59,59\nMvaa 44,196,52\nWL 3\nDK 230,9,20544\nDK 232,8,20608\nMvaa 44,200,56\nDK 234,9,20608\nCV 83,58,59\n"
"Mvaa 44,204,60\nEX 60,60\nMvav 192,208,96\nDK 192,8,20672\nDK 194,9,20672\nMvav 192,212,100\nEX 61,61\n"
"Mvav 196,208,104\nEX 62,62\nMvav 196,212,108\nEX 63,63\nMvav 200,208,112\nCV 90,60,61\nCV 91,62,63\n"
"Mvav 200,212,116\nbuffer_load_dword a191, v15, s[48:51], s93 offen\nMvav 204,208,120\n"
"s_add_u32 s93, s76, 196608\nEX 32,32\nMvav 204,212,124\nEX 33,33\nMvav 208,216,96\nEX 34,34\nMvav 208,220,100\n"
"buffer_load_dword a191, v15, s[48:51], s93 offen\nMvav 212,216,104\ns_add_u32 s93, s76, 262144\nEX 35,35\n"
"Mvav 212,220,108\nDT 196,2,15360\nCV 68,32,33\nMvav 216,216,112\nDT 198,6,15360\nCV 69,34,35\nMvav 216,220,116\n"
"WL 2\nbuffer_load_dword a191, v15, s[48:51], s93 offen\nMvav 220,216,120\ns_add_u32 s93, s76, 327680\n"
"DT 200,2,16384\nDT 202,6,16384\nMvav 220,220,124\nEX 36,36\nMvaac 48,224,0,16\nEX 37,37\nMvaa 48,228,4\n"
"EX 38,38\nMvaa 48,232,8\nEX 39,39\nMvaa 48,192,12\nDT 204,3,15360\nCV 76,36,37\nMvaac 52,224,16,20\n"
"LD 10,52,84,0\nMvaa 52,228,20\ns_addk_i32 s84, 3072\nDT 206,7,15360\nDT 208,3,16384\nMvaa 52,232,24\n"
"DT 210,7,16384\nCV 77,38,39\nMvaa 52,192,28\nEX 40,40\nMvaac 56,224,32,24\nEX 41,41\nMvaa 56,228,36\nEX 42,42\n"
"Mvaa 56,232,40\nEX 43,43\nMvaa 56,192,44\nCV 84,40,41\nCV 85,42,43\nMvaac 60,224,48,28\nLD 10,52,84,1024\n"
"Mvaa 60,228,52\nWL 0\nEX 44,44\nMvaa 60,232,56\ns_addk_i32 s84, 3072\nEX 45,45\nMvaa 60,192,60\n"
"s_waitcnt vmcnt(5)\nEX 46,46\nMvav 224,196,96\ns_barrier\nEX 47,47\nMvav 224,200,100\n"
"s_and_b32 s40, s60, 0xffe02000\nDK 212,8,26624\nCV 92,44,45\nMvav 228,196,104\ns_mov_b32 s41, s61\n"
"DK 214,9,26624\nCV 93,46,47\nMvav 228,200,108\ns_mov_b64 s[44:45], s[34:35]\nEX 48,48\nMvav 232,196,112\n"
"s_mov_b32 s46, 36864\nDK 216,8,26688\nDK 218,9,26688\nMvav 232,200,116\ns_lshr_b32 s91, s81, 1\nEX 49,49\n"
"Mvav 236,196,120\ns_addk_i32 s91, 0\nDK 220,8,26752\nDK 222,9,26752\nMvav 236,200,124\ns_lshl_b32 s82, s91, 15\n"
"EX 50,50\nMvav 240,204,96\ns_and_b32 s4, s91, 1\nDK 224,8,26816\nDK 226,9,26816\nMvav 240,208,100\n"
"s_mul_i32 s4, s4, 18400\nEX 51,51\nMvav 244,204,104\ns_lshl_b32 s5, s91, 5\nLD 10,52,84,2048\nMvav 244,208,108\n"
"s_addk_i32 s84, 3072\nCV 70,48,49\nCV 71,50,51\nMvav 248,204,112\ns_add_u32 s4, s4, s5\nEX 52,52\n"
"Mvav 248,208,116\ns_bfe_u32 s5, s81, 0x10002\nEX 53,53\nMvav 252,204,120\ns_mul_i32 s5, s5, 18432\nEX 54,54\n"
"Mvav 252,208,124\ns_and_b32 s78, s81, 8\nEX 55,55\nMaav 240,236,96\ns_lshl3_add_u32 s5, s78, s5\nCV 78,52,53\n"
"CV 79,54,55\nMaav 240,236,100\ns_addk_i32 s5, 512\nEX 56,56\nMaav 244,236,104\ns_and_b32 s78, s91, 12\n"
"EX 57,57\nMaav 244,236,108\ns_addk_i32 s78, 16\nDT 228,0,22528\nCV 86,56,57\nMaav 248,236,112\n"
"s_xor_b32 s35, s81, s77\nEX 58,58\nMaav 248,236,116\nWL 1\nDT 230,4,22528\nDT 232,0,23552\nMaav 252,236,120\n"
"s_lshr_b32 s34, s35, 1\nEX 59,59\nMaav 252,236,124\ns_and_b32 s35, s35, 3\nDT 234,4,23552\nCV 87,58,59\n"
"Mvaac 32,212,0,16\ns_min_u32 s35, s35, 1\nEX 60,60\nMvaa 32,216,4\ns_and_b32 s34, s34, s79\nEX 61,61\n"
"Mvaa 32,220,8\ns_mul_i32 s34, s34, 1179648\nEX 62,62\nMvaa 32,224,12\nDT 192,1,22528\nCV 94,60,61\n"
"Mvaac 36,212,16,20\nEX 63,63\nMvaa 36,216,20\nDT 194,5,22528\nDT 196,1,23552\nMvaa 36,220,24\nLD 10,52,84,3072\n"
"Mvaa 36,224,28\ns_addk_i32 s84, -25600\nDT 198,5,23552\nCV 95,62,63\nMvaac 40,212,32,24\n"
"s_add_u32 m0, s72, 31744\nEX 32,32\nMvaa 40,216,36\nDK 200,8,27648\nMvaa 40,220,40\nLD 11,56,84,0\n"
"Mvaa 40,224,44\ns_addk_i32 s84, 7168\nDK 202,9,27648\nDK 204,8,27712\nMvaac 44,212,48,28\nEX 33,33\n"
"Mvaa 44,216,52\nWL 4\nDK 206,9,27712\nDK 208,8,27776\nMvaa 44,220,56\nLD 11,56,84,1024\nMvaa 44,224,60\n"
"s_addk_i32 s84, -9088\nDK 210,9,27776\nDK 212,8,27840\nMvav 128,228,64\nEX 34,34\nMvav 128,232,68\n"
"DK 214,9,27840\nCV 96,32,33\nMvav 132,228,72\nLD 11,56,84,2048\nMvav 132,232,76\ns_addk_i32 s84, 7168\n"
"EX 35,35\nMvav 136,228,80\nEX 36,36\nMvav 136,232,84\nEX 37,37\nMvav 140,228,88\nWL 1\nLD 11,56,84,3072\n"
"Mvav 140,232,92\ns_add_u32 m0, s73, 4096\nCV 97,34,35\nCV 104,36,37\nMvav 144,192,64\ns_addk_i32 s84, 27520\n"
"EX 38,38\nMvav 144,196,68\nEX 39,39\nMvav 148,192,72\nEX 40,40\nMvav 148,196,76\nDT 216,2,22528\nCV 105,38,39\n"
"Mvav 152,192,80\nEX 41,41\nMvav 152,196,84\nDT 218,6,22528\nDT 220,2,23552\nMvav 156,192,88\nEX 42,42\n"
"Mvav 156,196,92\nDT 222,6,23552\nCV 112,40,41\nMvaac 48,200,0,16\nWL 4\nEX 43,43\nMvaa 48,204,4\nEX 44,44\n"
"Mvaa 48,208,8\nEX 45,45\nMvaa 48,212,12\nDT 224,3,22528\nCV 113,42,43\nMvaac 52,200,16,20\nEX 46,46\n"
"Mvaa 52,204,20\nDT 226,7,22528\nDT 228,3,23552\nMvaa 52,208,24\nEX 47,47\nMvaa 52,212,28\nDT 230,7,23552\n"
"CV 120,44,45\nMvaac 56,200,32,24\nEX 48,48\nMvaa 56,204,36\nDK 232,8,28672\nCV 121,46,47\nMvaa 56,208,40\n"
"EX 49,49\nMvaa 56,212,44\nDK 234,9,28672\nDK 192,8,28736\nMvaac 60,200,48,28\nEX 50,50\nMvaa 60,204,52\nWL 4\n"
"DK 194,9,28736\nDK 196,8,28800\nMvaa 60,208,56\nEX 51,51\nMvaa 60,212,60\nDK 198,9,28800\nDK 200,8,28864\n"
"Mvav 160,216,64\nDK 202,9,28864\nCV 98,48,49\nMvav 160,220,68\nEX 52,52\nMvav 164,216,72\nEX 53,53\n"
"Mvav 164,220,76\nCV 99,50,51\nCV 106,52,53\nMvav 168,216,80\nEX 54,54\nMvav 168,220,84\nEX 55,55\n"
"Mvav 172,216,88\nWL 1\nEX 56,56\nMvav 172,220,92\nEX 57,57\nMvav 176,224,64\nCV 107,54,55\nCV 114,56,57\n"
"Mvav 176,228,68\nEX 58,58\nMvav 180,224,72\nEX 59,59\nMvav 180,228,76\nDT 204,0,24576\nCV 115,58,59\n"
"Mvav 184,224,80\nEX 60,60\nMvav 184,228,84\nDT 206,4,24576\nDT 208,0,25600\nMvav 188,224,88\nEX 61,61\n"
"Mvav 188,228,92\nDT 210,4,25600\nCV 122,60,61\nMvaac 32,232,0,16\nWL 4\nEX 62,62\nMvaa 32,192,4\nEX 63,63\n"
"Mvaa 32,196,8\nbuffer_load_dword a191, v15, s[48:51], s93 offen\nMvaa 32,200,12\ns_add_u32 s93, s76, 393216\n"
"DT 212,1,24576\nCV 123,62,63\nMvaac 36,232,16,20\nDT 214,5,24576\nMvaa 36,192,20\nDT 216,1,25600\n"
"Mvaa 36,196,24\nbuffer_load_dword a191, v15, s[48:51], s93 offen\nMvaa 36,200,28\ns_add_u32 s93, s76, 458752\n"
"DT 218,5,25600\nMvaac 40,232,32,24\nEX 32,32\nMvaa 40,192,36\nDK 220,8,29696\nMvaa 40,196,40\n"
"buffer_load_dword a191, v15, s[48:51], s93 offen\nMvaa 40,200,44\ns_add_u32 s93, s76, 524288\nDK 222,9,29696\n"
"DK 224,8,29760\nMvaac 44,232,48,28\nEX 33,33\nMvaa 44,192,52\nWL 4\nDK 226,9,29760\nDK 228,8,29824\n"
"Mvaa 44,196,56\nEX 34,34\nMvaa 44,200,60\nDK 230,9,29824\nDK 232,8,29888\nMvav 192,204,64\nEX 35,35\n"
"Mvav 192,208,68\nDK 234,9,29888\nCV 100,32,33\nMvav 196,204,72\nEX 36,36\nMvav 196,208,76\nEX 37,37\n"
"Mvav 200,204,80\nCV 101,34,35\nCV 108,36,37\nMvav 200,208,84\nEX 38,38\nMvav 204,204,88\nWL 1\nEX 39,39\n"
"Mvav 204,208,92\nLD 10,52,84,0\nMvav 208,212,64\ns_addk_i32 s84, 3072\nEX 40,40\nMvav 208,216,68\nEX 41,41\n"
"Mvav 212,212,72\nCV 109,38,39\nCV 116,40,41\nMvav 212,216,76\nDT 192,2,24576\nMvav 216,212,80\nEX 42,42\n"
"Mvav 216,216,84\nDT 194,6,24576\nDT 196,2,25600\nMvav 220,212,88\nEX 43,43\nMvav 220,216,92\nDT 198,6,25600\n"
"CV 117,42,43\nMvaac 48,220,0,16\nWL 4\nLD 10,52,84,1024\nMvaa 48,224,4\ns_addk_i32 s84, 3072\nEX 44,44\n"
"Mvaa 48,228,8\nEX 45,45\nMvaa 48,232,12\nDT 200,3,24576\nCV 124,44,45\nMvaac 52,220,16,20\nEX 46,46\n"
"Mvaa 52,224,20\nDT 202,7,24576\nDT 204,3,25600\nMvaa 52,228,24\nEX 47,47\nMvaa 52,232,28\nDT 206,7,25600\n"
"CV 125,46,47\nMvaac 56,220,32,24\nEX 48,48\nMvaa 56,224,36\nEX 49,49\nMvaa 56,228,40\nEX 50,50\nMvaa 56,232,44\n"
"EX 51,51\nMvaac 60,220,48,28\nLD 10,52,84,2048\nMvaa 60,224,52\nWL 1\nCV 102,48,49\nCV 103,50,51\n"
"Mvaa 60,228,56\ns_addk_i32 s84, 3072\nEX 52,52\nMvaa 60,232,60\ns_waitcnt vmcnt(6)\nEX 53,53\nMvav 224,192,64\n"
"s_barrier\nEX 54,54\n"
"Lw30_%=:\ns_cbranch_execz Lhub_%=\nMvav 224,196,68\nDK 208,8,35840\nCV 110,52,53\nMvav 228,192,72\nEX 55,55\n"
"Mvav 228,196,76\nDK 210,9,35840\nDK 212,8,35904\nMvav 232,192,80\nDK 214,9,35904\nCV 111,54,55\n"
"Mvav 232,196,84\nEX 56,56\nMvav 236,192,88\nWL 4\nDK 216,8,35968\nDK 218,9,35968\nMvav 236,196,92\nEX 57,57\n"
"Mvav 240,200,64\nDK 220,8,36032\nDK 222,9,36032\nMvav 240,204,68\nEX 58,58\nMvav 244,200,72\nEX 59,59\n"
"Mvav 244,204,76\nCV 118,56,57\nCV 119,58,59\nMvav 248,200,80\nEX 60,60\nMvav 248,204,84\nEX 61,61\n"
"Mvav 252,200,88\nEX 62,62\nMvav 252,204,92\nEX 63,63\nMaav 240,236,64\nLD 10,52,84,3072\nMaav 240,236,68\n"
"s_add_u32 m0, s72, 0\nCV 126,60,61\nCV 127,62,63\nMaav 244,236,72\ns_addk_i32 s84, -25600\nMaav 244,236,76\n"
"DT 224,0,31744\nMaav 248,236,80\nLD 11,56,84,0\nMaav 248,236,84\nWL 1\nDT 226,4,31744\nDT 228,0,32768\n"
"Maav 252,236,88\ns_addk_i32 s84, 7168\nDT 230,4,32768\nMaav 252,236,92\nMvaac 32,208,0,16\nLD 11,56,84,1024\n"
"Mvaa 32,212,4\ns_addk_i32 s84, -9088\nMvaa 32,216,8\nMvaa 32,220,12\nDT 232,1,31744\nMvaac 36,208,16,20\n"
"LD 11,56,84,2048\nMvaa 36,212,20\ns_addk_i32 s84, 7168\nDT 234,5,31744\nDT 192,1,32768\nMvaa 36,216,24\n"
"EX 32,32\nMvaa 36,220,28\nDT 194,5,32768\nMvaac 40,208,32,24\nLD 11,56,84,3072\nMvaa 40,212,36\n"
"s_addk_i32 s84, 27520\nDK 196,8,36864\nMvaa 40,216,40\ns_add_u32 m0, s73, 17408\nEX 33,33\nMvaa 40,220,44\n"
"DK 198,9,36864\nDK 200,8,36928\nMvaac 44,208,48,28\nEX 34,34\nMvaa 44,212,52\nWL 4\nDK 202,9,36928\n"
"DK 204,8,36992\nMvaa 44,216,56\nEX 35,35\nMvaa 44,220,60\nDK 206,9,36992\nDK 208,8,37056\nMvav 128,224,96\n"
"DK 210,9,37056\nCV 64,32,33\nMvav 128,228,100\nEX 36,36\nMvav 132,224,104\nEX 37,37\nMvav 132,228,108\n"
"CV 65,34,35\nCV 72,36,37\nMvav 136,224,112\nEX 38,38\nMvav 136,228,116\nEX 39,39\nMvav 140,224,120\nWL 1\n"
"EX 40,40\nMvav 140,228,124\nEX 41,41\nMvav 144,232,96\nCV 73,38,39\nCV 80,40,41\nMvav 144,192,100\nEX 42,42\n"
"Mvav 148,232,104\nEX 43,43\nMvav 148,192,108\nDT 212,2,31744\nCV 81,42,43\nMvav 152,232,112\nEX 44,44\n"
"Mvav 152,192,116\nDT 214,6,31744\nDT 216,2,32768\nMvav 156,232,120\nEX 45,45\nMvav 156,192,124\nDT 218,6,32768\n"
"CV 88,44,45\nMvaac 48,196,0,16\nWL 4\nEX 46,46\nMvaa 48,200,4\nEX 47,47\nMvaa 48,204,8\n"
"buffer_load_dword a191, v15, s[48:51], s93 offen\nMvaa 48,208,12\ns_add_u32 s93, s76, 589824\nDT 220,3,31744\n"
"CV 89,46,47\nMvaac 52,196,16,20\nDT 222,7,31744\nMvaa 52,200,20\nDT 224,3,32768\nMvaa 52,204,24\nEX 48,48\n"
"Mvaa 52,208,28\nDT 226,7,32768\nMvaac 56,196,32,24\nEX 49,49\nMvaa 56,200,36\nDK 228,8,37888\nCV 66,48,49\n"
"Mvaa 56,204,40\nEX 50,50\nMvaa 56,208,44\nDK 230,9,37888\nDK 232,8,37952\nMvaac 60,196,48,28\nEX 51,51\n"
"Mvaa 60,200,52\nWL 4\nDK 234,9,37952\nDK 192,8,38016\nMvaa 60,204,56\nDK 194,9,38016\nCV 67,50,51\n"
"Mvaa 60,208,60\nEX 52,52\nMvav 160,212,96\nDK 196,8,38080\nDK 198,9,38080\nMvav 160,216,100\nEX 53,53\n"
"Mvav 164,212,104\nEX 54,54\nMvav 164,216,108\nEX 55,55\nMvav 168,212,112\nCV 74,52,53\nCV 75,54,55\n"
"Mvav 168,216,116\nEX 56,56\nMvav 172,212,120\nWL 2\nEX 57,57\nMvav 172,216,124\nEX 58,58\nMvav 176,220,96\n"
"EX 59,59\nMvav 176,224,100\nCV 82,56,57\nCV 83,58,59\nMvav 180,220,104\nEX 60,60\nMvav 180,224,108\n"
"DT 200,0,33792\nMvav 184,220,112\nEX 61,61\nMvav 184,224,116\nDT 202,4,33792\nDT 204,0,34816\nMvav 188,220,120\n"
"EX 62,62\nMvav 188,224,124\nDT 206,4,34816\nCV 90,60,61\nMvaac 32,228,0,16\nWL 4\nEX 63,63\nMvaa 32,232,4\n"
"buffer_load_dword a191, v15, s[48:51], s93 offen\nMvaa 32,192,8\ns_add_u32 s93, s76, 655360\nCV 91,62,63\n"
"Mvaa 32,196,12\nDT 208,1,33792\nMvaac 36,228,16,20\nDT 210,5,33792\nMvaa 36,232,20\n"
"buffer_load_dword a191, v15, s[48:51], s93 offen\nMvaa 36,192,24\nDT 212,1,34816\nDT 214,5,34816\n"
"Mvaa 36,196,28\nEX 32,32\nMvaac 40,228,32,24\nEX 33,33\nMvaa 40,232,36\nDK 216,8,38912\nCV 68,32,33\n"
"Mvaa 40,192,40\nEX 34,34\nMvaa 40,196,44\nDK 218,9,38912\nDK 220,8,38976\nMvaac 44,228,48,28\nEX 35,35\n"
"Mvaa 44,232,52\nWL 3\nDK 222,9,38976\nDK 224,8,39040\nMvaa 44,192,56\nDK 226,9,39040\nCV 69,34,35\n"
"Mvaa 44,196,60\nEX 36,36\nMvav 192,200,96\nDK 228,8,39104\nDK 230,9,39104\nMvav 192,204,100\nEX 37,37\n"
"Mvav 196,200,104\nEX 38,38\nMvav 196,204,108\nEX 39,39\nMvav 200,200,112\nLD 10,52,84,0\nMvav 200,204,116\n"
"s_addk_i32 s84, 3072\nCV 76,36,37\nCV 77,38,39\nMvav 204,200,120\nEX 40,40\nMvav 204,204,124\nEX 41,41\n"
"Mvav 208,208,96\nEX 42,42\nMvav 208,212,100\nEX 43,43\nMvav 212,208,104\nCV 84,40,41\nCV 85,42,43\n"
"Mvav 212,212,108\nDT 232,2,33792\nMvav 216,208,112\nLD 10,52,84,1024\nMvav 216,212,116\nWL 1\nDT 234,6,33792\n"
"DT 192,2,34816\nMvav 220,208,120\ns_addk_i32 s84, 3072\nEX 44,44\nMvav 220,212,124\nDT 194,6,34816\n"
"Mvaac 48,216,0,16\nEX 45,45\nMvaa 48,220,4\nEX 46,46\nMvaa 48,224,8\nEX 47,47\nMvaa 48,228,12\nDT 196,3,33792\n"
"CV 92,44,45\nMvaac 52,216,16,20\nDT 198,7,33792\nCV 93,46,47\nMvaa 52,220,20\nLD 10,52,84,2048\nMvaa 52,224,24\n"
"s_addk_i32 s84, 3072\nDT 200,3,34816\nDT 202,7,34816\nMvaa 52,228,28\nEX 48,48\nMvaac 56,216,32,24\nEX 49,49\n"
"Mvaa 56,220,36\nEX 50,50\nMvaa 56,224,40\nEX 51,51\nMvaa 56,228,44\nCV 70,48,49\nCV 71,50,51\n"
"Mvaac 60,216,48,28\nEX 52,52\nMvaa 60,220,52\nWL 0\nEX 53,53\nMvaa 60,224,56\nEX 54,54\nMvaa 60,228,60\n"
"s_waitcnt vmcnt(6)\nEX 55,55\nMvav 224,232,96\ns_barrier\nCV 78,52,53\nCV 79,54,55\nMvav 224,192,100\n"
"DK 204,8,4096\nMvav 228,232,104\nEX 56,56\nMvav 228,192,108\nDK 206,9,4096\nDK 208,8,4160\nMvav 232,232,112\n"
"EX 57,57\nMvav 232,192,116\nDK 210,9,4160\nDK 212,8,4224\nMvav 236,232,120\nEX 58,58\nMvav 236,192,124\n"
"DK 214,9,4224\nDK 216,8,4288\nMvav 240,196,96\nEX 59,59\nMvav 240,200,100\nDK 218,9,4288\nCV 86,56,57\n"
"Mvav 244,196,104\nEX 60,60\nMvav 244,200,108\nEX 61,61\nMvav 248,196,112\nCV 87,58,59\nCV 94,60,61\n"
"Mvav 248,200,116\nEX 62,62\nMvav 252,196,120\nEX 63,63\nMvav 252,200,124\nLD 10,52,84,3072\nMaav 240,236,96\n"
"s_add_u32 m0, s72, 13312\nCV 95,62,63\nMaav 240,236,100\ns_addk_i32 s84, -25600\nMaav 244,236,104\n"
"Maav 244,236,108\nDT 220,0,0\nMaav 248,236,112\nLD 11,56,84,0\nMaav 248,236,116\nWL 1\nDT 222,4,0\n"
"DT 224,0,1024\nMaav 252,236,120\ns_addk_i32 s84, 7168\nDT 226,4,1024\nMaav 252,236,124\nMvaac 32,204,0,16\n"
"LD 11,56,84,1024\nMvaa 32,208,4\ns_addk_i32 s84, -9088\nMvaa 32,212,8\nMvaa 32,216,12\nDT 228,1,0\n"
"Mvaac 36,204,16,20\nLD 11,56,84,2048\nMvaa 36,208,20\ns_addk_i32 s84, 7168\nDT 230,5,0\nDT 232,1,1024\n"
"Mvaa 36,212,24\nEX 32,32\nMvaa 36,216,28\nDT 234,5,1024\nMvaac 40,204,32,24\nLD 11,56,84,3072\nMvaa 40,208,36\n"
"s_addk_i32 s84, 27520\nDK 192,8,5120\nMvaa 40,212,40\ns_add_u32 m0, s73, 26624\nEX 33,33\nMvaa 40,216,44\n"
"DK 194,9,5120\nDK 196,8,5184\nMvaac 44,204,48,28\nEX 34,34\nMvaa 44,208,52\nWL 4\nDK 198,9,5184\nDK 200,8,5248\n"
"Mvaa 44,212,56\nEX 35,35\nMvaa 44,216,60\nDK 202,9,5248\nDK 204,8,5312\nMvav 128,220,64\nDK 206,9,5312\n"
"CV 96,32,33\nMvav 128,224,68\nEX 36,36\nMvav 132,220,72\nEX 37,37\nMvav 132,224,76\nCV 97,34,35\nCV 104,36,37\n"
"Mvav 136,220,80\nEX 38,38\nMvav 136,224,84\nEX 39,39\nMvav 140,220,88\nWL 1\nEX 40,40\nMvav 140,224,92\n"
"EX 41,41\nMvav 144,228,64\nCV 105,38,39\nCV 112,40,41\nMvav 144,232,68\nEX 42,42\nMvav 148,228,72\nEX 43,43\n"
"Mvav 148,232,76\nDT 208,2,0\nCV 113,42,43\nMvav 152,228,80\nEX 44,44\nMvav 152,232,84\nDT 210,6,0\n"
"DT 212,2,1024\nMvav 156,228,88\nEX 45,45\nMvav 156,232,92\nDT 214,6,1024\nCV 120,44,45\nMvaac 48,192,0,16\n"
"WL 4\nEX 46,46\nMvaa 48,196,4\nEX 47,47\nMvaa 48,200,8\nLD 10,52,84,0\nMvaa 48,204,12\ns_addk_i32 s84, 3072\n"
"DT 216,3,0\nCV 121,46,47\nMvaac 52,192,16,20\nDT 218,7,0\nMvaa 52,196,20\nDT 220,3,1024\nMvaa 52,200,24\n"
"EX 48,48\nMvaa 52,204,28\nDT 222,7,1024\nMvaac 56,192,32,24\nEX 49,49\nMvaa 56,196,36\nDK 224,8,6144\n"
"CV 98,48,49\nMvaa 56,200,40\nEX 50,50\nMvaa 56,204,44\nDK 226,9,6144\nDK 228,8,6208\nMvaac 60,192,48,28\n"
"EX 51,51\nMvaa 60,196,52\nWL 4\nDK 230,9,6208\nDK 232,8,6272\nMvaa 60,200,56\nDK 234,9,6272\nCV 99,50,51\n"
"Mvaa 60,204,60\nEX 52,52\nMvav 160,208,64\nDK 192,8,6336\nDK 194,9,6336\nMvav 160,212,68\nEX 53,53\n"
"Mvav 164,208,72\nEX 54,54\nMvav 164,212,76\nEX 55,55\nMvav 168,208,80\nCV 106,52,53\nCV 107,54,55\n"
"Mvav 168,212,84\nEX 56,56\nMvav 172,208,88\nWL 2\nEX 57,57\nMvav 172,212,92\nEX 58,58\nMvav 176,216,64\n"
"EX 59,59\nMvav 176,220,68\nCV 114,56,57\nCV 115,58,59\nMvav 180,216,72\nEX 60,60\nMvav 180,220,76\n"
"DT 196,0,2048\nMvav 184,216,80\nEX 61,61\nMvav 184,220,84\nDT 198,4,2048\nDT 200,0,3072\nMvav 188,216,88\n"
"EX 62,62\nMvav 188,220,92\nDT 202,4,3072\nCV 122,60,61\nMvaac 32,224,0,16\nWL 4\nEX 63,63\nMvaa 32,228,4\n"
"LD 10,52,84,1024\nMvaa 32,232,8\ns_addk_i32 s84, 3072\nCV 123,62,63\nMvaa 32,192,12\nDT 204,1,2048\n"
"Mvaac 36,224,16,20\nDT 206,5,2048\nMvaa 36,228,20\nLD 10,52,84,2048\nMvaa 36,232,24\ns_addk_i32 s84, 3072\n"
"DT 208,1,3072\nDT 210,5,3072\nMvaa 36,192,28\nEX 32,32\nMvaac 40,224,32,24\nEX 33,33\nMvaa 40,228,36\n"
"DK 212,8,7168\nCV 100,32,33\nMvaa 40,232,40\nEX 34,34\nMvaa 40,192,44\nDK 214,9,7168\nDK 216,8,7232\n"
"Mvaac 44,224,48,28\nEX 35,35\nMvaa 44,228,52\nWL 3\nDK 218,9,7232\nDK 220,8,7296\nMvaa 44,232,56\n"
"DK 222,9,7296\nCV 101,34,35\nMvaa 44,192,60\nEX 36,36\ns_nop 0\n"
"Lw31_%=:\ns_cbranch_execz Lhub_%=\nMvav 192,196,64\nDK 224,8,7360\nDK 226,9,7360\nMvav 192,200,68\nEX 37,37\n"
"Mvav 196,196,72\nEX 38,38\nMvav 196,200,76\nEX 39,39\nMvav 200,196,80\nCV 108,36,37\nCV 109,38,39\n"
"Mvav 200,200,84\nEX 40,40\nMvav 204,196,88\nEX 41,41\nMvav 204,200,92\nEX 42,42\nMvav 208,204,64\nEX 43,43\n"
"Mvav 208,208,68\nCV 116,40,41\nCV 117,42,43\nMvav 212,204,72\nEX 44,44\nMvav 212,208,76\nDT 228,2,2048\n"
"Mvav 216,204,80\nEX 45,45\nMvav 216,208,84\nWL 1\nDT 230,6,2048\nDT 232,2,3072\nMvav 220,204,88\nEX 46,46\n"
"Mvav 220,208,92\nDT 234,6,3072\nCV 124,44,45\nMvaac 48,212,0,16\nEX 47,47\nMvaa 48,216,4\nLD 10,52,84,3072\n"
"Mvaa 48,220,8\ns_add_u32 m0, s72, 22528\nCV 125,46,47\nMvaa 48,224,12\ns_addk_i32 s84, -25600\nDT 192,3,2048\n"
"Mvaac 52,212,16,20\nDT 194,7,2048\nMvaa 52,216,20\nLD 11,56,84,0\nMvaa 52,220,24\ns_addk_i32 s84, 7168\n"
"DT 196,3,3072\nDT 198,7,3072\nMvaa 52,224,28\nEX 48,48\nMvaac 56,212,32,24\nEX 49,49\nMvaa 56,216,36\nEX 50,50\n"
"Mvaa 56,220,40\nEX 51,51\nMvaa 56,224,44\nCV 102,48,49\nCV 103,50,51\nMvaac 60,212,48,28\nEX 52,52\n"
"Mvaa 60,216,52\nWL 0\nEX 53,53\nMvaa 60,220,56\nEX 54,54\nMvaa 60,224,60\ns_waitcnt vmcnt(5)\nEX 55,55\n"
"Mvav 224,228,64\ns_barrier\nCV 110,52,53\nCV 111,54,55\nMvav 224,232,68\nDK 200,8,17408\nMvav 228,228,72\n"
"EX 56,56\nMvav 228,232,76\nDK 202,9,17408\nDK 204,8,17472\nMvav 232,228,80\nEX 57,57\nMvav 232,232,84\n"
"DK 206,9,17472\nDK 208,8,17536\nMvav 236,228,88\nEX 58,58\nMvav 236,232,92\nDK 210,9,17536\nDK 212,8,17600\n"
"Mvav 240,192,64\nEX 59,59\nMvav 240,196,68\nDK 214,9,17600\nCV 118,56,57\nMvav 244,192,72\nEX 60,60\n"
"Mvav 244,196,76\nEX 61,61\nMvav 248,192,80\nCV 119,58,59\nCV 126,60,61\nMvav 248,196,84\nEX 62,62\n"
"Mvav 252,192,88\nEX 63,63\nMvav 252,196,92\nLD 11,56,84,1024\nMaav 240,236,64\ns_addk_i32 s84, -9088\n"
"CV 127,62,63\nMaav 240,236,68\nMaav 244,236,72\nMaav 244,236,76\nDT 216,0,13312\nMaav 248,236,80\n"
"LD 11,56,84,2048\nMaav 248,236,84\nWL 1\nDT 218,4,13312\nDT 220,0,14336\nMaav 252,236,88\ns_addk_i32 s84, 7168\n"
"DT 222,4,14336\nMaav 252,236,92\nMvaac 32,200,0,16\nLD 11,56,84,3072\nMvaa 32,204,4\ns_addk_i32 m0, 9216\n"
"Mvaa 32,208,8\ns_addk_i32 s84, 11136\nMvaa 32,212,12\nDT 224,1,13312\nMvaac 36,200,16,20\nLD 11,56,84,0\n"
"Mvaa 36,204,20\ns_addk_i32 s84, 7168\nDT 226,5,13312\nDT 228,1,14336\nMvaa 36,208,24\nEX 32,32\nMvaa 36,212,28\n"
"DT 230,5,14336\nMvaac 40,200,32,24\nEX 33,33\nMvaa 40,204,36\nDK 232,8,18432\nCV 64,32,33\nMvaa 40,208,40\n"
"EX 34,34\nMvaa 40,212,44\nDK 234,9,18432\nDK 192,8,18496\nMvaac 44,200,48,28\nEX 35,35\nMvaa 44,204,52\nWL 4\n"
"DK 194,9,18496\nDK 196,8,18560\nMvaa 44,208,56\nDK 198,9,18560\nCV 65,34,35\nMvaa 44,212,60\ns_barrier\n"
"EX 36,36\nMvav 128,216,96\nDK 200,8,18624\nDK 202,9,18624\nMvav 128,220,100\nEX 37,37\nMvav 132,216,104\n"
"EX 38,38\nMvav 132,220,108\nEX 39,39\nMvav 136,216,112\nCV 72,36,37\nCV 73,38,39\nMvav 136,220,116\nEX 40,40\n"
"Mvav 140,216,120\nWL 2\nEX 41,41\nMvav 140,220,124\nEX 42,42\nMvav 144,224,96\nEX 43,43\nMvav 144,228,100\n"
"CV 80,40,41\nCV 81,42,43\nMvav 148,224,104\nEX 44,44\nMvav 148,228,108\nDT 204,2,13312\nMvav 152,224,112\n"
"EX 45,45\nMvav 152,228,116\nDT 206,6,13312\nDT 208,2,14336\nMvav 156,224,120\nEX 46,46\nMvav 156,228,124\n"
"DT 210,6,14336\nCV 88,44,45\nMvaac 48,232,0,16\nWL 4\nEX 47,47\nMvaa 48,192,4\nLD 11,56,84,1024\nMvaa 48,196,8\n"
"s_addk_i32 s84, -9088\nCV 89,46,47\nMvaa 48,200,12\nDT 212,3,13312\nMvaac 52,232,16,20\nDT 214,7,13312\n"
"Mvaa 52,192,20\ns_mov_b64 exec, 1\nglobal_load_dword v10, v13, s[96:97] sc1\ns_mov_b64 exec, -1\n"
"Mvaa 52,196,24\nDT 216,3,14336\nDT 218,7,14336\nMvaa 52,200,28\nEX 48,48\nMvaac 56,232,32,24\nEX 49,49\n"
"Mvaa 56,192,36\nDK 220,8,19456\nCV 66,48,49\nMvaa 56,196,40\nLD 11,56,84,2048\nMvaa 56,200,44\n"
"s_addk_i32 s84, 7168\nDK 222,9,19456\nDK 224,8,19520\nMvaac 60,232,48,28\nEX 50,50\nMvaa 60,192,52\nWL 3\n"
"DK 226,9,19520\nDK 228,8,19584\nMvaa 60,196,56\nLD 11,56,84,3072\nMvaa 60,200,60\ns_mov_b32 s84, 368640\n"
"DK 230,9,19584\nDK 232,8,19648\nMvav 160,204,96\ns_addk_i32 m0, -31744\nEX 51,51\nMvav 160,208,100\n"
"DK 234,9,19648\nCV 67,50,51\nMvav 164,204,104\nLD 12,48,84,0\nMvav 164,208,108\ns_add_u32 s84, s84, s85\n"
"EX 52,52\nMvav 168,204,112\nEX 53,53\nMvav 168,208,116\nEX 54,54\nMvav 172,204,120\nLD 12,48,84,1024\n"
"Mvav 172,208,124\ns_add_u32 s84, s84, s85\nEX 55,55\nMvav 176,212,96\nCV 74,52,53\nCV 75,54,55\n"
"Mvav 176,216,100\nEX 56,56\nMvav 180,212,104\nLD 12,48,84,2048\nMvav 180,216,108\ns_add_u32 s84, s84, s85\n"
"DT 192,0,15360\nMvav 184,212,112\nEX 57,57\nMvav 184,216,116\nWL 1\nDT 194,4,15360\nDT 196,0,16384\n"
"Mvav 188,212,120\nLD 12,48,84,3072\nMvav 188,216,124\ns_mov_b32 s84, 516096\nDT 198,4,16384\nCV 82,56,57\n"
"Mvaac 32,220,0,16\ns_addk_i32 m0, 4096\nEX 58,58\nMvaa 32,224,4\nEX 59,59\nMvaa 32,228,8\nLD 12,48,84,0\n"
"Mvaa 32,232,12\ns_add_u32 s84, s84, s85\nDT 200,1,15360\nCV 83,58,59\nMvaac 36,220,16,20\nEX 60,60\n"
"Mvaa 36,224,20\nDT 202,5,15360\nDT 204,1,16384\nMvaa 36,228,24\nEX 61,61\nMvaa 36,232,28\nDT 206,5,16384\n"
"CV 90,60,61\nMvaac 40,220,32,24\nEX 62,62\ns_waitcnt vmcnt(7)\nv_cmp_eq_u32 vcc, s80, v10\nMvaa 40,224,36\n"
"s_bitcmp1_b32 vcc_lo, 0\ns_cbranch_scc1 Lpas_%=\n"
"Lpar_%=:\nDK 208,8,20480\nv_accvgpr_read_b32 v10, a190\nMvaa 40,228,40\nEX 63,63\nMvaa 40,232,44\n"
"DK 210,9,20480\nDK 212,8,20544\nMvaac 44,220,48,28\nDK 214,9,20544\nCV 91,62,63\nMvaa 44,224,52\nWL 5\n"
"LD 12,48,84,1024\nMvaa 44,228,56\ns_add_u32 s84, s84, s85\nDK 216,8,20608\nDK 218,9,20608\nMvaa 44,232,60\n"
"EX 32,32\nMvav 192,192,96\nDK 220,8,20672\nDK 222,9,20672\nMvav 192,196,100\nLD 12,48,84,2048\n"
"Mvav 196,192,104\ns_add_u32 s84, s84, s85\nEX 33,33\nMvav 196,196,108\nEX 34,34\nMvav 200,192,112\nEX 35,35\n"
"Mvav 200,196,116\nLD 12,48,84,3072\nMvav 204,192,120\nWL 2\nCV 68,32,33\nCV 69,34,35\nMvav 204,196,124\n"
"s_addk_i32 m0, 4096\nEX 36,36\nMvav 208,200,96\ns_movk_i32 s84, 0\nEX 37,37\nMvav 208,204,100\nLD 12,48,84,0\n"
"Mvav 212,200,104\ns_addk_i32 m0, 5120\nEX 38,38\nMvav 212,204,108\ns_mov_b32 s84, 36864\nDT 224,2,15360\n"
"CV 76,36,37\nMvav 216,200,112\nEX 39,39\nMvav 216,204,116\nDT 226,6,15360\nDT 228,2,16384\nMvav 220,200,120\n"
"DT 230,6,16384\nCV 77,38,39\nMvav 220,204,124\nEX 40,40\nMvaac 48,208,0,16\nWL 4\nEX 41,41\nMvaa 48,212,4\n"
"EX 42,42\nMvaa 48,216,8\nEX 43,43\nMvaa 48,220,12\nDT 232,3,15360\nCV 84,40,41\nMvaac 52,208,16,20\n"
"DT 234,7,15360\nCV 85,42,43\nMvaa 52,212,20\nEX 44,44\nMvaa 52,216,24\nDT 192,3,16384\nDT 194,7,16384\n"
"Mvaa 52,220,28\nEX 45,45\nMvaac 56,208,32,24\nEX 46,46\nMvaa 56,212,36\nEX 47,47\nMvaa 56,216,40\nCV 92,44,45\n"
"CV 93,46,47\nMvaa 56,220,44\nEX 48,48\nMvaac 60,208,48,28\nEX 49,49\nMvaa 60,212,52\nWL 0\nEX 50,50\n"
"Mvaa 60,216,56\nEX 51,51\nMvaa 60,220,60\ns_barrier\nCV 70,48,49\nCV 71,50,51\nMvav 224,224,96\nEX 52,52\n"
"Mvav 224,228,100\nDK 196,8,26624\nMvav 228,224,104\nEX 53,53\nMvav 228,228,108\nDK 198,9,26624\nDK 200,8,26688\n"
"Mvav 232,224,112\nEX 54,54\nMvav 232,228,116\nDK 202,9,26688\nDK 204,8,26752\nMvav 236,224,120\nEX 55,55\n"
"Mvav 236,228,124\nDK 206,9,26752\nDK 208,8,26816\nMvav 240,232,96\nDK 210,9,26816\nCV 78,52,53\n"
"Mvav 240,192,100\nEX 56,56\nMvav 244,232,104\nEX 57,57\nMvav 244,192,108\nCV 79,54,55\nCV 86,56,57\n"
"Mvav 248,232,112\nEX 58,58\nMvav 248,192,116\nEX 59,59\nMvav 252,232,120\nEX 60,60\nMvav 252,192,124\nEX 61,61\n"
"Maav 240,236,96\nCV 87,58,59\nCV 94,60,61\nMaav 240,236,100\nEX 62,62\nMaav 244,236,104\nEX 63,63\n"
"Maav 244,236,108\nDT 212,0,22528\nCV 95,62,63\nMaav 248,236,112\n"
"buffer_load_dwordx4 a[128:131], v13, s[64:67], s74 offen sc1\nMaav 248,236,116\nWL 1\nDT 214,4,22528\n"
"DT 216,0,23552\nMaav 252,236,120\ns_movk_i32 s74, 3072\nDT 218,4,23552\nMaav 252,236,124\nMvaac 32,196,0,16\n"
"buffer_load_dwordx4 a[132:135], v13, s[64:67], s75 offen sc1\nMvaa 32,200,4\ns_movk_i32 s75, 2048\n"
"Mvaa 32,204,8\nMvaa 32,208,12\nDT 220,1,22528\nMvaac 36,196,16,20\n"
"buffer_load_dwordx4 a[136:139], v13, s[64:67], s74 offen sc1\nMvaa 36,200,20\ns_movk_i32 s74, 5120\n"
"DT 222,5,22528\nDT 224,1,23552\nMvaa 36,204,24\nEX 32,32\nMvaa 36,208,28\nDT 226,5,23552\nMvaac 40,196,32,24\n"
"EX 33,33\nMvaa 40,200,36\nDK 228,8,27648\nCV 96,32,33\nMvaa 40,204,40\nEX 34,34\nMvaa 40,208,44\n"
"DK 230,9,27648\nDK 232,8,27712\nMvaac 44,196,48,28\nEX 35,35\nMvaa 44,200,52\nWL 4\nDK 234,9,27712\n"
"DK 192,8,27776\nMvaa 44,204,56\nDK 194,9,27776\nCV 97,34,35\nMvaa 44,208,60\ns_barrier\nEX 36,36\n"
"Mvav 128,212,64\nDK 196,8,27840\nDK 198,9,27840\nMvav 128,216,68\nEX 37,37\nMvav 132,212,72\nEX 38,38\n"
"Mvav 132,216,76\nEX 39,39\nMvav 136,212,80\nCV 104,36,37\nCV 105,38,39\nMvav 136,216,84\nEX 40,40\n"
"Mvav 140,212,88\nWL 2\nEX 41,41\nMvav 140,216,92\nEX 42,42\nMvav 144,220,64\nEX 43,43\nMvav 144,224,68\n"
"CV 112,40,41\nCV 113,42,43\nMvav 148,220,72\nEX 44,44\nMvav 148,224,76\nDT 200,2,22528\nMvav 152,220,80\n"
"EX 45,45\nMvav 152,224,84\nDT 202,6,22528\nDT 204,2,23552\nMvav 156,220,88\nEX 46,46\nMvav 156,224,92\n"
"DT 206,6,23552\nCV 120,44,45\nMvaac 48,228,0,16\nWL 4\nEX 47,47\nMvaa 48,232,4\n"
"buffer_load_dwordx4 a[140:143], v13, s[64:67], s75 offen sc1\nMvaa 48,192,8\ns_movk_i32 s75, 4096\n"
"CV 121,46,47\nMvaa 48,196,12\nDT 208,3,22528\nMvaac 52,228,16,20\nDT 210,7,22528\nMvaa 52,232,20\n"
"buffer_load_dwordx4 a[144:147], v13, s[64:67], s74 offen sc1\nMvaa 52,192,24\ns_movk_i32 s74, 7168\n"
"DT 212,3,23552\nDT 214,7,23552\nMvaa 52,196,28\nEX 48,48\nMvaac 56,228,32,24\nEX 49,49\nMvaa 56,232,36\n"
"DK 216,8,28672\nCV 98,48,49\nMvaa 56,192,40\nbuffer_load_dwordx4 a[148:151], v13, s[64:67], s75 offen sc1\n"
"Mvaa 56,196,44\ns_movk_i32 s75, 6144\nDK 218,9,28672\nDK 220,8,28736\ns_nop 0\n"
"Lw32_%=:\ns_cbranch_execz Lhub_%=\nMvaac 60,228,48,28\nEX 50,50\nMvaa 60,232,52\nWL 3\nDK 222,9,28736\n"
"DK 224,8,28800\nMvaa 60,192,56\nEX 51,51\nMvaa 60,196,60\nDK 226,9,28800\nDK 228,8,28864\nMvav 160,200,64\n"
"buffer_load_dwordx4 a[152:155], v13, s[64:67], s74 offen sc1\nMvav 160,204,68\ns_movk_i32 s74, 17408\n"
"DK 230,9,28864\nCV 99,50,51\nMvav 164,200,72\nEX 52,52\nMvav 164,204,76\nEX 53,53\nMvav 168,200,80\nEX 54,54\n"
"Mvav 168,204,84\nEX 55,55\nMvav 172,200,88\nbuffer_load_dwordx4 a[156:159], v13, s[64:67], s75 offen sc1\n"
"Mvav 172,204,92\ns_movk_i32 s75, 16384\nCV 106,52,53\nCV 107,54,55\nMvav 176,208,64\nEX 56,56\nMvav 176,212,68\n"
"EX 57,57\nMvav 180,208,72\nLD 12,48,84,0\nMvav 180,212,76\ns_add_u32 s84, s84, s85\nDT 232,0,24576\n"
"CV 114,56,57\nMvav 184,208,80\nEX 58,58\nMvav 184,212,84\nWL 1\nDT 234,4,24576\nDT 192,0,25600\n"
"Mvav 188,208,88\nEX 59,59\nMvav 188,212,92\nDT 194,4,25600\nCV 115,58,59\nMvaac 32,216,0,16\n"
"buffer_load_dwordx4 a[160:163], v13, s[64:67], s74 offen sc1\nMvaa 32,220,4\ns_movk_i32 s74, 19456\nEX 60,60\n"
"Mvaa 32,224,8\nEX 61,61\nMvaa 32,228,12\nDT 196,1,24576\nCV 122,60,61\nMvaac 36,216,16,20\nLD 12,48,84,1024\n"
"Mvaa 36,220,20\ns_add_u32 s84, s84, s85\nDT 198,5,24576\nDT 200,1,25600\nMvaa 36,224,24\nEX 62,62\n"
"Mvaa 36,228,28\nDT 202,5,25600\nMvaac 40,216,32,24\nEX 63,63\nMvaa 40,220,36\nDK 204,8,29696\nCV 123,62,63\n"
"Mvaa 40,224,40\nbuffer_load_dwordx4 a[164:167], v13, s[64:67], s75 offen sc1\nMvaa 40,228,44\n"
"s_movk_i32 s75, 18432\nDK 206,9,29696\nDK 208,8,29760\nMvaac 44,216,48,28\nEX 32,32\nMvaa 44,220,52\nWL 4\n"
"DK 210,9,29760\nDK 212,8,29824\nMvaa 44,224,56\nLD 12,48,84,2048\nMvaa 44,228,60\ns_add_u32 s84, s84, s85\n"
"DK 214,9,29824\nDK 216,8,29888\nMvav 192,232,64\nEX 33,33\nMvav 192,192,68\nDK 218,9,29888\nCV 100,32,33\n"
"Mvav 196,232,72\nbuffer_load_dwordx4 a[168:171], v13, s[64:67], s74 offen sc1\nMvav 196,192,76\n"
"s_movk_i32 s74, 21504\nEX 34,34\nMvav 200,232,80\nEX 35,35\nMvav 200,192,84\nCV 101,34,35\nv_mov_b32 v9, 1\n"
"Mvav 204,232,88\nWL 1\nLD 12,48,84,3072\nMvav 204,192,92\ns_addk_i32 m0, 4096\nEX 36,36\nMvav 208,196,64\n"
"s_mov_b32 s84, 184320\nEX 37,37\nMvav 208,200,68\nEX 38,38\nMvav 212,196,72\n"
"buffer_load_dwordx4 a[172:175], v13, s[64:67], s75 offen sc1\nMvav 212,200,76\ns_movk_i32 s75, 20480\n"
"DT 220,2,24576\nCV 108,36,37\nMvav 216,196,80\nEX 39,39\nMvav 216,200,84\nDT 222,6,24576\nDT 224,2,25600\n"
"Mvav 220,196,88\nLD 12,48,84,0\nMvav 220,200,92\ns_add_u32 s84, s84, s85\nDT 226,6,25600\nCV 109,38,39\n"
"Mvaac 48,204,0,16\nWL 4\nEX 40,40\nMvaa 48,208,4\nEX 41,41\nMvaa 48,212,8\n"
"buffer_load_dwordx4 a[176:179], v13, s[64:67], s74 offen sc1\nMvaa 48,216,12\ns_movk_i32 s74, 23552\n"
"DT 228,3,24576\nCV 116,40,41\nMvaac 52,204,16,20\nEX 42,42\nMvaa 52,208,20\nDT 230,7,24576\nDT 232,3,25600\n"
"Mvaa 52,212,24\nLD 12,48,84,1024\nMvaa 52,216,28\ns_add_u32 s84, s84, s85\nDT 234,7,25600\nMvaac 56,204,32,24\n"
"EX 48,48\nMvaa 56,208,36\nEX 49,49\nMvaa 56,212,40\n"
"buffer_load_dwordx4 a[180:183], v13, s[64:67], s75 offen sc1\nMvaa 56,216,44\ns_movk_i32 s75, 22528\nEX 50,50\n"
"Mvaac 60,204,48,28\nEX 51,51\nMvaa 60,208,52\nWL 1\nCV 102,48,49\nCV 103,50,51\nMvaa 60,212,56\n"
"LD 12,48,84,2048\nMvaa 60,216,60\ns_add_u32 s84, s84, s85\nEX 52,52\nMvav 224,220,64\nEX 53,53\n"
"Mvav 224,224,68\nEX 54,54\nMvav 228,220,72\nbuffer_load_dwordx4 a[184:187], v13, s[64:67], s74 offen sc1\n"
"Mvav 228,224,76\ns_mov_b32 s74, 33792\nEX 55,55\nMvav 232,220,80\nCV 110,52,53\nCV 111,54,55\nMvav 232,224,84\n"
"EX 43,43\nMvav 236,220,88\nWL 0\nLD 12,48,84,3072\nMvav 236,224,92\ns_addk_i32 m0, 4096\nEX 56,56\n"
"Mvav 240,228,64\ns_mov_b32 s84, 331776\nEX 57,57\nMvav 240,232,68\nEX 58,58\nMvav 244,228,72\n"
"buffer_load_dwordx4 a[188:191], v13, s[64:67], s75 offen sc1\nMvav 244,232,76\ns_mov_b32 s75, 32768\nEX 59,59\n"
"Mvav 248,228,80\nCV 117,42,43\nCV 118,56,57\nMvav 248,232,84\nEX 44,44\nMvav 252,228,88\nLD 12,48,84,0\n"
"Mvav 252,232,92\ns_waitcnt vmcnt(34)\nEX 45,45\nMaav 240,236,64\ns_barrier\nDT 192,0,31744\nDT 194,4,31744\n"
"Maav 240,236,68\ns_mov_b32 s84, 367744\nDT 196,0,32768\nCV 119,58,59\nMaav 244,236,72\nEX 46,46\n"
"Maav 244,236,76\nDT 198,4,32768\nDT 200,1,31744\nMaav 248,236,80\nEX 47,47\nMaav 248,236,84\nDT 202,5,31744\n"
"DT 204,1,32768\nMaav 252,236,88\nEX 60,60\nMaav 252,236,92\nDT 206,5,32768\nCV 124,44,45\nMaav 240,236,96\n"
"EX 61,61\nMaav 240,236,100\nEX 62,62\nMaav 244,236,104\nEX 63,63\nMaav 244,236,108\nDT 208,2,31744\n"
"CV 125,46,47\nMaav 248,236,112\nDT 210,6,31744\nCV 126,60,61\nMaav 248,236,116\nWL 3\nDT 212,2,32768\n"
"CV 127,62,63\nMaav 252,236,120\nDT 214,6,32768\nv_accvgpr_read_b32 v16, a240\nMaav 252,236,124\n"
"v_accvgpr_read_b32 v18, a244\nv_accvgpr_read_b32 v20, a248\nMvav 128,192,96\nv_add_f32 v24, v16, v18\n"
"Mvav 128,196,100\nMvav 132,192,104\nMvav 132,196,108\nDT 216,3,31744\nv_accvgpr_read_b32 v22, a252\n"
"Mvav 136,192,112\nDT 218,7,31744\nv_add_f32 v25, v20, v22\nMvav 136,196,116\nDT 220,3,32768\n"
"v_add_f32 v24, v24, v25\nMvav 140,192,120\nWL 3\nDT 222,7,32768\nv_cmp_nge_f32 vcc, 0x5d800000, v24\n"
"Mvav 140,196,124\ns_cbranch_vccnz Lfb_%=\n"
"Lfbr1_%=:\nbuffer_load_dwordx4 a[0:3], v13, s[64:67], s74 offen sc1\nMvav 144,200,96\ns_mov_b32 s74, 35840\n"
"RC 16,16\nMvav 144,204,100\nRC 18,18\nMvav 148,200,104\nv_mov_b32 v17, v16\nv_mov_b32 v19, v18\n"
"Mvav 148,204,108\nDT 224,0,33792\nbuffer_load_dwordx4 a[4:7], v13, s[64:67], s75 offen sc1\nMvav 152,200,112\n"
"s_mov_b32 s75, 34816\nRC 20,20\nRC 22,22\nv_mov_b32 v21, v20\nv_pk_mul_f32 v[128:129], v[128:129], v[16:17]\n"
"v_pk_mul_f32 v[130:131], v[130:131], v[16:17]\nv_pk_mul_f32 v[144:145], v[144:145], v[16:17]\n"
"v_pk_mul_f32 v[146:147], v[146:147], v[16:17]\nv_pk_mul_f32 v[132:133], v[132:133], v[18:19]\n"
"v_pk_mul_f32 v[134:135], v[134:135], v[18:19]\nv_pk_mul_f32 v[148:149], v[148:149], v[18:19]\n"
"v_pk_mul_f32 v[150:151], v[150:151], v[18:19]\nv_pk_mul_f32 v[136:137], v[136:137], v[20:21]\n"
"v_pk_mul_f32 v[138:139], v[138:139], v[20:21]\nCV 32,128,129\nMvav 152,204,116\nDT 226,4,33792\nDT 228,0,34816\n"
"Mvav 156,200,120\nDT 230,4,34816\nCV 33,130,131\nMvav 156,204,124\n"
"buffer_load_dwordx4 a[8:11], v13, s[64:67], s74 offen sc1\nMvav 160,208,96\ns_add_u32 s74, s82, 0\n"
"CV 34,144,145\nCV 35,146,147\nMvav 160,212,100\nCV 36,132,133\nCV 37,134,135\nMvav 164,208,104\nCV 38,148,149\n"
"CV 39,150,151\nMvav 164,212,108\nDT 232,1,33792\nCV 64,136,137\nMvav 168,208,112\n"
"buffer_load_dwordx4 a[12:15], v13, s[64:67], s75 offen sc1\nMvav 168,212,116\nWL 1\nDT 234,5,33792\n"
"DT 192,1,34816\nCV 65,138,139\nv_mov_b32 v23, v22\nv_pk_mul_f32 v[152:153], v[152:153], v[20:21]\n"
"v_pk_mul_f32 v[154:155], v[154:155], v[20:21]\nv_pk_mul_f32 v[140:141], v[140:141], v[22:23]\n"
"v_pk_mul_f32 v[142:143], v[142:143], v[22:23]\nv_pk_mul_f32 v[156:157], v[156:157], v[22:23]\n"
"v_pk_mul_f32 v[158:159], v[158:159], v[22:23]\nv_pk_mul_f32 v[160:161], v[160:161], v[16:17]\n"
"v_pk_mul_f32 v[162:163], v[162:163], v[16:17]\nv_pk_mul_f32 v[164:165], v[164:165], v[18:19]\n"
"v_pk_mul_f32 v[166:167], v[166:167], v[18:19]\nv_pk_mul_f32 v[168:169], v[168:169], v[20:21]\n"
"v_pk_mul_f32 v[170:171], v[170:171], v[20:21]\nCV 66,152,153\nMvav 172,208,120\ns_add_u32 s75, s82, 4096\n"
"DT 194,5,34816\nCV 67,154,155\nMvav 172,212,124\nCV 68,140,141\nCV 69,142,143\nMvav 176,216,96\n"
"buffer_store_dwordx4 v[32:35], v13, s[68:71], s87 offen sc1\nMvav 176,220,100\nCV 70,156,157\nCV 71,158,159\n"
"Mvav 180,216,104\ns_movk_i32 s87, 17408\nCV 40,160,161\nCV 41,162,163\nMvav 180,220,108\nDT 196,2,33792\n"
"CV 44,164,165\nMvav 184,216,112\nbuffer_store_dwordx4 v[36:39], v13, s[68:71], s88 offen sc1\nMvav 184,220,116\n"
"DT 198,6,33792\nDT 200,2,34816\nMvav 188,216,120\ns_movk_i32 s88, 16384\nDT 202,6,34816\nCV 45,166,167\n"
"CV 72,168,169\nCV 73,170,171\nv_pk_mul_f32 v[176:177], v[176:177], v[16:17]\n"
"v_pk_mul_f32 v[178:179], v[178:179], v[16:17]\nv_pk_mul_f32 v[180:181], v[180:181], v[18:19]\n"
"v_pk_mul_f32 v[182:183], v[182:183], v[18:19]\nv_pk_mul_f32 v[184:185], v[184:185], v[20:21]\n"
"v_pk_mul_f32 v[186:187], v[186:187], v[20:21]\nv_pk_mul_f32 v[172:173], v[172:173], v[22:23]\n"
"v_pk_mul_f32 v[174:175], v[174:175], v[22:23]\nCV 42,176,177\nMvav 188,220,124\nCV 43,178,179\nCV 46,180,181\n"
"Mvav 192,224,96\nDT 204,3,33792\nCV 47,182,183\nMvav 192,228,100\n"
"buffer_store_dwordx4 v[64:67], v13, s[68:71], s87 offen sc1\nMvav 196,224,104\ns_waitcnt vmcnt(32)\n"
"DT 206,7,33792\nDT 208,3,34816\nMvav 196,228,108\ns_barrier\nDT 210,7,34816\nCV 74,184,185\nMvav 200,224,112\n"
"s_movk_i32 s87, 3072\nDT 16,0,0\nCV 75,186,187\nMvav 200,228,116\nWL 5\n"
"buffer_store_dwordx4 v[68:71], v13, s[68:71], s88 offen sc1\nCV 76,172,173\nCV 77,174,175\n"
"v_pk_mul_f32 v[188:189], v[188:189], v[22:23]\nv_pk_mul_f32 v[190:191], v[190:191], v[22:23]\n"
"v_pk_mul_f32 v[192:193], v[192:193], v[16:17]\nv_pk_mul_f32 v[194:195], v[194:195], v[16:17]\n"
"v_pk_mul_f32 v[196:197], v[196:197], v[18:19]\nv_pk_mul_f32 v[198:199], v[198:199], v[18:19]\n"
"v_pk_mul_f32 v[200:201], v[200:201], v[20:21]\nv_pk_mul_f32 v[202:203], v[202:203], v[20:21]\nCV 78,188,189\n"
"Mvav 204,224,120\ns_movk_i32 s88, 2048\nDT 18,4,0\nDT 20,0,1024\nMvav 204,228,124\nDT 22,4,1024\nCV 79,190,191\n"
"Mvav 208,232,96\nDT 24,0,2048\nCV 48,192,193\nMvav 208,192,100\n"
"buffer_store_dwordx4 v[40:43], v13, s[68:71], s87 offen sc1\nMvav 212,232,104\nDT 26,4,2048\nDT 28,0,3072\n"
"Mvav 212,192,108\ns_movk_i32 s87, 19456\nDT 30,4,3072\nCV 49,194,195\nMvav 216,232,112\nDT 32,0,4096\n"
"CV 52,196,197\nMvav 216,192,116\nbuffer_store_dwordx4 v[44:47], v13, s[68:71], s88 offen sc1\nCV 53,198,199\n"
"CV 80,200,201\nCV 81,202,203\nv_pk_mul_f32 v[208:209], v[208:209], v[16:17]\n"
"v_pk_mul_f32 v[210:211], v[210:211], v[16:17]\nv_pk_mul_f32 v[212:213], v[212:213], v[18:19]\n"
"v_pk_mul_f32 v[214:215], v[214:215], v[18:19]\nv_pk_mul_f32 v[216:217], v[216:217], v[20:21]\n"
"v_pk_mul_f32 v[218:219], v[218:219], v[20:21]\nv_pk_mul_f32 v[204:205], v[204:205], v[22:23]\n"
"v_pk_mul_f32 v[206:207], v[206:207], v[22:23]\nCV 50,208,209\nMvav 220,232,120\ns_movk_i32 s88, 18432\n"
"DT 34,4,4096\nDT 36,0,5120\nMvav 220,192,124\nDT 38,4,5120\nCV 51,210,211\nMvav 224,196,96\nDT 40,0,6144\n"
"CV 54,212,213\nMvav 224,200,100\nbuffer_store_dwordx4 v[72:75], v13, s[68:71], s87 offen sc1\nMvav 228,196,104\n"
"DT 42,4,6144\nDT 44,0,7168\nMvav 228,200,108\ns_movk_i32 s87, 5120\nDT 46,4,7168\nCV 55,214,215\n"
"Mvav 232,196,112\nDT 48,0,8192\nCV 82,216,217\nMvav 232,200,116\nWL 6\n"
"buffer_store_dwordx4 v[76:79], v13, s[68:71], s88 offen sc1\nCV 83,218,219\nCV 84,204,205\nCV 85,206,207\n"
"v_pk_mul_f32 v[220:221], v[220:221], v[22:23]\nv_pk_mul_f32 v[222:223], v[222:223], v[22:23]\n"
"v_pk_mul_f32 v[224:225], v[224:225], v[16:17]\nv_pk_mul_f32 v[226:227], v[226:227], v[16:17]\n"
"v_pk_mul_f32 v[228:229], v[228:229], v[18:19]\nv_pk_mul_f32 v[230:231], v[230:231], v[18:19]\n"
"v_pk_mul_f32 v[232:233], v[232:233], v[20:21]\nv_pk_mul_f32 v[234:235], v[234:235], v[20:21]\nCV 86,220,221\n"
"Mvav 236,196,120\ns_movk_i32 s88, 4096\nDT 50,4,8192\nDT 52,1,0\nMvav 236,200,124\nDT 54,5,0\nCV 87,222,223\n"
"Mvav 240,204,96\nDT 56,1,1024\nCV 56,224,225\nMvav 240,208,100\n"
"buffer_store_dwordx4 v[48:51], v13, s[68:71], s87 offen sc1\nMvav 244,204,104\nDT 58,5,1024\nDT 60,1,2048\n"
"Mvav 244,208,108\ns_movk_i32 s87, 21504\nDT 62,5,2048\nCV 57,226,227\nMvav 248,204,112\nDT 212,1,3072\n"
"CV 60,228,229\nMvav 248,208,116\nbuffer_store_dwordx4 v[52:55], v13, s[68:71], s88 offen sc1\nCV 61,230,231\n"
"CV 88,232,233\nCV 89,234,235\nv_pk_mul_f32 v[240:241], v[240:241], v[16:17]\n"
"v_pk_mul_f32 v[242:243], v[242:243], v[16:17]\nv_pk_mul_f32 v[244:245], v[244:245], v[18:19]\n"
"v_pk_mul_f32 v[246:247], v[246:247], v[18:19]\nv_pk_mul_f32 v[248:249], v[248:249], v[20:21]\n"
"v_pk_mul_f32 v[250:251], v[250:251], v[20:21]\nv_pk_mul_f32 v[236:237], v[236:237], v[22:23]\n"
"v_pk_mul_f32 v[238:239], v[238:239], v[22:23]\nCV 58,240,241\nMvav 252,204,120\ns_movk_i32 s88, 20480\n"
"DT 214,5,3072\nDT 216,1,4096\nMvav 252,208,124\nDT 218,5,4096\nCV 59,242,243\nMvaaz 96,16,64\nDT 220,1,5120\n"
"CV 62,244,245\nMvaa 96,20,72\nbuffer_store_dwordx4 v[80:83], v13, s[68:71], s87 offen sc1\nMvaa 96,24,80\nWL 5\n"
"DT 222,5,5120\nDT 224,1,6144\nMvaa 96,28,88\ns_movk_i32 s87, 7168\nDT 226,5,6144\nCV 63,246,247\nMvaa 96,32,96\n"
"DT 228,1,7168\nCV 90,248,249\nMvaa 96,36,104\nbuffer_store_dwordx4 v[84:87], v13, s[68:71], s88 offen sc1\n"
"Mvaa 96,40,112\nDT 230,5,7168\nDT 232,1,8192\ns_nop 0\n"
"Lw33_%=:\ns_cbranch_execz Lhub_%=\nMvaa 96,44,120\ns_waitcnt vmcnt(40)\nDT 234,5,8192\nCV 91,250,251\n"
"Mvaa 96,48,128\ns_movk_i32 s88, 6144\nDT 192,2,0\nCV 92,236,237\nMvaaz 100,16,68\n"
"buffer_store_dwordx4 v[56:59], v13, s[68:71], s87 offen sc1\nMvaa 100,20,76\nDT 194,6,0\nDT 196,2,1024\n"
"Mvaa 100,24,84\ns_movk_i32 s87, 23552\nDT 198,6,1024\nCV 93,238,239\n"
"v_pk_mul_f32 v[252:253], v[252:253], v[22:23]\nv_pk_mul_f32 v[254:255], v[254:255], v[22:23]\nCV 94,252,253\n"
"Mvaa 100,28,92\nDT 200,2,2048\nCV 95,254,255\nMvaa 100,32,100\n"
"buffer_store_dwordx4 v[60:63], v13, s[68:71], s88 offen sc1\nMvaa 100,36,108\nDT 202,6,2048\nDT 204,2,3072\n"
"Mvaa 100,40,116\ns_movk_i32 s88, 22528\nDT 206,6,3072\nMvaa 100,44,124\ns_waitcnt vmcnt(41)\nDT 208,2,4096\n"
"Mvaa 100,48,132\nbuffer_store_dwordx4 v[88:91], v13, s[68:71], s87 offen sc1\nMvaaz 104,52,64\nWL 5\n"
"DT 210,6,4096\nDT 16,2,5120\nMvaa 104,56,72\ns_mov_b32 s87, 32768\nDT 18,6,5120\nMvaa 104,60,80\nDT 20,2,6144\n"
"Mvaa 104,212,88\nbuffer_store_dwordx4 v[92:95], v13, s[68:71], s88 offen sc1\nMvaa 104,216,96\nDT 22,6,6144\n"
"DT 24,2,7168\nMvaa 104,220,104\ns_movk_i32 s88, 0\nDT 26,6,7168\nMvaa 104,224,112\nDT 28,2,8192\n"
"Mvaa 104,228,120\nLD 12,48,84,1024\nMvaa 104,232,128\ns_add_u32 s84, s84, s85\nDT 30,6,8192\nDT 32,3,0\n"
"Mvaaz 108,52,68\nDT 34,7,0\nMvaa 108,56,76\nDT 36,3,1024\nMvaa 108,60,84\nLD 12,48,84,2048\nMvaa 108,212,92\n"
"s_add_u32 s84, s84, s85\nDT 38,7,1024\nDT 40,3,2048\nMvaa 108,216,100\nDT 42,7,2048\nMvaa 108,220,108\n"
"DT 44,3,3072\nMvaa 108,224,116\nLD 12,48,84,3072\nMvaa 108,228,124\ns_mov_b32 s84, 479360\nDT 46,7,3072\n"
"DT 48,3,4096\nMvaa 108,232,132\nWL 6\nDT 50,7,4096\nMvaaz 112,192,64\ns_addk_i32 m0, 4096\nDT 52,3,5120\n"
"Mvaa 112,196,72\nLD 12,48,84,0\nMvaa 112,200,80\ns_add_u32 s84, s84, s85\nDT 54,7,5120\nDT 56,3,6144\n"
"Mvaa 112,204,88\nDT 58,7,6144\nMvaa 112,208,96\nDT 60,3,7168\nMvaa 112,16,104\nLD 12,48,84,1024\n"
"Mvaa 112,20,112\ns_waitcnt vmcnt(25)\nDT 62,7,7168\nDT 212,3,8192\nMvaa 112,24,120\ns_barrier\nDT 214,7,8192\n"
"Mvaa 112,28,128\ns_add_u32 s84, s84, s85\nDT 216,0,13312\nMvaaz 116,192,68\nLD 12,48,84,2048\nMvaa 116,196,76\n"
"s_add_u32 s84, s84, s85\nDT 218,4,13312\nDT 220,0,14336\nMvaa 116,200,84\nDT 222,4,14336\nMvaa 116,204,92\n"
"DT 224,0,15360\nMvaa 116,208,100\nLD 12,48,84,3072\nMvaa 116,16,108\ns_mov_b32 s84, 626816\nDT 226,4,15360\n"
"DT 228,0,16384\nMvaa 116,20,116\ns_addk_i32 m0, 4096\nDT 230,4,16384\nMvaa 116,24,124\nWL 7\nDT 232,0,17408\n"
"Mvaa 116,28,132\nLD 12,48,84,0\nMvaaz 120,32,64\ns_movk_i32 s84, 128\nDT 234,4,17408\nDT 192,0,18432\n"
"Mvaa 120,36,72\ns_addk_i32 m0, 1024\nDT 194,4,18432\nMvaa 120,40,80\nDT 196,0,19456\nMvaa 120,44,88\n"
"LD 12,48,84,0\nMvaa 120,48,96\ns_add_u32 s84, s84, s85\nDT 198,4,19456\nDT 200,0,20480\nMvaa 120,52,104\n"
"DT 202,4,20480\nMvaa 120,56,112\nDT 204,0,21504\nMvaa 120,60,120\nLD 12,48,84,1024\nMvaa 120,212,128\n"
"s_add_u32 s84, s84, s85\nDT 206,4,21504\nDT 208,1,13312\nMvaaz 124,32,68\nDT 210,5,13312\nMvaa 124,36,76\n"
"DT 16,1,14336\nMvaa 124,40,84\nLD 12,48,84,2048\nMvaa 124,44,92\ns_add_u32 s84, s84, s85\nDT 18,5,14336\n"
"DT 20,1,15360\nMvaa 124,48,100\nDT 22,5,15360\nMvaa 124,52,108\nDT 24,1,16384\nMvaa 124,56,116\nWL 6\n"
"s_waitcnt vmcnt(11)\ns_mov_b64 exec, 1\nglobal_atomic_add v13, v4, s[94:95]\ns_mov_b64 exec, -1\n"
"Mvaa 124,60,124\nDT 26,5,16384\nDT 28,1,17408\nMvaa 124,212,132\nDT 30,5,17408\nMvaa 96,216,136\nDT 32,1,18432\n"
"Mvaa 96,220,144\nLD 12,48,84,3072\nMvaa 96,224,152\ns_mov_b32 s84, 147584\nDT 34,5,18432\nDT 36,1,19456\n"
"Mvaa 96,228,160\ns_addk_i32 m0, 4096\nDT 38,5,19456\nMvaa 96,232,168\nDT 40,1,20480\nMvaa 96,192,176\n"
"LD 12,48,84,0\nMvaa 96,196,184\ns_add_u32 s84, s84, s85\nDT 42,5,20480\nDT 44,1,21504\nMvaa 96,200,0\n"
"DT 46,5,21504\nMvaa 96,204,8\nDT 48,2,13312\nMvaa 100,216,140\nLD 12,48,84,1024\nMvaa 100,220,148\n"
"s_add_u32 s84, s84, s85\nDT 50,6,13312\nDT 52,2,14336\nMvaa 100,224,156\nDT 54,6,14336\nCV 24,96,97\n"
"Mvaa 100,228,164\nDT 56,2,15360\nCV 25,98,99\nMvaa 100,232,172\nLD 12,48,84,2048\nMvaa 100,192,180\n"
"s_add_u32 s84, s84, s85\nDT 58,6,15360\nDT 60,2,16384\nMvaa 100,196,188\nWL 6\nDT 62,6,16384\nMvaa 100,200,4\n"
"DT 212,2,17408\nMvaa 100,204,12\nLD 12,48,84,3072\nMvaa 104,208,136\ns_mov_b32 s84, 295040\nDT 214,6,17408\n"
"DT 216,2,18432\nMvaa 104,16,144\ns_addk_i32 m0, 4096\nDT 218,6,18432\nCV 28,100,101\nMvaa 104,20,152\n"
"DT 220,2,19456\nCV 29,102,103\nMvaa 104,24,160\nLD 12,48,84,0\nMvaa 104,28,168\ns_add_u32 s84, s84, s85\n"
"DT 222,6,19456\nDT 224,2,20480\nMvaa 104,32,176\nDT 226,6,20480\nMvaa 104,36,184\nDT 228,2,21504\n"
"Mvaa 104,40,0\nDT 230,6,21504\nMvaa 104,44,8\nDT 232,3,13312\nMvaa 108,208,140\nDT 234,7,13312\n"
"Mvaa 108,16,148\nLD 12,48,84,1024\nMvaa 108,20,156\ns_mov_b32 s84, 368896\nDT 192,3,14336\nDT 194,7,14336\n"
"Mvaa 108,24,164\ns_add_u32 m0, s72, 0\nDT 196,3,15360\nCV 26,104,105\nMvaa 108,28,172\nDT 198,7,15360\n"
"CV 27,106,107\nMvaa 108,32,180\nbuffer_store_dwordx4 v[24:27], v13, s[60:63], s87 offen sc1\nMvaa 108,36,188\n"
"WL 5\nDT 200,3,16384\nDT 202,7,16384\nMvaa 108,40,4\ns_mov_b32 s87, 98304\nDT 204,3,17408\nMvaa 108,44,12\n"
"DT 206,7,17408\nMvaa 112,48,136\nDT 208,3,18432\nMvaa 112,52,144\nDT 210,7,18432\nMvaa 112,56,152\n"
"DT 16,3,19456\nMvaa 112,60,160\nDT 18,7,19456\nCV 30,108,109\nMvaa 112,212,168\nDT 20,3,20480\nCV 31,110,111\n"
"Mvaa 112,216,176\nbuffer_store_dwordx4 v[28:31], v13, s[60:63], s88 offen sc1\nMvaa 112,220,184\n"
"s_waitcnt vmcnt(12)\nDT 22,7,20480\nDT 24,3,21504\nMvaa 112,224,0\ns_barrier\nDT 26,7,21504\nMvaa 112,228,8\n"
"s_mov_b32 s88, 65536\nDT 28,0,22528\nMvaa 116,48,140\nLD 12,48,84,0\nMvaa 116,52,148\ns_add_u32 s84, s84, s85\n"
"DT 30,4,22528\nDT 32,0,23552\nMvaa 116,56,156\nDT 34,4,23552\nCV 20,112,113\nMvaa 116,60,164\nDT 36,0,24576\n"
"CV 21,114,115\nMvaa 116,212,172\nDT 38,4,24576\nMvaa 116,216,180\nDT 40,0,25600\nMvaa 116,220,188\nWL 6\n"
"LD 12,48,84,1024\nMvaa 116,224,4\ns_add_u32 s84, s84, s85\nDT 42,4,25600\nDT 44,0,26624\nMvaa 116,228,12\n"
"DT 46,4,26624\nMvaa 120,232,136\nDT 48,0,27648\nMvaa 120,192,144\nDT 50,4,27648\nMvaa 120,196,152\n"
"DT 52,0,28672\nMvaa 120,200,160\nDT 54,4,28672\nCV 24,116,117\nMvaa 120,204,168\nLD 12,48,84,2048\n"
"Mvaa 120,208,176\ns_add_u32 s84, s84, s85\nDT 56,0,29696\nDT 58,4,29696\nMvaa 120,16,184\nDT 60,0,30720\n"
"CV 25,118,119\nMvaa 120,20,0\nDT 62,4,30720\nMvaa 120,24,8\nDT 212,1,22528\nMvaa 124,232,140\nDT 214,5,22528\n"
"Mvaa 124,192,148\nDT 216,1,23552\nMvaa 124,196,156\nLD 12,48,84,3072\nMvaa 124,200,164\ns_mov_b32 s84, 516352\n"
"DT 218,5,23552\nDT 220,1,24576\nMvaa 124,204,172\ns_addk_i32 m0, 4096\nDT 222,5,24576\nCV 22,120,121\n"
"Mvaa 124,208,180\nDT 224,1,25600\nCV 23,122,123\nMvaa 124,16,188\nWL 6\n"
"buffer_store_dwordx4 v[20:23], v13, s[60:63], s87 offen sc1\nMvaa 124,20,4\nDT 226,5,25600\nDT 228,1,26624\n"
"Mvaa 124,24,12\ns_mov_b32 s87, 163840\nDT 230,5,26624\nMvaaz 128,28,64\nDT 232,1,27648\nMvaa 128,32,72\n"
"LD 12,48,84,0\nMvaa 128,36,80\ns_add_u32 s84, s84, s85\nDT 234,5,27648\nDT 192,1,28672\nMvaa 128,40,88\n"
"DT 194,5,28672\nCV 26,124,125\nMvaa 128,44,96\nDT 196,1,29696\nCV 27,126,127\nMvaa 128,48,104\n"
"buffer_store_dwordx4 v[24:27], v13, s[60:63], s88 offen sc1\nMvaa 128,52,112\nDT 198,5,29696\nDT 200,1,30720\n"
"Mvaa 128,56,120\ns_mov_b32 s88, 131072\nDT 202,5,30720\nMvaa 128,60,128\nDT 204,2,22528\nMvaaz 132,28,68\n"
"LD 12,48,84,1024\nMvaa 132,32,76\ns_add_u32 s84, s84, s85\nDT 206,6,22528\nDT 208,2,23552\nMvaa 132,36,84\n"
"DT 210,6,23552\nMvaa 132,40,92\nDT 16,2,24576\nMvaa 132,44,100\nDT 18,6,24576\nMvaa 132,48,108\n"
"LD 12,48,84,2048\nMvaa 132,52,116\nWL 5\nDT 20,2,25600\nDT 22,6,25600\nMvaa 132,56,124\n"
"s_add_u32 s84, s84, s85\nDT 24,2,26624\nMvaa 132,60,132\nDT 26,6,26624\nMvaaz 136,212,64\nDT 28,2,27648\n"
"Mvaa 136,216,72\nDT 30,6,27648\nMvaa 136,220,80\nDT 32,2,28672\nMvaa 136,224,88\nLD 12,48,84,3072\n"
"Mvaa 136,228,96\ns_movk_i32 s84, 256\nDT 34,6,28672\nDT 36,2,29696\nMvaa 136,232,104\ns_addk_i32 m0, 4096\n"
"DT 38,6,29696\nMvaa 136,192,112\nDT 40,2,30720\nMvaa 136,196,120\nDT 42,6,30720\nMvaa 136,200,128\n"
"DT 44,3,22528\nMvaaz 140,212,68\nDT 46,7,22528\nMvaa 140,216,76\nLD 12,48,84,0\nMvaa 140,220,84\n"
"s_mov_b32 s84, 37120\nDT 48,3,23552\nDT 50,7,23552\nMvaa 140,224,92\ns_addk_i32 m0, 5120\nDT 52,3,24576\n"
"Mvaa 140,228,100\nDT 54,7,24576\nMvaa 140,232,108\nDT 56,3,25600\nMvaa 140,192,116\nWL 6\nDT 58,7,25600\n"
"Mvaa 140,196,124\nDT 60,3,26624\nMvaa 140,200,132\nDT 62,7,26624\nMvaaz 144,204,64\nDT 212,3,27648\n"
"Mvaa 144,208,72\nDT 214,7,27648\nMvaa 144,16,80\nDT 216,3,28672\nMvaa 144,20,88\nDT 218,7,28672\n"
"Mvaa 144,24,96\nDT 220,3,29696\nMvaa 144,28,104\nDT 222,7,29696\nMvaa 144,32,112\ns_waitcnt vmcnt(13)\n"
"DT 224,3,30720\nMvaa 144,36,120\ns_barrier\nDT 226,7,30720\nMvaa 144,40,128\nLD 12,48,84,0\nMvaaz 148,204,68\n"
"s_add_u32 s84, s84, s85\nDT 228,0,31744\nDT 230,4,31744\nMvaa 148,208,76\nDT 232,0,32768\nMvaa 148,16,84\n"
"DT 234,4,32768\nMvaa 148,20,92\nDT 192,0,33792\nMvaa 148,24,100\nDT 194,4,33792\nMvaa 148,28,108\n"
"s_waitcnt vmcnt(5)\ns_bfm_b64 exec, 1, 16\nglobal_atomic_add v13, v9, s[98:99]\ns_mov_b64 exec, -1\n"
"Mvaa 148,32,116\nWL 6\nDT 196,0,34816\nDT 198,4,34816\nMvaa 148,36,124\nDT 200,0,35840\nMvaa 148,40,132\n"
"DT 202,4,35840\nMvaaz 152,44,64\nLD 12,48,84,1024\nMvaa 152,48,72\ns_add_u32 s84, s84, s85\nDT 204,0,36864\n"
"DT 206,4,36864\nMvaa 152,52,80\nDT 208,0,37888\nMvaa 152,56,88\nDT 210,4,37888\nMvaa 152,60,96\n"
"LD 12,48,84,2048\nMvaa 152,212,104\ns_add_u32 s84, s84, s85\nDT 16,0,38912\nDT 18,4,38912\nMvaa 152,216,112\n"
"DT 20,0,39936\nMvaa 152,220,120\nDT 22,4,39936\nMvaa 152,224,128\nDT 24,1,31744\nMvaaz 156,44,68\n"
"DT 26,5,31744\nMvaa 156,48,76\nDT 28,1,32768\nMvaa 156,52,84\nLD 12,48,84,3072\n"
"Lw34_%=:\ns_cbranch_execz Lhub_%=\nMvaa 156,56,92\ns_mov_b32 s84, 184576\nDT 30,5,32768\nDT 32,1,33792\n"
"Mvaa 156,60,100\ns_addk_i32 m0, 4096\nDT 34,5,33792\nMvaa 156,212,108\nDT 36,1,34816\nMvaa 156,216,116\nWL 6\n"
"DT 38,5,34816\nMvaa 156,220,124\nDT 40,1,35840\nMvaa 156,224,132\nDT 42,5,35840\nMvaa 128,228,136\n"
"LD 12,48,84,0\nMvaa 128,232,144\ns_add_u32 s84, s84, s85\nDT 44,1,36864\nDT 46,5,36864\nMvaa 128,192,152\n"
"DT 48,1,37888\nMvaa 128,196,160\nDT 50,5,37888\nMvaa 128,200,168\nDT 52,1,38912\nMvaa 128,204,176\n"
"DT 54,5,38912\nMvaa 128,208,184\nDT 56,1,39936\nMvaa 128,16,0\nLD 12,48,84,1024\nMvaa 128,20,8\n"
"s_add_u32 s84, s84, s85\nDT 58,5,39936\nDT 60,2,31744\nMvaa 132,228,140\nDT 62,6,31744\nMvaa 132,232,148\n"
"DT 212,2,32768\nMvaa 132,192,156\nDT 214,6,32768\nCV 28,128,129\nMvaa 132,196,164\nDT 216,2,33792\n"
"CV 29,130,131\nMvaa 132,200,172\nDT 218,6,33792\nMvaa 132,204,180\nLD 12,48,84,2048\nMvaa 132,208,188\nWL 5\n"
"DT 220,2,34816\nDT 222,6,34816\nMvaa 132,16,4\ns_add_u32 s84, s84, s85\nDT 224,2,35840\nMvaa 132,20,12\n"
"DT 226,6,35840\nMvaa 136,24,136\nDT 228,2,36864\nMvaa 136,28,144\nDT 230,6,36864\nMvaa 136,32,152\n"
"DT 232,2,37888\nMvaa 136,36,160\nLD 12,48,84,3072\nMvaa 136,40,168\ns_mov_b32 s84, 332032\nDT 234,6,37888\n"
"DT 192,2,38912\nMvaa 136,44,176\ns_addk_i32 m0, 4096\nDT 194,6,38912\nCV 20,132,133\nMvaa 136,48,184\n"
"DT 196,2,39936\nCV 21,134,135\nMvaa 136,52,0\nDT 198,6,39936\nMvaa 136,56,8\nDT 200,3,31744\nMvaa 140,24,140\n"
"DT 202,7,31744\nMvaa 140,28,148\nLD 12,48,84,0\nMvaa 140,32,156\ns_mov_b32 s84, 368000\nDT 204,3,32768\n"
"DT 206,7,32768\nMvaa 140,36,164\nDT 208,3,33792\nCV 30,136,137\nMvaa 140,40,172\nDT 210,7,33792\nCV 31,138,139\n"
"Mvaa 140,44,180\nbuffer_store_dwordx4 v[28:31], v13, s[60:63], s87 offen sc1\nMvaa 140,48,188\nWL 5\n"
"DT 16,3,34816\nDT 18,7,34816\nMvaa 140,52,4\ns_mov_b32 s87, 229376\nDT 20,3,35840\nMvaa 140,56,12\n"
"DT 22,7,35840\nMvaa 144,60,136\nDT 24,3,36864\nMvaa 144,212,144\nDT 26,7,36864\nMvaa 144,216,152\n"
"DT 28,3,37888\nMvaa 144,220,160\nDT 30,7,37888\nCV 22,140,141\nMvaa 144,224,168\nDT 32,3,38912\nCV 23,142,143\n"
"Mvaa 144,228,176\nbuffer_store_dwordx4 v[20:23], v13, s[60:63], s88 offen sc1\nMvaa 144,232,184\n"
"s_waitcnt vmcnt(12)\nDT 34,7,38912\nDT 36,3,39936\nMvaa 144,192,0\ns_barrier\nDT 38,7,39936\nMvaa 144,196,8\n"
"s_mov_b32 s88, 196608\nDT 40,0,0\nMvaa 148,60,140\nLD 12,48,84,1024\nMvaa 148,212,148\ns_add_u32 s84, s84, s85\n"
"DT 42,4,0\nDT 44,0,1024\nMvaa 148,216,156\nDT 46,4,1024\nCV 24,144,145\nMvaa 148,220,164\nDT 48,0,2048\n"
"CV 25,146,147\nMvaa 148,224,172\nDT 50,4,2048\nMvaa 148,228,180\nDT 52,0,3072\nMvaa 148,232,188\nWL 6\n"
"LD 12,48,84,2048\nMvaa 148,192,4\ns_add_u32 s84, s84, s85\nDT 54,4,3072\nDT 56,0,4096\nMvaa 148,196,12\n"
"DT 58,4,4096\nMvaa 152,200,136\nDT 60,0,5120\nMvaa 152,204,144\nDT 62,4,5120\nMvaa 152,208,152\nDT 212,0,6144\n"
"Mvaa 152,16,160\nDT 214,4,6144\nCV 28,148,149\nMvaa 152,20,168\nLD 12,48,84,3072\nMvaa 152,24,176\n"
"s_mov_b32 s84, 479616\nDT 216,0,7168\nDT 218,4,7168\nMvaa 152,28,184\ns_addk_i32 m0, 4096\nDT 220,0,8192\n"
"CV 29,150,151\nMvaa 152,32,0\nDT 222,4,8192\nMvaa 152,36,8\nDT 224,1,0\nMvaa 156,200,140\nDT 226,5,0\n"
"Mvaa 156,204,148\nDT 228,1,1024\nMvaa 156,208,156\nLD 12,48,84,0\nMvaa 156,16,164\ns_add_u32 s84, s84, s85\n"
"DT 230,5,1024\nDT 232,1,2048\nMvaa 156,20,172\nDT 234,5,2048\nCV 26,152,153\nMvaa 156,24,180\nDT 192,1,3072\n"
"CV 27,154,155\nMvaa 156,28,188\nWL 6\nbuffer_store_dwordx4 v[24:27], v13, s[60:63], s87 offen sc1\n"
"Mvaa 156,32,4\nDT 194,5,3072\nDT 196,1,4096\nMvaa 156,36,12\ns_mov_b32 s87, 294912\nDT 198,5,4096\n"
"Mvaaz 96,40,64\nDT 200,1,5120\nMvaa 96,44,72\nLD 12,48,84,1024\nMvaa 96,48,80\ns_add_u32 s84, s84, s85\n"
"DT 202,5,5120\nDT 204,1,6144\nMvaa 96,52,88\nDT 206,5,6144\nCV 30,156,157\nMvaa 96,56,96\nDT 208,1,7168\n"
"CV 31,158,159\nMvaa 96,60,104\nbuffer_store_dwordx4 v[28:31], v13, s[60:63], s88 offen sc1\nMvaa 96,212,112\n"
"DT 210,5,7168\nDT 16,1,8192\nMvaa 96,216,120\ns_mov_b32 s88, 262144\nDT 18,5,8192\nMvaa 96,220,128\nDT 20,2,0\n"
"Mvaaz 100,40,68\nLD 12,48,84,2048\nMvaa 100,44,76\ns_add_u32 s84, s84, s85\nDT 22,6,0\nDT 24,2,1024\n"
"Mvaa 100,48,84\nDT 26,6,1024\nMvaa 100,52,92\nDT 28,2,2048\nMvaa 100,56,100\nDT 30,6,2048\nMvaa 100,60,108\n"
"LD 12,48,84,3072\nMvaa 100,212,116\nWL 5\nDT 32,2,3072\nDT 34,6,3072\nMvaa 100,216,124\ns_addk_i32 m0, 4096\n"
"DT 36,2,4096\nMvaa 100,220,132\ns_mov_b32 s84, 627072\nDT 38,6,4096\nMvaaz 104,224,64\nDT 40,2,5120\n"
"Mvaa 104,228,72\nDT 42,6,5120\nMvaa 104,232,80\nDT 44,2,6144\nMvaa 104,192,88\nLD 12,48,84,0\nMvaa 104,196,96\n"
"s_movk_i32 s84, 384\nDT 46,6,6144\nDT 48,2,7168\nMvaa 104,200,104\ns_addk_i32 m0, 1024\nDT 50,6,7168\n"
"Mvaa 104,204,112\nDT 52,2,8192\nMvaa 104,208,120\nDT 54,6,8192\nMvaa 104,16,128\nDT 56,3,0\nMvaaz 108,224,68\n"
"DT 58,7,0\nMvaa 108,228,76\nLD 12,48,84,0\nMvaa 108,232,84\ns_add_u32 s84, s84, s85\nDT 60,3,1024\n"
"DT 62,7,1024\nMvaa 108,192,92\nDT 212,3,2048\nMvaa 108,196,100\nDT 214,7,2048\nMvaa 108,200,108\nDT 216,3,3072\n"
"Mvaa 108,204,116\nWL 6\nDT 218,7,3072\nMvaa 108,208,124\nDT 220,3,4096\nMvaa 108,16,132\nDT 222,7,4096\n"
"Mvaaz 112,20,64\nDT 224,3,5120\nMvaa 112,24,72\nDT 226,7,5120\nMvaa 112,28,80\nDT 228,3,6144\nMvaa 112,32,88\n"
"DT 230,7,6144\nMvaa 112,36,96\nDT 232,3,7168\nMvaa 112,40,104\nDT 234,7,7168\nMvaa 112,44,112\n"
"s_waitcnt vmcnt(13)\nDT 192,3,8192\nMvaa 112,48,120\ns_barrier\nDT 194,7,8192\nMvaa 112,52,128\n"
"LD 12,48,84,1024\nMvaaz 116,20,68\ns_add_u32 s84, s84, s85\nDT 196,0,13312\nDT 198,4,13312\nMvaa 116,24,76\n"
"DT 200,0,14336\nMvaa 116,28,84\nDT 202,4,14336\nMvaa 116,32,92\nDT 204,0,15360\nMvaa 116,36,100\n"
"DT 206,4,15360\nMvaa 116,40,108\ns_waitcnt vmcnt(5)\ns_bfm_b64 exec, 1, 20\n"
"global_atomic_add v13, v9, s[98:99]\ns_mov_b64 exec, -1\nMvaa 116,44,116\nWL 6\nDT 208,0,16384\nDT 210,4,16384\n"
"Mvaa 116,48,124\nDT 16,0,17408\nMvaa 116,52,132\nDT 18,4,17408\nMvaaz 120,56,64\nLD 12,48,84,2048\n"
"Mvaa 120,60,72\ns_add_u32 s84, s84, s85\nDT 20,0,18432\nDT 22,4,18432\nMvaa 120,212,80\nDT 24,0,19456\n"
"Mvaa 120,216,88\nDT 26,4,19456\nMvaa 120,220,96\nLD 12,48,84,3072\nMvaa 120,224,104\ns_mov_b32 s84, 147840\n"
"DT 28,0,20480\nDT 30,4,20480\nMvaa 120,228,112\ns_addk_i32 m0, 4096\nDT 32,0,21504\nMvaa 120,232,120\n"
"DT 34,4,21504\nMvaa 120,192,128\nDT 36,1,13312\nMvaaz 124,56,68\nDT 38,5,13312\nMvaa 124,60,76\nDT 40,1,14336\n"
"Mvaa 124,212,84\nLD 12,48,84,0\nMvaa 124,216,92\ns_add_u32 s84, s84, s85\nDT 42,5,14336\nDT 44,1,15360\n"
"Mvaa 124,220,100\nDT 46,5,15360\nMvaa 124,224,108\nDT 48,1,16384\nMvaa 124,228,116\nWL 6\nDT 50,5,16384\n"
"Mvaa 124,232,124\nDT 52,1,17408\nMvaa 124,192,132\nDT 54,5,17408\nMvaa 96,196,136\nLD 12,48,84,1024\n"
"Mvaa 96,200,144\ns_add_u32 s84, s84, s85\nDT 56,1,18432\nDT 58,5,18432\nMvaa 96,204,152\nDT 60,1,19456\n"
"Mvaa 96,208,160\nDT 62,5,19456\nMvaa 96,16,168\nDT 212,1,20480\nMvaa 96,20,176\nDT 214,5,20480\nMvaa 96,24,184\n"
"DT 216,1,21504\nMvaa 96,28,0\nLD 12,48,84,2048\nMvaa 96,32,8\ns_add_u32 s84, s84, s85\nDT 218,5,21504\n"
"DT 220,2,13312\nMvaa 100,196,140\nDT 222,6,13312\nMvaa 100,200,148\nDT 224,2,14336\nMvaa 100,204,156\n"
"DT 226,6,14336\nCV 20,96,97\nMvaa 100,208,164\nDT 228,2,15360\nCV 21,98,99\nMvaa 100,16,172\nDT 230,6,15360\n"
"Mvaa 100,20,180\nLD 12,48,84,3072\nMvaa 100,24,188\nWL 5\nDT 232,2,16384\nDT 234,6,16384\nMvaa 100,28,4\n"
"s_addk_i32 m0, 4096\nDT 192,2,17408\nMvaa 100,32,12\ns_mov_b32 s84, 295296\nDT 194,6,17408\nMvaa 104,36,136\n"
"DT 196,2,18432\nMvaa 104,40,144\nDT 198,6,18432\nMvaa 104,44,152\nDT 200,2,19456\nCV 24,100,101\n"
"Mvaa 104,48,160\nLD 12,48,84,0\nMvaa 104,52,168\ns_add_u32 s84, s84, s85\nDT 202,6,19456\nDT 204,2,20480\n"
"Mvaa 104,56,176\nDT 206,6,20480\nCV 25,102,103\nMvaa 104,60,184\nDT 208,2,21504\nMvaa 104,212,0\n"
"DT 210,6,21504\nMvaa 104,216,8\nDT 16,3,13312\nMvaa 108,36,140\nDT 18,7,13312\nMvaa 108,40,148\n"
"LD 12,48,84,1024\nMvaa 108,44,156\ns_mov_b32 s84, 369152\nDT 20,3,14336\nDT 22,7,14336\nMvaa 108,48,164\n"
"s_add_u32 m0, s72, 0\nDT 24,3,15360\nCV 22,104,105\nMvaa 108,52,172\nDT 26,7,15360\nCV 23,106,107\n"
"Mvaa 108,56,180\nbuffer_store_dwordx4 v[20:23], v13, s[60:63], s87 offen sc1\nMvaa 108,60,188\nWL 5\n"
"DT 28,3,16384\nDT 30,7,16384\nMvaa 108,212,4\ns_mov_b32 s87, 360448\nDT 32,3,17408\nMvaa 108,216,12\n"
"DT 34,7,17408\nMvaa 112,220,136\nDT 36,3,18432\nMvaa 112,224,144\nDT 38,7,18432\nMvaa 112,228,152\n"
"DT 40,3,19456\nMvaa 112,232,160\nDT 42,7,19456\nCV 26,108,109\nMvaa 112,192,168\nDT 44,3,20480\nCV 27,110,111\n"
"Mvaa 112,196,176\nbuffer_store_dwordx4 v[24:27], v13, s[60:63], s88 offen sc1\nMvaa 112,200,184\n"
"s_waitcnt vmcnt(12)\nDT 46,7,20480\nDT 48,3,21504\nMvaa 112,204,0\ns_barrier\nDT 50,7,21504\n"
"v_lshrrev_b32 v17, 8, v13\nMvaa 112,208,8\ns_mov_b32 s88, 327680\nDT 52,0,22528\nv_bfe_u32 v18, v13, 4, 4\n"
"Mvaa 116,220,140\nLD 12,48,84,0\nMvaa 116,224,148\ns_add_u32 s84, s84, s85\nDT 54,4,22528\nDT 56,0,23552\n"
"Mvaa 116,228,156\nDT 58,4,23552\nv_mul_u32_u24 v18, 1152, v18\nMvaa 116,232,164\nDT 60,0,24576\n"
"v_lshl_add_u32 v11, v17, 4, v18\nMvaa 116,192,172\nDT 62,4,24576\nv_bfe_u32 v18, v13, 6, 1\nMvaa 116,196,180\n"
"DT 212,0,25600\nv_lshlrev_b32 v18, 1, v18\nMvaa 116,200,188\nWL 6\nLD 12,48,84,1024\nMvaa 116,204,4\n"
"s_add_u32 s84, s84, s85\nDT 214,4,25600\nDT 216,0,26624\nMvaa 116,208,12\nDT 218,4,26624\nCV 28,112,113\n"
"Mvaa 120,16,136\nDT 220,0,27648\nCV 29,114,115\n"
"Lw35_%=:\ns_cbranch_execz Lhub_%=\nMvaa 120,20,144\ns_min_u32 s0, s77, 3\nDT 222,4,27648\n"
"v_sub_u32 v17, v17, v18\nMvaa 120,24,152\ns_lshl_b32 s0, s0, 12\nDT 224,0,28672\nv_bfe_u32 v18, v13, 7, 1\n"
"Mvaa 120,28,160\ns_add_u32 s0, s0, Lw36_%=-Lstart_%=\nDT 226,4,28672\nv_bfe_u32 v19, v13, 5, 1\n"
"Mvaa 120,32,168\ns_add_u32 s0, s0, s30\ns_addc_u32 s1, s31, 0\nLD 12,48,84,2048\nMvaa 120,36,176\n"
"s_add_u32 s84, s84, s85\nDT 228,0,29696\nDT 230,4,29696\nMvaa 120,40,184\ns_load_dword s83, s[0:1], 0\n"
"DT 232,0,30720\nv_lshl_add_u32 v18, v18, 1, v19\nMvaa 120,44,0\nDT 234,4,30720\n"
"v_lshl_add_u32 v17, v17, 2, v18\nMvaa 120,48,8\nDT 192,1,22528\nv_and_b32 v18, 16, v13\nMvaa 124,16,140\n"
"DT 194,5,22528\nv_mov_b32 v19, 0x3f80\nMvaa 124,20,148\nDT 196,1,23552\nv_lshlrev_b32 v18, v18, v19\n"
"Mvaa 124,24,156\nLD 12,48,84,3072\nMvaa 124,28,164\ns_mov_b32 s84, 516608\nDT 198,5,23552\nDT 200,1,24576\n"
"Mvaa 124,32,172\ns_addk_i32 m0, 4096\nDT 202,5,24576\nv_cmp_eq_u32 vcc, 0, v17\nMvaa 124,36,180\n"
"DT 204,1,25600\nv_cndmask_b32 v19, 0, v18, vcc\nMvaa 124,40,188\nWL 6\nDT 206,5,25600\n"
"v_cmp_eq_u32 vcc, 1, v17\nMvaa 124,44,4\nDT 208,1,26624\nAW 240,19\nMvaa 124,48,12\nDT 210,5,26624\n"
"v_cndmask_b32 v19, 0, v18, vcc\nMvaaz 128,52,64\nLD 12,48,84,0\nMvaa 128,56,72\ns_add_u32 s84, s84, s85\n"
"DT 16,1,27648\nDT 18,5,27648\nMvaa 128,60,80\nDT 20,1,28672\nv_cmp_eq_u32 vcc, 2, v17\nMvaa 128,212,88\n"
"DT 22,5,28672\nAW 241,19\nMvaa 128,216,96\nDT 24,1,29696\nv_cndmask_b32 v19, 0, v18, vcc\nMvaa 128,220,104\n"
"DT 26,5,29696\nv_cmp_eq_u32 vcc, 3, v17\nMvaa 128,224,112\nDT 28,1,30720\nCV 20,116,117\nMvaa 128,228,120\n"
"LD 12,48,84,1024\nMvaa 128,232,128\ns_add_u32 s84, s84, s85\nDT 30,5,30720\nDT 32,2,22528\nMvaaz 132,52,68\n"
"DT 34,6,22528\nCV 21,118,119\nMvaa 132,56,76\nDT 36,2,23552\nAW 242,19\nMvaa 132,60,84\nDT 38,6,23552\n"
"v_cndmask_b32 v19, 0, v18, vcc\nMvaa 132,212,92\nDT 40,2,24576\nv_cmp_eq_u32 vcc, 4, v17\nMvaa 132,216,100\n"
"DT 42,6,24576\nAW 243,19\nMvaa 132,220,108\nLD 12,48,84,2048\nMvaa 132,224,116\nWL 5\nDT 44,2,25600\n"
"DT 46,6,25600\nMvaa 132,228,124\ns_add_u32 s84, s84, s85\nDT 48,2,26624\nv_cndmask_b32 v19, 0, v18, vcc\n"
"Mvaa 132,232,132\nDT 50,6,26624\nv_cmp_eq_u32 vcc, 5, v17\nMvaaz 136,192,64\nDT 52,2,27648\nAW 244,19\n"
"Mvaa 136,196,72\nDT 54,6,27648\nv_cndmask_b32 v19, 0, v18, vcc\nMvaa 136,200,80\nDT 56,2,28672\n"
"v_cmp_eq_u32 vcc, 6, v17\nMvaa 136,204,88\nLD 12,48,84,3072\nMvaa 136,208,96\ns_movk_i32 s84, 512\n"
"DT 58,6,28672\nDT 60,2,29696\nMvaa 136,16,104\ns_addk_i32 m0, 4096\nDT 62,6,29696\nAW 245,19\nMvaa 136,20,112\n"
"DT 212,2,30720\nv_cndmask_b32 v19, 0, v18, vcc\nMvaa 136,24,120\nDT 214,6,30720\nv_cmp_eq_u32 vcc, 7, v17\n"
"Mvaa 136,28,128\nDT 216,3,22528\nAW 246,19\nMvaaz 140,192,68\nDT 218,7,22528\nv_cndmask_b32 v19, 0, v18, vcc\n"
"Mvaa 140,196,76\nLD 12,48,84,0\nMvaa 140,200,84\ns_mov_b32 s84, 37376\nDT 220,3,23552\nDT 222,7,23552\n"
"Mvaa 140,204,92\ns_addk_i32 m0, 5120\nDT 224,3,24576\nAW 247,19\nMvaa 140,208,100\nDT 226,7,24576\n"
"v_mov_b32 v8, s35\nMvaa 140,16,108\nDT 228,3,25600\nCV 30,120,121\nMvaa 140,20,116\nWL 6\nDT 230,7,25600\n"
"CV 31,122,123\nMvaa 140,24,124\nDT 232,3,26624\nCV 22,124,125\nMvaa 140,28,132\ns_mov_b64 exec, 0\n"
"s_setpc_b64 s[0:1]\n"
"Lrl01_%=:\ns_mov_b64 exec, -1\nDT 234,7,26624\nCV 23,126,127\nMvaaz 144,32,64\n"
"buffer_store_dwordx4 v[28:31], v13, s[60:63], s87 offen sc1\nMvaa 144,36,72\nDT 192,3,27648\nDT 194,7,27648\n"
"Mvaa 144,40,80\ns_mov_b32 s87, 425984\nDT 196,3,28672\nMvaa 144,44,88\nDT 198,7,28672\nMvaa 144,48,96\n"
"buffer_store_dwordx4 v[20:23], v13, s[60:63], s88 offen sc1\nMvaa 144,52,104\nDT 200,3,29696\nDT 202,7,29696\n"
"Mvaa 144,56,112\ns_waitcnt vmcnt(13)\nDT 204,3,30720\nMvaa 144,60,120\ns_barrier\nDT 206,7,30720\n"
"Mvaa 144,212,128\ns_mov_b32 s88, 393216\nLD 12,48,84,0\nMvaaz 148,32,68\ns_add_u32 s84, s84, s85\n"
"DT 208,0,31744\nDT 210,4,31744\nMvaa 148,36,76\nDT 16,0,32768\nMvaa 148,40,84\nDT 18,4,32768\nMvaa 148,44,92\n"
"DT 20,0,33792\nMvaa 148,48,100\nDT 22,4,33792\nMvaa 148,52,108\nDT 24,0,34816\nMvaa 148,56,116\nWL 7\n"
"LD 12,48,84,1024\nMvaa 148,60,124\ns_add_u32 s84, s84, s85\nDT 26,4,34816\nDT 28,0,35840\nMvaa 148,212,132\n"
"DT 30,4,35840\nMvaaz 152,216,64\nDT 32,0,36864\nMvaa 152,220,72\nDT 34,4,36864\nMvaa 152,224,80\nDT 36,0,37888\n"
"Mvaa 152,228,88\nDT 38,4,37888\nMvaa 152,232,96\nLD 12,48,84,2048\nMvaa 152,192,104\ns_add_u32 s84, s84, s85\n"
"DT 40,0,38912\nDT 42,4,38912\nMvaa 152,196,112\nDT 44,0,39936\nMvaa 152,200,120\nDT 46,4,39936\n"
"Mvaa 152,204,128\nDT 48,1,31744\nMvaaz 156,216,68\nDT 50,5,31744\nMvaa 156,220,76\nDT 52,1,32768\n"
"Mvaa 156,224,84\nLD 12,48,84,3072\nMvaa 156,228,92\ns_mov_b32 s84, 184832\nDT 54,5,32768\nDT 56,1,33792\n"
"Mvaa 156,232,100\ns_addk_i32 m0, 4096\nDT 58,5,33792\nMvaa 156,192,108\nDT 60,1,34816\nMvaa 156,196,116\nWL 6\n"
"DT 62,5,34816\nMvaa 156,200,124\nDT 212,1,35840\nMvaa 156,204,132\nDT 214,5,35840\nMvaa 128,208,136\n"
"LD 12,48,84,0\nMvaa 128,16,144\ns_add_u32 s84, s84, s85\nDT 216,1,36864\nDT 218,5,36864\nMvaa 128,20,152\n"
"DT 220,1,37888\nMvaa 128,24,160\nDT 222,5,37888\nMvaa 128,28,168\nDT 224,1,38912\nMvaa 128,32,176\n"
"DT 226,5,38912\nMvaa 128,36,184\nDT 228,1,39936\nMvaa 128,40,0\nLD 12,48,84,1024\nMvaa 128,44,8\n"
"s_add_u32 s84, s84, s85\nDT 230,5,39936\nDT 232,2,31744\nMvaa 132,208,140\nDT 234,6,31744\nMvaa 132,16,148\n"
"DT 192,2,32768\nMvaa 132,20,156\nDT 194,6,32768\nCV 24,128,129\nMvaa 132,24,164\nDT 196,2,33792\nCV 25,130,131\n"
"Mvaa 132,28,172\ns_waitcnt vmcnt(6)\ns_bfm_b64 exec, 1, 24\nglobal_atomic_add v13, v9, s[98:99]\n"
"s_mov_b64 exec, -1\nMvaa 132,32,180\nDT 198,6,33792\nDT 200,2,34816\nMvaa 132,36,188\nWL 6\nDT 202,6,34816\n"
"Mvaa 132,40,4\nDT 204,2,35840\nMvaa 132,44,12\nLD 12,48,84,2048\nMvaa 136,48,136\ns_add_u32 s84, s84, s85\n"
"DT 206,6,35840\nDT 208,2,36864\nMvaa 136,52,144\nDT 210,6,36864\nMvaa 136,56,152\nDT 16,2,37888\nCV 28,132,133\n"
"Mvaa 136,60,160\nLD 12,48,84,3072\nMvaa 136,212,168\ns_mov_b32 s84, 332288\nDT 18,6,37888\nDT 20,2,38912\n"
"Mvaa 136,216,176\ns_addk_i32 m0, 4096\nDT 22,6,38912\nCV 29,134,135\nMvaa 136,220,184\nDT 24,2,39936\n"
"Mvaa 136,224,0\nDT 26,6,39936\nMvaa 136,228,8\nDT 28,3,31744\nMvaa 140,48,140\nDT 30,7,31744\nMvaa 140,52,148\n"
"LD 12,48,84,0\nMvaa 140,56,156\ns_mov_b32 s84, 368256\nDT 32,3,32768\nDT 34,7,32768\nMvaa 140,60,164\n"
"DT 36,3,33792\nCV 26,136,137\nMvaa 140,212,172\nDT 38,7,33792\nCV 27,138,139\nMvaa 140,216,180\n"
"buffer_store_dwordx4 v[24:27], v13, s[60:63], s87 offen sc1\nMvaa 140,220,188\nWL 5\nDT 40,3,34816\n"
"DT 42,7,34816\nMvaa 140,224,4\ns_mov_b32 s87, 491520\nDT 44,3,35840\nMvaa 140,228,12\nDT 46,7,35840\n"
"Mvaa 144,232,136\nDT 48,3,36864\nMvaa 144,192,144\nDT 50,7,36864\nMvaa 144,196,152\nDT 52,3,37888\n"
"Mvaa 144,200,160\nDT 54,7,37888\nCV 30,140,141\nMvaa 144,204,168\nDT 56,3,38912\nCV 31,142,143\n"
"Mvaa 144,208,176\nbuffer_store_dwordx4 v[28:31], v13, s[60:63], s88 offen sc1\nMvaa 144,16,184\ns_barrier\n"
"DT 58,7,38912\nDT 60,3,39936\nMvaa 144,20,0\ns_mov_b32 s88, 458752\nDT 62,7,39936\nMvaa 144,24,8\nDT 212,0,0\n"
"Mvaa 148,232,140\nLD 12,48,84,1024\nMvaa 148,192,148\ns_add_u32 s84, s84, s85\nDT 214,4,0\nDT 216,0,1024\n"
"Mvaa 148,196,156\nDT 218,4,1024\nCV 20,144,145\nMvaa 148,200,164\nDT 220,0,2048\nCV 21,146,147\n"
"Mvaa 148,204,172\nDT 222,4,2048\nMvaa 148,208,180\nLD 12,48,84,2048\nMvaa 148,16,188\nWL 5\nDT 224,0,3072\n"
"DT 226,4,3072\nMvaa 148,20,4\ns_add_u32 s84, s84, s85\nDT 228,0,4096\nMvaa 148,24,12\nDT 230,4,4096\n"
"Mvaa 152,28,136\nDT 232,0,5120\nMvaa 152,32,144\nDT 234,4,5120\nMvaa 152,36,152\nDT 192,0,6144\n"
"Mvaa 152,40,160\nLD 12,48,84,3072\nMvaa 152,44,168\ns_mov_b32 s84, 479872\nDT 194,4,6144\nDT 196,0,7168\n"
"Mvaa 152,48,176\ns_addk_i32 m0, 4096\nDT 198,4,7168\nCV 24,148,149\nMvaa 152,52,184\nDT 200,0,8192\n"
"CV 25,150,151\nMvaa 152,56,0\nDT 202,4,8192\nMvaa 152,60,8\nDT 204,1,0\nMvaa 156,28,140\nDT 206,5,0\n"
"Mvaa 156,32,148\nLD 12,48,84,0\nMvaa 156,36,156\ns_add_u32 s84, s84, s85\nDT 208,1,1024\nDT 210,5,1024\n"
"Mvaa 156,40,164\nDT 16,1,2048\nCV 22,152,153\nMvaa 156,44,172\nDT 18,5,2048\nCV 23,154,155\nMvaa 156,48,180\n"
"buffer_store_dwordx4 v[20:23], v13, s[60:63], s87 offen sc1\nMvaa 156,52,188\nWL 5\nDT 20,1,3072\nDT 22,5,3072\n"
"Mvaa 156,56,4\ns_mov_b32 s87, 557056\nDT 24,1,4096\nMvaa 156,60,12\nDT 26,5,4096\nMvaaz 96,212,64\n"
"LD 12,48,84,1024\nMvaa 96,216,72\ns_add_u32 s84, s84, s85\nDT 28,1,5120\nDT 30,5,5120\nMvaa 96,220,80\n"
"DT 32,1,6144\nCV 26,156,157\nMvaa 96,224,88\nDT 34,5,6144\nCV 27,158,159\nMvaa 96,228,96\n"
"buffer_store_dwordx4 v[24:27], v13, s[60:63], s88 offen sc1\nMvaa 96,232,104\nDT 36,1,7168\nDT 38,5,7168\n"
"Mvaa 96,192,112\ns_mov_b32 s88, 524288\nDT 40,1,8192\nMvaa 96,196,120\nDT 42,5,8192\nMvaa 96,200,128\n"
"LD 12,48,84,2048\nMvaaz 100,212,68\ns_add_u32 s84, s84, s85\nDT 44,2,0\nDT 46,6,0\nMvaa 100,216,76\n"
"DT 48,2,1024\nMvaa 100,220,84\nDT 50,6,1024\nMvaa 100,224,92\nDT 52,2,2048\nMvaa 100,228,100\nLD 12,48,84,3072\n"
"Mvaa 100,232,108\ns_mov_b32 s84, 627328\nDT 54,6,2048\nDT 56,2,3072\nMvaa 100,192,116\nWL 7\nDT 58,6,3072\n"
"Mvaa 100,196,124\ns_addk_i32 m0, 4096\nDT 60,2,4096\nMvaa 100,200,132\nDT 62,6,4096\nMvaaz 104,204,64\n"
"DT 212,2,5120\nMvaa 104,208,72\nDT 214,6,5120\nMvaa 104,16,80\nLD 12,48,84,0\nMvaa 104,20,88\n"
"s_movk_i32 s84, 640\nDT 216,2,6144\nDT 218,6,6144\nMvaa 104,24,96\ns_addk_i32 m0, 1024\nDT 220,2,7168\n"
"Mvaa 104,28,104\nDT 222,6,7168\nMvaa 104,32,112\nDT 224,2,8192\nMvaa 104,36,120\nDT 226,6,8192\n"
"Mvaa 104,40,128\nDT 228,3,0\nMvaaz 108,204,68\nLD 12,48,84,0\nMvaa 108,208,76\ns_add_u32 s84, s84, s85\n"
"DT 230,7,0\nDT 232,3,1024\nMvaa 108,16,84\nDT 234,7,1024\nMvaa 108,20,92\nDT 192,3,2048\nMvaa 108,24,100\n"
"DT 194,7,2048\nMvaa 108,28,108\nDT 196,3,3072\nMvaa 108,32,116\nWL 6\nDT 198,7,3072\nMvaa 108,36,124\n"
"DT 200,3,4096\nMvaa 108,40,132\nDT 202,7,4096\ns_nop 0\n"
"Lw36_%=:\ns_cbranch_execz Lrl01_%=\nMvaaz 112,44,64\nDT 204,3,5120\nMvaa 112,48,72\nDT 206,7,5120\n"
"Mvaa 112,52,80\nDT 208,3,6144\nMvaa 112,56,88\nDT 210,7,6144\nMvaa 112,60,96\nDT 16,3,7168\nMvaa 112,212,104\n"
"DT 18,7,7168\nMvaa 112,216,112\ns_waitcnt vmcnt(13)\nDT 20,3,8192\nMvaa 112,220,120\ns_barrier\nDT 22,7,8192\n"
"Mvaa 112,224,128\nLD 12,48,84,1024\nMvaaz 116,44,68\ns_add_u32 s84, s84, s85\nDT 24,0,13312\nDT 26,4,13312\n"
"Mvaa 116,48,76\nDT 28,0,14336\nMvaa 116,52,84\nDT 30,4,14336\nMvaa 116,56,92\nDT 32,0,15360\nMvaa 116,60,100\n"
"s_waitcnt vmcnt(5)\ns_bfm_b64 exec, 1, 28\nglobal_atomic_add v13, v9, s[98:99]\ns_mov_b64 exec, -1\n"
"Mvaa 116,212,108\nDT 34,4,15360\nDT 36,0,16384\nMvaa 116,216,116\nWL 7\nDT 38,4,16384\nMvaa 116,220,124\n"
"DT 40,0,17408\nMvaa 116,224,132\nLD 12,48,84,2048\nMvaaz 120,228,64\ns_add_u32 s84, s84, s85\nDT 42,4,17408\n"
"DT 44,0,18432\nMvaa 120,232,72\nDT 46,4,18432\nMvaa 120,192,80\nDT 48,0,19456\nMvaa 120,196,88\nDT 50,4,19456\n"
"Mvaa 120,200,96\nLD 12,48,84,3072\nMvaa 120,204,104\ns_mov_b32 s84, 148096\nDT 52,0,20480\nDT 54,4,20480\n"
"Mvaa 120,208,112\ns_addk_i32 m0, 4096\nDT 56,0,21504\nMvaa 120,16,120\nDT 58,4,21504\nMvaa 120,20,128\n"
"DT 60,1,13312\nMvaaz 124,228,68\nDT 62,5,13312\nMvaa 124,232,76\nDT 212,1,14336\nMvaa 124,192,84\n"
"LD 12,48,84,0\nMvaa 124,196,92\ns_add_u32 s84, s84, s85\nDT 214,5,14336\nDT 216,1,15360\nMvaa 124,200,100\n"
"DT 218,5,15360\nMvaa 124,204,108\nDT 220,1,16384\nMvaa 124,208,116\nWL 6\nDT 222,5,16384\nMvaa 124,16,124\n"
"DT 224,1,17408\nMvaa 124,20,132\nDT 226,5,17408\nMvaa 96,24,136\nLD 12,48,84,1024\nMvaa 96,28,144\n"
"s_add_u32 s84, s84, s85\nDT 228,1,18432\nDT 230,5,18432\nMvaa 96,32,152\nDT 232,1,19456\nMvaa 96,36,160\n"
"DT 234,5,19456\nMvaa 96,40,168\nDT 192,1,20480\nMvaa 96,44,176\nDT 194,5,20480\nMvaa 96,48,184\nDT 196,1,21504\n"
"Mvaa 96,52,0\nLD 12,48,84,2048\nMvaa 96,56,8\ns_add_u32 s84, s84, s85\nDT 198,5,21504\nDT 200,2,13312\n"
"Mvaa 100,24,140\nDT 202,6,13312\nMvaa 100,28,148\nDT 204,2,14336\nMvaa 100,32,156\nDT 206,6,14336\nCV 28,96,97\n"
"Mvaa 100,36,164\nDT 208,2,15360\nCV 29,98,99\nMvaa 100,40,172\nDT 210,6,15360\nMvaa 100,44,180\n"
"LD 12,48,84,3072\nMvaa 100,48,188\nWL 5\nDT 16,2,16384\nDT 18,6,16384\nMvaa 100,52,4\ns_addk_i32 m0, 4096\n"
"DT 20,2,17408\nMvaa 100,56,12\ns_mov_b32 s84, 295552\nDT 22,6,17408\nMvaa 104,60,136\nDT 24,2,18432\n"
"Mvaa 104,212,144\nDT 26,6,18432\nMvaa 104,216,152\nDT 28,2,19456\nCV 20,100,101\nMvaa 104,220,160\n"
"LD 12,48,84,0\nMvaa 104,224,168\ns_add_u32 s84, s84, s85\nDT 30,6,19456\nDT 32,2,20480\nMvaa 104,228,176\n"
"DT 34,6,20480\nCV 21,102,103\nMvaa 104,232,184\nDT 36,2,21504\nMvaa 104,192,0\nDT 38,6,21504\nMvaa 104,196,8\n"
"DT 40,3,13312\nMvaa 108,60,140\nDT 42,7,13312\nMvaa 108,212,148\nLD 12,48,84,1024\nMvaa 108,216,156\n"
"s_mov_b32 s84, 369408\nDT 44,3,14336\nDT 46,7,14336\nMvaa 108,220,164\ns_add_u32 m0, s72, 0\nDT 48,3,15360\n"
"CV 30,104,105\nMvaa 108,224,172\nDT 50,7,15360\nCV 31,106,107\nMvaa 108,228,180\n"
"buffer_store_dwordx4 v[28:31], v13, s[60:63], s87 offen sc1\nMvaa 108,232,188\nWL 5\nDT 52,3,16384\n"
"DT 54,7,16384\nMvaa 108,192,4\ns_mov_b32 s87, 622592\nDT 56,3,17408\nMvaa 108,196,12\nDT 58,7,17408\n"
"Mvaa 112,200,136\nDT 60,3,18432\nMvaa 112,204,144\nDT 62,7,18432\nMvaa 112,208,152\nDT 212,3,19456\n"
"Mvaa 112,16,160\nDT 214,7,19456\nCV 22,108,109\nMvaa 112,20,168\nDT 216,3,20480\nCV 23,110,111\n"
"Mvaa 112,24,176\nbuffer_store_dwordx4 v[20:23], v13, s[60:63], s88 offen sc1\nMvaa 112,28,184\n"
"s_waitcnt vmcnt(12)\nDT 218,7,20480\nDT 220,3,21504\nMvaa 112,32,0\ns_barrier\nDT 222,7,21504\nMvaa 112,36,8\n"
"s_mov_b32 s88, 589824\nDT 224,0,22528\nMvaa 116,200,140\ns_mov_b32 exec_lo, 0x11110000\n"
"s_mov_b32 exec_hi, 0x11111\nglobal_load_dword v14, v13, s[98:99] sc1\ns_mov_b64 exec, -1\nMvaa 116,204,148\n"
"DT 226,4,22528\nDT 228,0,23552\nMvaa 116,208,156\nDT 230,4,23552\nCV 24,112,113\nMvaa 116,16,164\n"
"DT 232,0,24576\nCV 25,114,115\nMvaa 116,20,172\nLD 12,48,84,0\nMvaa 116,24,180\ns_add_u32 s84, s84, s85\n"
"DT 234,4,24576\nDT 192,0,25600\nMvaa 116,28,188\nWL 6\nDT 194,4,25600\nMvaa 116,32,4\nDT 196,0,26624\n"
"Mvaa 116,36,12\nLD 12,48,84,1024\nMvaa 120,40,136\ns_add_u32 s84, s84, s85\nDT 198,4,26624\nDT 200,0,27648\n"
"Mvaa 120,44,144\nDT 202,4,27648\nMvaa 120,48,152\nDT 204,0,28672\nCV 28,116,117\nMvaa 120,52,160\n"
"DT 206,4,28672\nCV 29,118,119\nMvaa 120,56,168\nLD 12,48,84,2048\nMvaa 120,60,176\ns_add_u32 s84, s84, s85\n"
"DT 208,0,29696\nDT 210,4,29696\nMvaa 120,212,184\nDT 16,0,30720\nMvaa 120,216,0\nDT 18,4,30720\nMvaa 120,220,8\n"
"DT 20,1,22528\nMvaa 124,40,140\nDT 22,5,22528\nMvaa 124,44,148\nDT 24,1,23552\nMvaa 124,48,156\n"
"LD 12,48,84,3072\nMvaa 124,52,164\ns_mov_b32 s84, 516864\nDT 26,5,23552\nDT 28,1,24576\nMvaa 124,56,172\n"
"s_addk_i32 m0, 4096\nDT 30,5,24576\nCV 26,120,121\nMvaa 124,60,180\nDT 32,1,25600\nCV 27,122,123\n"
"Mvaa 124,212,188\nWL 6\nbuffer_store_dwordx4 v[24:27], v13, s[60:63], s87 offen sc1\nMvaa 124,216,4\n"
"DT 34,5,25600\nDT 36,1,26624\nMvaa 124,220,12\ns_mov_b32 s87, 688128\nDT 38,5,26624\nMvaaz 128,224,64\n"
"DT 40,1,27648\nMvaa 128,228,72\nLD 12,48,84,0\nMvaa 128,232,80\ns_add_u32 s84, s84, s85\nDT 42,5,27648\n"
"DT 44,1,28672\nMvaa 128,192,88\nDT 46,5,28672\nCV 30,124,125\nMvaa 128,196,96\nDT 48,1,29696\nCV 31,126,127\n"
"Mvaa 128,200,104\nbuffer_store_dwordx4 v[28:31], v13, s[60:63], s88 offen sc1\nMvaa 128,204,112\nDT 50,5,29696\n"
"DT 52,1,30720\nMvaa 128,208,120\ns_mov_b32 s88, 655360\nDT 54,5,30720\nMvaa 128,16,128\nDT 56,2,22528\n"
"Mvaaz 132,224,68\nLD 12,48,84,1024\nMvaa 132,228,76\ns_add_u32 s84, s84, s85\nDT 58,6,22528\nDT 60,2,23552\n"
"Mvaa 132,232,84\nDT 62,6,23552\nMvaa 132,192,92\nDT 212,2,24576\nMvaa 132,196,100\nDT 214,6,24576\n"
"Mvaa 132,200,108\nLD 12,48,84,2048\nMvaa 132,204,116\nWL 5\nDT 216,2,25600\nDT 218,6,25600\nMvaa 132,208,124\n"
"s_add_u32 s84, s84, s85\nDT 220,2,26624\nMvaa 132,16,132\nDT 222,6,26624\nMvaaz 136,20,64\nDT 224,2,27648\n"
"Mvaa 136,24,72\nDT 226,6,27648\nMvaa 136,28,80\nDT 228,2,28672\nMvaa 136,32,88\nLD 12,48,84,3072\n"
"Mvaa 136,36,96\ns_movk_i32 s84, 768\nDT 230,6,28672\nDT 232,2,29696\nMvaa 136,40,104\ns_addk_i32 m0, 4096\n"
"DT 234,6,29696\nMvaa 136,44,112\nDT 192,2,30720\nMvaa 136,48,120\nDT 194,6,30720\nMvaa 136,52,128\n"
"DT 196,3,22528\nMvaaz 140,20,68\nDT 198,7,22528\nMvaa 140,24,76\nLD 12,48,84,0\nMvaa 140,28,84\n"
"s_mov_b32 s84, 37632\nDT 200,3,23552\nDT 202,7,23552\nMvaa 140,32,92\ns_addk_i32 m0, 5120\nDT 204,3,24576\n"
"Mvaa 140,36,100\nDT 206,7,24576\ns_waitcnt vmcnt(11)\nv_sub_u32 v14, v14, v10\nMvaa 140,40,108\nDT 208,3,25600\n"
"v_cmp_eq_u32 vcc, 16, v14\nMvaa 140,44,116\nWL 6\nDT 210,7,25600\nMvaa 140,48,124\ns_bitcmp1_b64 vcc, s78\n"
"s_cbranch_scc0 Lb01s_%=\n"
"Lb01r_%=:\nDT 16,3,26624\nMvaa 140,52,132\ns_bitcmp1_b64 vcc, s78\ns_cselect_b32 s42, 1179648, 0\n"
"DT 18,7,26624\nMvaaz 144,56,64\nDT 20,3,27648\nMvaa 144,60,72\nDT 22,7,27648\nMvaa 144,212,80\nDT 24,3,28672\n"
"Mvaa 144,216,88\nDT 26,7,28672\nMvaa 144,220,96\nDT 28,3,29696\nMvaa 144,224,104\nDT 30,7,29696\n"
"Mvaa 144,228,112\ns_barrier\nbuffer_load_dwordx4 v[224:227], v11, s[36:39], s4 offen\nMvaa 144,232,120\n"
"DT 32,3,30720\nDT 34,7,30720\nMvaa 144,192,128\nDT 36,0,31744\nMvaaz 148,56,68\nDT 38,4,31744\nMvaa 148,60,76\n"
"LD 12,48,84,0\nMvaa 148,212,84\ns_add_u32 s84, s84, s85\nDT 40,0,32768\nDT 42,4,32768\nMvaa 148,216,92\n"
"DT 44,0,33792\nMvaa 148,220,100\nDT 46,4,33792\nMvaa 148,224,108\ns_waitcnt vmcnt(6)\ns_bfm_b64 exec, 1, 32\n"
"global_atomic_add v13, v9, s[98:99]\ns_mov_b64 exec, -1\nMvaa 148,228,116\nWL 5\nDT 48,0,34816\nDT 50,4,34816\n"
"Mvaa 148,232,124\nDT 52,0,35840\nMvaa 148,192,132\nDT 54,4,35840\nMvaaz 152,196,64\nLD 12,48,84,1024\n"
"Mvaa 152,200,72\ns_add_u32 s84, s84, s85\nDT 56,0,36864\nDT 58,4,36864\nMvaa 152,204,80\nDT 60,0,37888\n"
"Mvaa 152,208,88\nDT 62,4,37888\nMvaa 152,16,96\nLD 12,48,84,2048\nMvaa 152,20,104\ns_add_u32 s84, s84, s85\n"
"DT 212,0,38912\nDT 214,4,38912\nMvaa 152,24,112\nDT 216,0,39936\nMvaa 152,28,120\nDT 218,4,39936\n"
"Mvaa 152,32,128\nbuffer_load_dwordx4 v[160:163], v13, s[40:43], s74 offen offset:0 sc1\nMvaaz 156,196,68\n"
"DT 220,1,31744\nDT 222,5,31744\nMvaa 156,200,76\nDT 224,1,32768\nMvaa 156,204,84\nDT 226,5,32768\n"
"Mvaa 156,208,92\nLD 12,48,84,3072\nMvaa 156,16,100\ns_mov_b32 s84, 185088\nDT 228,1,33792\nDT 230,5,33792\n"
"Mvaa 156,20,108\ns_addk_i32 m0, 4096\nDT 232,1,34816\nMvaa 156,24,116\nWL 7\nDT 234,5,34816\nMvaa 156,28,124\n"
"DT 192,1,35840\nMvaa 156,32,132\nLD 12,48,84,0\nMvaa 128,36,136\ns_add_u32 s84, s84, s85\nDT 194,5,35840\n"
"DT 196,1,36864\nMvaa 128,40,144\nDT 198,5,36864\nMvaa 128,44,152\nDT 200,1,37888\nMvaa 128,48,160\n"
"DT 202,5,37888\nMvaa 128,52,168\nDT 204,1,38912\nMvaa 128,56,176\nDT 206,5,38912\nMvaa 128,60,184\n"
"LD 12,48,84,1024\nMvaa 128,212,0\ns_add_u32 s84, s84, s85\nDT 208,1,39936\nDT 210,5,39936\nMvaa 128,216,8\n"
"DT 16,2,31744\nMvaa 132,36,140\nDT 18,6,31744\nMvaa 132,40,148\n"
"buffer_load_dwordx4 v[164:167], v13, s[40:43], s74 offen offset:1024 sc1\nMvaa 132,44,156\nDT 20,2,32768\n"
"DT 22,6,32768\nMvaa 132,48,164\nDT 24,2,33792\nCV 20,128,129\nMvaa 132,52,172\nDT 26,6,33792\nCV 21,130,131\n"
"Mvaa 132,56,180\nLD 12,48,84,2048\nMvaa 132,60,188\nWL 5\nDT 28,2,34816\nDT 30,6,34816\nMvaa 132,212,4\n"
"s_add_u32 s84, s84, s85\nDT 32,2,35840\nMvaa 132,216,12\nDT 34,6,35840\nMvaa 136,220,136\nDT 36,2,36864\n"
"Mvaa 136,224,144\nDT 38,6,36864\nMvaa 136,228,152\nLD 12,48,84,3072\nMvaa 136,232,160\ns_mov_b32 s84, 332544\n"
"DT 40,2,37888\nDT 42,6,37888\nMvaa 136,192,168\ns_addk_i32 m0, 4096\nDT 44,2,38912\nCV 24,132,133\n"
"Mvaa 136,196,176\nDT 46,6,38912\nCV 25,134,135\nMvaa 136,200,184\nDT 48,2,39936\nMvaa 136,204,0\nDT 50,6,39936\n"
"Mvaa 136,208,8\ns_nop 0\n"
"Lw37_%=:\ns_cbranch_execz Lrl01_%=\nDT 52,3,31744\nMvaa 140,220,140\nLD 12,48,84,0\nMvaa 140,224,148\n"
"s_mov_b32 s84, 368512\nDT 54,7,31744\nDT 56,3,32768\nMvaa 140,228,156\nDT 58,7,32768\nCV 22,136,137\n"
"Mvaa 140,232,164\nDT 60,3,33792\nCV 23,138,139\nMvaa 140,192,172\n"
"buffer_store_dwordx4 v[20:23], v13, s[60:63], s87 offen sc1\nMvaa 140,196,180\nDT 62,7,33792\nDT 212,3,34816\n"
"Mvaa 140,200,188\nWL 6\nDT 214,7,34816\nMvaa 140,204,4\ns_mov_b32 s87, 753664\nDT 216,3,35840\nMvaa 140,208,12\n"
"buffer_load_dwordx4 v[168:171], v13, s[40:43], s74 offen offset:2048 sc1\nMvaa 144,16,136\nDT 218,7,35840\n"
"DT 220,3,36864\nMvaa 144,20,144\nDT 222,7,36864\nMvaa 144,24,152\nDT 224,3,37888\nCV 26,140,141\n"
"Mvaa 144,28,160\nDT 226,7,37888\nCV 27,142,143\nMvaa 144,32,168\n"
"buffer_store_dwordx4 v[24:27], v13, s[60:63], s88 offen sc1\nMvaa 144,36,176\nDT 228,3,38912\nDT 230,7,38912\n"
"Mvaa 144,40,184\ns_waitcnt vmcnt(16)\nDT 232,3,39936\nMvaa 144,44,0\ns_barrier\nDT 234,7,39936\nMvaa 144,48,8\n"
"s_mov_b32 s88, 720896\nLD 12,48,84,1024\nMvaa 148,16,140\ns_add_u32 s84, s84, s85\nDT 192,0,0\nDT 194,4,0\n"
"Mvaa 148,20,148\nDT 196,0,1024\nCV 28,144,145\nMvaa 148,24,156\nDT 198,4,1024\nCV 29,146,147\nMvaa 148,28,164\n"
"DT 200,0,2048\nMvaa 148,32,172\nDT 202,4,2048\nMvaa 148,36,180\n"
"buffer_load_dwordx4 v[172:175], v13, s[40:43], s74 offen offset:3072 sc1\nMvaa 148,40,188\nWL 6\nDT 204,0,3072\n"
"DT 206,4,3072\nMvaa 148,44,4\ns_add_u32 s74, s82, 16384\nDT 208,0,4096\nMvaa 148,48,12\nDT 210,4,4096\n"
"Mvaa 152,52,136\nLD 12,48,84,2048\nMvaa 152,56,144\ns_add_u32 s84, s84, s85\nDT 16,0,5120\nDT 18,4,5120\n"
"Mvaa 152,60,152\nDT 20,0,6144\nCV 20,148,149\nMvaa 152,212,160\nDT 22,4,6144\nCV 21,150,151\nMvaa 152,216,168\n"
"LD 12,48,84,3072\nMvaa 152,220,176\ns_mov_b32 s84, 480128\nDT 24,0,7168\nDT 26,4,7168\nMvaa 152,224,184\n"
"s_addk_i32 m0, 4096\nDT 28,0,8192\nMvaa 152,228,0\nDT 30,4,8192\nMvaa 152,232,8\nDT 32,1,0\nMvaa 156,52,140\n"
"DT 34,5,0\nMvaa 156,56,148\nDT 36,1,1024\nMvaa 156,60,156\nLD 12,48,84,0\nMvaa 156,212,164\n"
"s_add_u32 s84, s84, s85\nDT 38,5,1024\nDT 40,1,2048\nMvaa 156,216,172\nDT 42,5,2048\nCV 30,152,153\n"
"Mvaa 156,220,180\nDT 44,1,3072\nCV 31,154,155\nMvaa 156,224,188\nWL 6\n"
"buffer_store_dwordx4 v[28:31], v13, s[60:63], s87 offen sc1\nMvaa 156,228,4\nDT 46,5,3072\nDT 48,1,4096\n"
"Mvaa 156,232,12\ns_mov_b32 s87, 819200\nDT 50,5,4096\nMvaaz 96,192,64\nDT 52,1,5120\nMvaa 96,196,72\n"
"LD 12,48,84,1024\nMvaa 96,200,80\ns_add_u32 s84, s84, s85\nDT 54,5,5120\nDT 56,1,6144\nMvaa 96,204,88\n"
"DT 58,5,6144\nCV 22,156,157\nMvaa 96,208,96\nDT 60,1,7168\nCV 23,158,159\nMvaa 96,16,104\n"
"buffer_store_dwordx4 v[20:23], v13, s[60:63], s88 offen sc1\nMvaa 96,20,112\nDT 62,5,7168\nDT 212,1,8192\n"
"Mvaa 96,24,120\ns_mov_b32 s88, 786432\nDT 214,5,8192\nMvaa 96,28,128\nDT 216,2,0\nMvaaz 100,192,68\n"
"LD 12,48,84,2048\nMvaa 100,196,76\ns_add_u32 s84, s84, s85\nDT 218,6,0\nDT 220,2,1024\nMvaa 100,200,84\n"
"DT 222,6,1024\nMvaa 100,204,92\nDT 224,2,2048\nMvaa 100,208,100\n"
"buffer_load_dwordx4 v[176:179], v13, s[40:43], s75 offen offset:0 sc1\nMvaa 100,16,108\nDT 226,6,2048\n"
"DT 228,2,3072\nMvaa 100,20,116\nWL 6\nDT 230,6,3072\nMvaa 100,24,124\nDT 232,2,4096\nMvaa 100,28,132\n"
"LD 12,48,84,3072\nMvaaz 104,32,64\ns_mov_b32 s84, 627584\nDT 234,6,4096\nDT 192,2,5120\nMvaa 104,36,72\n"
"s_addk_i32 m0, 4096\nDT 194,6,5120\nMvaa 104,40,80\nDT 196,2,6144\nMvaa 104,44,88\nLD 12,48,84,0\n"
"Mvaa 104,48,96\ns_movk_i32 s84, 896\nDT 198,6,6144\nDT 200,2,7168\nMvaa 104,52,104\ns_addk_i32 m0, 1024\n"
"DT 202,6,7168\nMvaa 104,56,112\nDT 204,2,8192\nMvaa 104,60,120\n"
"buffer_load_dwordx4 v[180:183], v13, s[40:43], s75 offen offset:1024 sc1\nMvaa 104,212,128\nDT 206,6,8192\n"
"DT 208,3,0\nMvaaz 108,32,68\nDT 210,7,0\nMvaa 108,36,76\nDT 16,3,1024\nMvaa 108,40,84\nLD 12,48,84,0\n"
"Mvaa 108,44,92\ns_add_u32 s84, s84, s85\nDT 18,7,1024\nDT 20,3,2048\nMvaa 108,48,100\nDT 22,7,2048\n"
"Mvaa 108,52,108\nDT 24,3,3072\nMvaa 108,56,116\nWL 6\nDT 26,7,3072\nMvaa 108,60,124\nDT 28,3,4096\n"
"Mvaa 108,212,132\nDT 30,7,4096\nMvaaz 112,216,64\nDT 32,3,5120\nMvaa 112,220,72\nDT 34,7,5120\nMvaa 112,224,80\n"
"buffer_load_dwordx4 v[184:187], v13, s[40:43], s75 offen offset:2048 sc1\nMvaa 112,228,88\nDT 36,3,6144\n"
"DT 38,7,6144\nMvaa 112,232,96\nDT 40,3,7168\nMvaa 112,192,104\nDT 42,7,7168\nMvaa 112,196,112\n"
"s_waitcnt vmcnt(18)\nDT 44,3,8192\nMvaa 112,200,120\ns_barrier\nDT 46,7,8192\nMvaa 112,204,128\n"
"LD 12,48,84,1024\nMvaaz 116,216,68\ns_add_u32 s84, s84, s85\nDT 48,0,13312\nDT 50,4,13312\nMvaa 116,220,76\n"
"DT 52,0,14336\nMvaa 116,224,84\nDT 54,4,14336\nMvaa 116,228,92\nDT 56,0,15360\nMvaa 116,232,100\nDT 58,4,15360\n"
"Mvaa 116,192,108\nDT 60,0,16384\nMvaa 116,196,116\nWL 7\nLD 12,48,84,2048\nMvaa 116,200,124\n"
"s_add_u32 s84, s84, s85\nDT 62,4,16384\nDT 212,0,17408\nMvaa 116,204,132\nDT 214,4,17408\nMvaaz 120,208,64\n"
"DT 216,0,18432\nMvaa 120,16,72\nDT 218,4,18432\nMvaa 120,20,80\nDT 220,0,19456\nMvaa 120,24,88\nDT 222,4,19456\n"
"Mvaa 120,28,96\nLD 12,48,84,3072\nMvaa 120,32,104\ns_mov_b32 s84, 148352\nDT 224,0,20480\nDT 226,4,20480\n"
"Mvaa 120,36,112\ns_addk_i32 m0, 4096\nDT 228,0,21504\nMvaa 120,40,120\nDT 230,4,21504\nMvaa 120,44,128\n"
"buffer_load_dwordx4 v[188:191], v13, s[40:43], s75 offen offset:3072 sc1\nMvaaz 124,208,68\n"
"s_add_u32 s75, s82, 20480\nDT 232,1,13312\nDT 234,5,13312\nMvaa 124,16,76\nDT 192,1,14336\nMvaa 124,20,84\n"
"DT 194,5,14336\nMvaa 124,24,92\nLD 12,48,84,0\nMvaa 124,28,100\ns_add_u32 s84, s84, s85\nDT 196,1,15360\n"
"DT 198,5,15360\nMvaa 124,32,108\nDT 200,1,16384\nMvaa 124,36,116\nWL 7\nDT 202,5,16384\nMvaa 124,40,124\n"
"DT 204,1,17408\nMvaa 124,44,132\nDT 206,5,17408\nMvaa 96,48,136\nLD 12,48,84,1024\nMvaa 96,52,144\n"
"s_add_u32 s84, s84, s85\nDT 208,1,18432\nDT 210,5,18432\nMvaa 96,56,152\nDT 16,1,19456\nMvaa 96,60,160\n"
"DT 18,5,19456\nMvaa 96,212,168\ns_waitcnt vmcnt(13)\ns_bfm_b64 exec, 1, 36\n"
"global_atomic_add v13, v9, s[98:99]\ns_mov_b64 exec, -1\nMvaa 96,216,176\nDT 20,1,20480\nDT 22,5,20480\n"
"Mvaa 96,220,184\nDT 24,1,21504\nMvaa 96,224,0\nDT 26,5,21504\nMvaa 96,228,8\nLD 12,48,84,2048\nMvaa 100,48,140\n"
"s_add_u32 s84, s84, s85\nDT 28,2,13312\nDT 30,6,13312\nMvaa 100,52,148\nDT 32,2,14336\nMvaa 100,56,156\n"
"DT 34,6,14336\nCV 24,96,97\nMvaa 100,60,164\nDT 36,2,15360\nCV 25,98,99\nMvaa 100,212,172\n"
"buffer_load_dwordx4 v[192:195], v13, s[40:43], s74 offen offset:0 sc1\nMvaa 100,216,180\nDT 38,6,15360\n"
"DT 40,2,16384\nMvaa 100,220,188\nWL 7\nDT 42,6,16384\nMvaa 100,224,4\nDT 44,2,17408\nMvaa 100,228,12\n"
"LD 12,48,84,3072\nMvaa 104,232,136\ns_mov_b32 s84, 295808\nDT 46,6,17408\nDT 48,2,18432\nMvaa 104,192,144\n"
"s_addk_i32 m0, 4096\nDT 50,6,18432\nCV 28,100,101\nMvaa 104,196,152\nDT 52,2,19456\nCV 29,102,103\n"
"Mvaa 104,200,160\nLD 12,48,84,0\nMvaa 104,204,168\ns_add_u32 s84, s84, s85\nDT 54,6,19456\nDT 56,2,20480\n"
"Mvaa 104,208,176\nDT 58,6,20480\nMvaa 104,16,184\nDT 60,2,21504\nMvaa 104,20,0\nDT 62,6,21504\nMvaa 104,24,8\n"
"buffer_load_dwordx4 v[196:199], v13, s[40:43], s74 offen offset:1024 sc1\nMvaa 108,232,140\nDT 212,3,13312\n"
"DT 214,7,13312\nMvaa 108,192,148\nDT 216,3,14336\nMvaa 108,196,156\nDT 218,7,14336\nCV 26,104,105\n"
"Mvaa 108,200,164\nLD 12,48,84,1024\nMvaa 108,204,172\ns_mov_b32 s84, 369664\nDT 220,3,15360\nDT 222,7,15360\n"
"Mvaa 108,208,180\ns_add_u32 m0, s72, 0\nDT 224,3,16384\nCV 27,106,107\nMvaa 108,16,188\nWL 7\nDT 226,7,16384\n"
"Mvaa 108,20,4\nbuffer_store_dwordx4 v[24:27], v13, s[60:63], s87 offen sc1\nMvaa 108,24,12\nDT 228,3,17408\n"
"DT 230,7,17408\nMvaa 112,28,136\ns_mov_b32 s87, 884736\nDT 232,3,18432\nMvaa 112,32,144\nDT 234,7,18432\n"
"Mvaa 112,36,152\nDT 192,3,19456\nCV 30,108,109\nMvaa 112,40,160\nDT 194,7,19456\nCV 31,110,111\n"
"Mvaa 112,44,168\nbuffer_store_dwordx4 v[28:31], v13, s[60:63], s88 offen sc1\nMvaa 112,48,176\nDT 196,3,20480\n"
"DT 198,7,20480\nMvaa 112,52,184\ns_waitcnt vmcnt(16)\nDT 200,3,21504\nMvaa 112,56,0\ns_barrier\nDT 202,7,21504\n"
"Mvaa 112,60,8\ns_mov_b32 s88, 851968\nLD 12,48,84,0\nMvaa 116,28,140\ns_add_u32 s84, s84, s85\nDT 204,0,22528\n"
"DT 206,4,22528\nMvaa 116,32,148\nDT 208,0,23552\nCV 20,112,113\nMvaa 116,36,156\nDT 210,4,23552\nCV 21,114,115\n"
"Mvaa 116,40,164\nbuffer_load_dwordx4 v[200:203], v13, s[40:43], s74 offen offset:2048 sc1\nMvaa 116,44,172\n"
"DT 16,0,24576\nDT 18,4,24576\nMvaa 116,48,180\nDT 20,0,25600\nMvaa 116,52,188\nWL 7\nDT 22,4,25600\n"
"Mvaa 116,56,4\nLD 12,48,84,1024\nMvaa 116,60,12\ns_add_u32 s84, s84, s85\nDT 24,0,26624\nDT 26,4,26624\n"
"Mvaa 120,212,136\nDT 28,0,27648\nMvaa 120,216,144\nDT 30,4,27648\nMvaa 120,220,152\nDT 32,0,28672\n"
"CV 24,116,117\nMvaa 120,224,160\nDT 34,4,28672\nCV 25,118,119\nMvaa 120,228,168\nLD 12,48,84,2048\n"
"Mvaa 120,232,176\ns_add_u32 s84, s84, s85\nDT 36,0,29696\nDT 38,4,29696\nMvaa 120,192,184\nDT 40,0,30720\n"
"Mvaa 120,196,0\nDT 42,4,30720\nMvaa 120,200,8\nDT 44,1,22528\nMvaa 124,212,140\nDT 46,5,22528\n"
"Mvaa 124,216,148\nDT 48,1,23552\nMvaa 124,220,156\nLD 12,48,84,3072\nMvaa 124,224,164\ns_mov_b32 s84, 517120\n"
"DT 50,5,23552\nDT 52,1,24576\nMvaa 124,228,172\ns_addk_i32 m0, 4096\nDT 54,5,24576\nCV 22,120,121\n"
"Mvaa 124,232,180\nDT 56,1,25600\nCV 23,122,123\nMvaa 124,192,188\nWL 6\n"
"buffer_load_dwordx4 v[204:207], v13, s[40:43], s74 offen offset:3072 sc1\nMvaa 124,196,4\nDT 58,5,25600\n"
"DT 60,1,26624\nMvaa 124,200,12\nDT 62,5,26624\nMvaaz 128,204,64\nDT 212,1,27648\nMvaa 128,208,72\n"
"LD 12,48,84,0\nMvaa 128,16,80\ns_add_u32 s84, s84, s85\nDT 214,5,27648\nDT 216,1,28672\nMvaa 128,20,88\n"
"DT 218,5,28672\nCV 26,124,125\nMvaa 128,24,96\nDT 220,1,29696\nCV 27,126,127\nMvaa 128,28,104\n"
"buffer_store_dwordx4 v[20:23], v13, s[60:63], s87 offen sc1\nMvaa 128,32,112\nDT 222,5,29696\nDT 224,1,30720\n"
"Mvaa 128,36,120\ns_mov_b32 s87, 950272\nDT 226,5,30720\nMvaa 128,40,128\nDT 228,2,22528\nMvaaz 132,204,68\n"
"LD 12,48,84,1024\nMvaa 132,208,76\ns_add_u32 s84, s84, s85\nDT 230,6,22528\nDT 232,2,23552\nMvaa 132,16,84\n"
"Lw38_%=:\ns_cbranch_execz Lrl01_%=\nDT 234,6,23552\nMvaa 132,20,92\nDT 192,2,24576\nMvaa 132,24,100\n"
"buffer_store_dwordx4 v[24:27], v13, s[60:63], s88 offen sc1\nMvaa 132,28,108\nDT 194,6,24576\nDT 196,2,25600\n"
"Mvaa 132,32,116\nWL 6\nDT 198,6,25600\nMvaa 132,36,124\ns_mov_b32 s88, 917504\nDT 200,2,26624\nMvaa 132,40,132\n"
"LD 12,48,84,2048\nMvaaz 136,44,64\ns_add_u32 s84, s84, s85\nDT 202,6,26624\nDT 204,2,27648\nMvaa 136,48,72\n"
"DT 206,6,27648\nMvaa 136,52,80\nDT 208,2,28672\nMvaa 136,56,88\nLD 12,48,84,3072\nMvaa 136,60,96\n"
"s_movk_i32 s84, 1024\nDT 210,6,28672\nDT 16,2,29696\nMvaa 136,212,104\ns_addk_i32 m0, 4096\nDT 18,6,29696\n"
"Mvaa 136,216,112\nDT 20,2,30720\nMvaa 136,220,120\n"
"buffer_load_dwordx4 v[208:211], v13, s[40:43], s75 offen offset:0 sc1\nMvaa 136,224,128\nDT 22,6,30720\n"
"DT 24,3,22528\nMvaaz 140,44,68\nDT 26,7,22528\nMvaa 140,48,76\nDT 28,3,23552\nMvaa 140,52,84\nLD 12,48,84,0\n"
"Mvaa 140,56,92\ns_mov_b32 s84, 37888\nDT 30,7,23552\nDT 32,3,24576\nMvaa 140,60,100\ns_addk_i32 m0, 5120\n"
"DT 34,7,24576\nMvaa 140,212,108\nDT 36,3,25600\nMvaa 140,216,116\nWL 6\nDT 38,7,25600\nMvaa 140,220,124\n"
"buffer_load_dwordx4 v[212:215], v13, s[40:43], s75 offen offset:1024 sc1\nMvaa 140,224,132\nDT 40,3,26624\n"
"DT 42,7,26624\nMvaaz 144,228,64\nDT 44,3,27648\nMvaa 144,232,72\nDT 46,7,27648\nMvaa 144,192,80\nDT 48,3,28672\n"
"Mvaa 144,196,88\nDT 50,7,28672\nMvaa 144,200,96\nDT 52,3,29696\nMvaa 144,204,104\nDT 54,7,29696\n"
"Mvaa 144,208,112\ns_waitcnt vmcnt(17)\nDT 56,3,30720\nMvaa 144,16,120\ns_barrier\nDT 58,7,30720\n"
"Mvaa 144,20,128\nLD 12,48,84,0\nMvaaz 148,228,68\ns_add_u32 s84, s84, s85\nDT 60,0,31744\nDT 62,4,31744\n"
"Mvaa 148,232,76\nDT 212,0,32768\nMvaa 148,192,84\nDT 214,4,32768\nMvaa 148,196,92\nDT 216,0,33792\n"
"Mvaa 148,200,100\nDT 218,4,33792\nMvaa 148,204,108\nDT 220,0,34816\nMvaa 148,208,116\nWL 7\nLD 12,48,84,1024\n"
"Mvaa 148,16,124\ns_add_u32 s84, s84, s85\nDT 222,4,34816\nDT 224,0,35840\nMvaa 148,20,132\nDT 226,4,35840\n"
"Mvaaz 152,24,64\nDT 228,0,36864\nMvaa 152,28,72\n"
"buffer_load_dwordx4 v[216:219], v13, s[40:43], s75 offen offset:2048 sc1\nMvaa 152,32,80\nDT 230,4,36864\n"
"DT 232,0,37888\nMvaa 152,36,88\nDT 234,4,37888\nMvaa 152,40,96\nDT 192,0,38912\nMvaa 152,44,104\n"
"LD 12,48,84,2048\nMvaa 152,48,112\ns_add_u32 s84, s84, s85\nDT 194,4,38912\nDT 196,0,39936\nMvaa 152,52,120\n"
"DT 198,4,39936\nMvaa 152,56,128\nDT 200,1,31744\nMvaaz 156,24,68\nDT 202,5,31744\nMvaa 156,28,76\n"
"DT 204,1,32768\nMvaa 156,32,84\nLD 12,48,84,3072\nMvaa 156,36,92\ns_mov_b32 s84, 185344\nDT 206,5,32768\n"
"DT 208,1,33792\nMvaa 156,40,100\ns_addk_i32 m0, 4096\nDT 210,5,33792\nMvaa 156,44,108\nDT 16,1,34816\n"
"Mvaa 156,48,116\nWL 6\nDT 18,5,34816\nMvaa 156,52,124\nDT 20,1,35840\nMvaa 156,56,132\nDT 22,5,35840\n"
"Mvaa 128,60,136\nLD 12,48,84,0\nMvaa 128,212,144\ns_add_u32 s84, s84, s85\nDT 24,1,36864\nDT 26,5,36864\n"
"Mvaa 128,216,152\nDT 28,1,37888\nMvaa 128,220,160\nDT 30,5,37888\nMvaa 128,224,168\n"
"buffer_load_dwordx4 v[220:223], v13, s[40:43], s75 offen offset:3072 sc1\nMvaa 128,228,176\nDT 32,1,38912\n"
"DT 34,5,38912\nMvaa 128,232,184\nDT 36,1,39936\nMvaa 128,192,0\nDT 38,5,39936\nMvaa 128,196,8\n"
"LD 12,48,84,1024\nMvaa 132,60,140\ns_add_u32 s84, s84, s85\nDT 40,2,31744\nDT 42,6,31744\nMvaa 132,212,148\n"
"DT 44,2,32768\nMvaa 132,216,156\nDT 46,6,32768\nCV 28,128,129\nMvaa 132,220,164\ns_waitcnt vmcnt(13)\n"
"s_bfm_b64 exec, 1, 40\nglobal_atomic_add v13, v9, s[98:99]\ns_mov_b64 exec, -1\nMvaa 132,224,172\n"
"DT 48,2,33792\nDT 50,6,33792\nMvaa 132,228,180\nDT 52,2,34816\nCV 29,130,131\nMvaa 132,232,188\nWL 7\n"
"DT 54,6,34816\nMvaa 132,192,4\nLD 12,48,84,2048\nMvaa 132,196,12\ns_add_u32 s84, s84, s85\nDT 56,2,35840\n"
"DT 58,6,35840\nMvaa 136,200,136\nDT 60,2,36864\nMvaa 136,204,144\nDT 62,6,36864\nMvaa 136,208,152\n"
"DT 212,2,37888\nCV 20,132,133\nMvaa 136,16,160\nLD 12,48,84,3072\nMvaa 136,20,168\ns_mov_b32 s84, 332800\n"
"DT 214,6,37888\nDT 216,2,38912\nMvaa 136,24,176\ns_addk_i32 m0, 4096\nDT 218,6,38912\nCV 21,134,135\n"
"Mvaa 136,28,184\nDT 220,2,39936\nMvaa 136,32,0\nDT 222,6,39936\nMvaa 136,36,8\nDT 224,3,31744\n"
"Mvaa 140,200,140\nDT 226,7,31744\nMvaa 140,204,148\nLD 12,48,84,0\nMvaa 140,208,156\nDT 228,3,32768\n"
"DT 230,7,32768\nMvaa 140,16,164\nDT 232,3,33792\nCV 30,136,137\nMvaa 140,20,172\nDT 234,7,33792\nCV 31,138,139\n"
"Mvaa 140,24,180\nbuffer_store_dwordx4 v[28:31], v13, s[60:63], s87 offen sc1\nMvaa 140,28,188\nWL 5\n"
"DT 192,3,34816\nDT 194,7,34816\nMvaa 140,32,4\ns_mov_b32 s87, 1015808\nDT 196,3,35840\nMvaa 140,36,12\n"
"DT 198,7,35840\nMvaa 144,40,136\nDT 200,3,36864\nMvaa 144,44,144\nDT 202,7,36864\nMvaa 144,48,152\n"
"DT 204,3,37888\nMvaa 144,52,160\nDT 206,7,37888\nCV 22,140,141\nMvaa 144,56,168\nDT 208,3,38912\nCV 23,142,143\n"
"Mvaa 144,60,176\nbuffer_store_dwordx4 v[20:23], v13, s[60:63], s88 offen sc1\nMvaa 144,212,184\n"
"s_waitcnt vmcnt(15)\nDT 210,7,38912\nDT 16,3,39936\nMvaa 144,216,0\ns_barrier\nDT 18,7,39936\nMvaa 144,220,8\n"
"s_mov_b32 s88, 983040\nDT 20,0,0\nMvaa 148,40,140\nDT 22,4,0\nMvaa 148,44,148\nDT 24,0,1024\nMvaa 148,48,156\n"
"DT 26,4,1024\nCV 24,144,145\nMvaa 148,52,164\nDT 28,0,2048\nCV 25,146,147\nMvaa 148,56,172\nDT 30,4,2048\n"
"Mvaa 148,60,180\nDT 32,0,3072\nMvaa 148,212,188\nWL 6\nDT 34,4,3072\nMvaa 148,216,4\nDT 36,0,4096\n"
"Mvaa 148,220,12\nDT 38,4,4096\nMvaa 152,224,136\nDT 40,0,5120\nMvaa 152,228,144\nDT 42,4,5120\n"
"Mvaa 152,232,152\nDT 44,0,6144\nMvaa 152,192,160\nDT 46,4,6144\nCV 28,148,149\nMvaa 152,196,168\nDT 48,0,7168\n"
"CV 29,150,151\nMvaa 152,200,176\nDT 50,4,7168\nMvaa 152,204,184\nDT 52,0,8192\nMvaa 152,208,0\nDT 54,4,8192\n"
"Mvaa 152,16,8\nDT 56,1,0\nMvaa 156,224,140\nDT 58,5,0\nMvaa 156,228,148\nDT 60,1,1024\nMvaa 156,232,156\n"
"DT 62,5,1024\nMvaa 156,192,164\nDT 212,1,2048\nCV 26,152,153\nMvaa 156,196,172\nDT 214,5,2048\nCV 27,154,155\n"
"Mvaa 156,200,180\nbuffer_store_dwordx4 v[24:27], v13, s[60:63], s87 offen sc1\nMvaa 156,204,188\nWL 5\n"
"DT 216,1,3072\nDT 218,5,3072\nMvaa 156,208,4\ns_mov_b32 s87, 1081344\nDT 220,1,4096\nMvaa 156,16,12\n"
"DT 222,5,4096\nMvaaz 96,20,64\nDT 224,1,5120\nMvaa 96,24,72\nDT 226,5,5120\nMvaa 96,28,80\nDT 228,1,6144\n"
"Mvaa 96,32,88\nDT 230,5,6144\nCV 30,156,157\nMvaa 96,36,96\nDT 232,1,7168\nCV 31,158,159\nMvaa 96,40,104\n"
"buffer_store_dwordx4 v[28:31], v13, s[60:63], s88 offen sc1\nMvaa 96,44,112\nDT 234,5,7168\nDT 192,1,8192\n"
"Mvaa 96,48,120\ns_mov_b32 s88, 1048576\nDT 194,5,8192\nMvaa 96,52,128\nDT 196,2,0\nMvaaz 100,20,68\nDT 198,6,0\n"
"Mvaa 100,24,76\nDT 200,2,1024\nMvaa 100,28,84\nDT 202,6,1024\nMvaa 100,32,92\nDT 204,2,2048\nMvaa 100,36,100\n"
"DT 206,6,2048\nMvaa 100,40,108\nDT 208,2,3072\nMvaa 100,44,116\nWL 6\nDT 210,6,3072\nMvaa 100,48,124\n"
"DT 16,2,4096\nMvaa 100,52,132\nDT 18,6,4096\nMvaaz 104,56,64\nDT 20,2,5120\nMvaa 104,60,72\nDT 22,6,5120\n"
"Mvaa 104,212,80\nDT 24,2,6144\nMvaa 104,216,88\nDT 26,6,6144\nMvaa 104,220,96\nDT 28,2,7168\nMvaa 104,224,104\n"
"DT 30,6,7168\nMvaa 104,228,112\nDT 32,2,8192\nMvaa 104,232,120\nDT 34,6,8192\nMvaa 104,192,128\nDT 36,3,0\n"
"Mvaaz 108,56,68\nDT 38,7,0\nMvaa 108,60,76\nDT 40,3,1024\nMvaa 108,212,84\nDT 42,7,1024\nMvaa 108,216,92\n"
"DT 44,3,2048\nMvaa 108,220,100\nDT 46,7,2048\nMvaa 108,224,108\nDT 48,3,3072\nMvaa 108,228,116\nWL 6\n"
"DT 50,7,3072\nMvaa 108,232,124\nDT 52,3,4096\nMvaa 108,192,132\nDT 54,7,4096\nMvaaz 112,196,64\nDT 56,3,5120\n"
"Mvaa 112,200,72\nDT 58,7,5120\nMvaa 112,204,80\nDT 60,3,6144\nMvaa 112,208,88\nDT 62,7,6144\nMvaa 112,16,96\n"
"DT 212,3,7168\nMvaa 112,20,104\nDT 214,7,7168\nMvaa 112,24,112\ns_waitcnt vmcnt(4)\nDT 216,3,8192\n"
"Mvaa 112,28,120\ns_barrier\nDT 218,7,8192\nMvaa 112,32,128\nDT 220,0,13312\nMvaaz 116,196,68\nDT 222,4,13312\n"
"Mvaa 116,200,76\nDT 224,0,14336\nMvaa 116,204,84\nDT 226,4,14336\nMvaa 116,208,92\nDT 228,0,15360\n"
"Mvaa 116,16,100\nDT 230,4,15360\nMvaa 116,20,108\nDT 232,0,16384\nMvaa 116,24,116\nWL 6\nDT 234,4,16384\n"
"Mvaa 116,28,124\nDT 192,0,17408\nMvaa 116,32,132\nDT 194,4,17408\nMvaaz 120,36,64\nDT 196,0,18432\n"
"Mvaa 120,40,72\nDT 198,4,18432\nMvaa 120,44,80\nDT 200,0,19456\nMvaa 120,48,88\nDT 202,4,19456\nMvaa 120,52,96\n"
"DT 204,0,20480\nMvaa 120,56,104\nDT 206,4,20480\nMvaa 120,60,112\nDT 208,0,21504\nMvaa 120,212,120\n"
"DT 210,4,21504\nMvaa 120,216,128\nDT 16,1,13312\nMvaaz 124,36,68\nDT 18,5,13312\nMvaa 124,40,76\nDT 20,1,14336\n"
"Mvaa 124,44,84\nDT 22,5,14336\nMvaa 124,48,92\nDT 24,1,15360\nMvaa 124,52,100\nDT 26,5,15360\nMvaa 124,56,108\n"
"DT 28,1,16384\nMvaa 124,60,116\nWL 6\nDT 30,5,16384\nMvaa 124,212,124\nDT 32,1,17408\nMvaa 124,216,132\n"
"DT 34,5,17408\nMvaa 96,220,136\nDT 36,1,18432\nMvaa 96,224,144\nDT 38,5,18432\nMvaa 96,228,152\nDT 40,1,19456\n"
"Mvaa 96,232,160\ns_waitcnt vmcnt(0)\ns_bfm_b64 exec, 1, 44\nglobal_atomic_add v13, v9, s[98:99]\n"
"s_mov_b64 exec, -1\nMvaa 96,192,168\nDT 42,5,19456\nDT 44,1,20480\nMvaa 96,196,176\nDT 46,5,20480\n"
"Mvaa 96,200,184\nDT 48,1,21504\nMvaa 96,204,0\nDT 50,5,21504\nMvaa 96,208,8\nDT 52,2,13312\nMvaa 100,220,140\n"
"DT 54,6,13312\nMvaa 100,224,148\nDT 56,2,14336\nMvaa 100,228,156\nDT 58,6,14336\nMvaa 100,232,164\n"
"DT 60,2,15360\nCV 20,96,97\nMvaa 100,192,172\nDT 62,6,15360\nCV 21,98,99\nMvaa 100,196,180\nDT 212,2,16384\n"
"Mvaa 100,200,188\nWL 6\nDT 214,6,16384\nMvaa 100,204,4\nDT 216,2,17408\nMvaa 100,208,12\n"
"Lw39_%=:\ns_cbranch_execz Lrl01_%=\nDT 218,6,17408\nMvaa 104,16,136\nDT 220,2,18432\nMvaa 104,20,144\n"
"DT 222,6,18432\nMvaa 104,24,152\nDT 224,2,19456\nMvaa 104,28,160\nDT 226,6,19456\nCV 24,100,101\n"
"Mvaa 104,32,168\nDT 228,2,20480\nCV 25,102,103\nMvaa 104,36,176\nDT 230,6,20480\nMvaa 104,40,184\n"
"DT 232,2,21504\nMvaa 104,44,0\nDT 234,6,21504\nMvaa 104,48,8\nDT 192,3,13312\nMvaa 108,16,140\nDT 194,7,13312\n"
"Mvaa 108,20,148\nDT 196,3,14336\nMvaa 108,24,156\nDT 198,7,14336\nMvaa 108,28,164\nDT 200,3,15360\n"
"CV 22,104,105\nMvaa 108,32,172\nDT 202,7,15360\nCV 23,106,107\nMvaa 108,36,180\n"
"buffer_store_dwordx4 v[20:23], v13, s[60:63], s87 offen sc1\nMvaa 108,40,188\nWL 5\nDT 204,3,16384\n"
"DT 206,7,16384\nMvaa 108,44,4\ns_mov_b32 s87, 1146880\nDT 208,3,17408\nMvaa 108,48,12\nDT 210,7,17408\n"
"Mvaa 112,52,136\nDT 16,3,18432\nMvaa 112,56,144\nDT 18,7,18432\nMvaa 112,60,152\nDT 20,3,19456\n"
"Mvaa 112,212,160\nDT 22,7,19456\nCV 26,108,109\nMvaa 112,216,168\nDT 24,3,20480\nCV 27,110,111\n"
"Mvaa 112,220,176\nbuffer_store_dwordx4 v[24:27], v13, s[60:63], s88 offen sc1\nMvaa 112,224,184\nDT 26,7,20480\n"
"DT 28,3,21504\nMvaa 112,228,0\ns_mov_b32 s88, 1114112\nDT 30,7,21504\nMvaa 112,232,8\nMvaa 116,52,140\n"
"Mvaa 116,56,148\nMvaa 116,60,156\nMvaa 116,212,164\nMvaa 116,216,172\nMvaa 116,220,180\nMvaa 116,224,188\nWL 0\n"
"CV 28,112,113\nCV 29,114,115\nMvaa 116,228,4\nMvaa 116,232,12\ns_bfm_b64 exec, 1, 63\n"
"global_atomic_add v16, v13, v8, s[98:99] sc0\ns_mov_b64 exec, -1\nMvaa 120,192,136\nMvaa 120,196,144\n"
"Mvaa 120,200,152\nMvaa 120,204,160\ns_mov_b32 exec_lo, 0x11110000\ns_mov_b32 exec_hi, 0x11111\n"
"global_load_dword v14, v13, s[98:99] sc1\ns_mov_b64 exec, -1\nMvaa 120,208,168\nMvaa 120,16,176\nCV 20,116,117\n"
"CV 21,118,119\nMvaa 120,20,184\nMvaa 120,24,0\nMvaa 120,28,8\nMvaa 124,192,140\nMvaa 124,196,148\n"
"Mvaa 124,200,156\nMvaa 124,204,164\nMvaa 124,208,172\nMvaa 124,16,180\nMvaa 124,20,188\nMvaa 124,24,4\n"
"CV 30,120,121\nCV 31,122,123\nMvaa 124,28,12\nbuffer_store_dwordx4 v[28:31], v13, s[60:63], s87 offen sc1\n"
"s_nop 6\nCV 22,124,125\nCV 23,126,127\nbuffer_store_dwordx4 v[20:23], v13, s[60:63], s88 offen sc1\n"
"s_waitcnt vmcnt(2)\ns_mov_b32 s84, s42\ns_cmp_eq_u32 s34, s34\ns_cselect_b32 s85, -1, 0\n"
"s_and_b32 s3, s84, s85\n"
"Lredx1_%=:\nWL 0\nv_sub_u32 v16, v16, v10\nv_sub_u32 v14, v14, v10\ns_mov_b32 s42, 1179648\n"
"v_cmp_eq_u32 s[6:7], 16, v14\ns_cmp_lg_u32 s3, 0\ns_cbranch_scc0 Lrslowx1_%=\n"
"Lrhx1_%=:\nv_readlane_b32 s91, v16, 63\ns_lshr_b32 s92, s81, 2\ns_add_u32 s92, s92, 32\n"
"s_mul_i32 s91, s91, s35\ns_cmp_lt_u32 s91, 16\ns_cselect_b32 s74, 1, 0\ns_min_u32 s91, s91, 15\n"
"s_add_u32 s91, s91, 16\ns_cmp_eq_u32 s35, 0\ns_cselect_b32 s91, s92, s91\ns_lshl_b32 s82, s91, 15\n"
"s_and_b32 s2, s91, 1\ns_mul_i32 s2, s2, 18400\ns_lshl_b32 s92, s91, 5\ns_add_u32 s2, s2, s92\n"
"s_and_b32 s91, s91, 60\ns_add_u32 s91, s91, 16\ns_cmp_lg_u32 s3, 0\ns_cbranch_scc0 Lrslow2x1_%=\n"
"Lrmx1_%=:\nMvavz 104,240,164\nv_lshlrev_b32 v120, 16, v224\nv_and_b32 v121, 0xffff0000, v224\n"
"Mvavz 108,244,164\nv_lshlrev_b32 v122, 16, v225\nv_and_b32 v123, 0xffff0000, v225\nMvav 104,240,172\n"
"v_lshlrev_b32 v124, 16, v226\nv_and_b32 v125, 0xffff0000, v226\nMvav 108,244,172\nv_lshlrev_b32 v126, 16, v227\n"
"v_and_b32 v127, 0xffff0000, v227\nMvav 104,240,180\nMvav 108,244,180\nMvav 104,240,188\nMvav 108,244,188\n"
"Mvav 104,240,196\nMvav 108,244,196\nMvav 104,240,204\nMvav 108,244,204\nMvav 104,240,212\nMvav 108,244,212\n"
"s_cmp_lg_u32 s3, 0\ns_cbranch_scc0 Lrnx1_%=\ns_waitcnt vmcnt(0)\ns_bfm_b64 exec, 1, 48\n"
"global_atomic_add v13, v9, s[98:99]\ns_mov_b64 exec, -1\ns_cmp_eq_u32 s74, 0\ns_cbranch_scc1 Lre0x1_%=\n"
"buffer_load_dwordx4 v[232:235], v11, s[36:39], s2 offen\ns_bitcmp1_b64 s[6:7], s91\ns_cbranch_scc0 Lre0x1_%=\n"
"s_add_u32 s92, s82, 0\nbuffer_load_dwordx4 a[68:71], v13, s[40:43], s92 offen offset:1024 sc1\n"
"buffer_load_dwordx4 a[76:79], v13, s[40:43], s92 offen offset:3072 sc1\ns_add_u32 s92, s82, 4096\n"
"buffer_load_dwordx4 a[84:87], v13, s[40:43], s92 offen offset:1024 sc1\n"
"buffer_load_dwordx4 a[92:95], v13, s[40:43], s92 offen offset:3072 sc1\ns_add_u32 s92, s82, 16384\n"
"buffer_load_dwordx4 a[100:103], v13, s[40:43], s92 offen offset:1024 sc1\n"
"buffer_load_dwordx4 a[108:111], v13, s[40:43], s92 offen offset:3072 sc1\ns_add_u32 s92, s82, 20480\n"
"buffer_load_dwordx4 a[116:119], v13, s[40:43], s92 offen offset:1024 sc1\n"
"buffer_load_dwordx4 a[124:127], v13, s[40:43], s92 offen offset:3072 sc1\ns_add_u32 s92, s82, 0\n"
"buffer_load_dwordx4 a[64:67], v13, s[40:43], s92 offen offset:0 sc1\n"
"buffer_load_dwordx4 a[72:75], v13, s[40:43], s92 offen offset:2048 sc1\ns_add_u32 s92, s82, 4096\n"
"buffer_load_dwordx4 a[80:83], v13, s[40:43], s92 offen offset:0 sc1\n"
"buffer_load_dwordx4 a[88:91], v13, s[40:43], s92 offen offset:2048 sc1\ns_add_u32 s92, s82, 16384\n"
"buffer_load_dwordx4 a[96:99], v13, s[40:43], s92 offen offset:0 sc1\n"
"buffer_load_dwordx4 a[104:107], v13, s[40:43], s92 offen offset:2048 sc1\ns_add_u32 s92, s82, 20480\n"
"buffer_load_dwordx4 a[112:115], v13, s[40:43], s92 offen offset:0 sc1\n"
"buffer_load_dwordx4 a[120:123], v13, s[40:43], s92 offen offset:2048 sc1\n"
"Lre0x1_%=:\ns_nop 0\n"
"Lrnx1_%=:\nMvav 104,240,220\nMvav 108,244,220\nMvavz 96,240,160\ns_nop 1\nMvavz 100,244,160\ns_nop 1\n"
"Mvav 96,240,168\nMvav 100,244,168\nMvav 96,240,176\nEX 112,104\nEX 113,105\nEX 114,106\nEX 115,107\n"
"Mvav 100,244,176\nEX 116,108\nEX 117,109\nEX 118,110\nEX 119,111\nMvav 96,240,184\nv_mul_f32 v104, s89, v104\n"
"v_mul_f32 v105, s89, v105\nv_mul_f32 v106, s89, v106\nv_mul_f32 v107, s89, v107\nMvav 100,244,184\n"
"v_mul_f32 v108, s89, v108\nv_mul_f32 v109, s89, v109\nv_mul_f32 v110, s89, v110\nv_mul_f32 v111, s89, v111\n"
"Mvav 96,240,192\nv_add_f32 v112, 1.0, v112\nv_add_f32 v113, 1.0, v113\nv_add_f32 v114, 1.0, v114\n"
"v_add_f32 v115, 1.0, v115\nMvav 100,244,192\nv_add_f32 v116, 1.0, v116\nv_add_f32 v117, 1.0, v117\n"
"v_add_f32 v118, 1.0, v118\nv_add_f32 v119, 1.0, v119\nMvav 96,240,200\nRC 112,112\nRC 113,113\nRC 114,114\n"
"RC 115,115\nMvav 100,244,200\nRC 116,116\nRC 117,117\nRC 118,118\nRC 119,119\nMvav 96,240,208\nML 104,104,112\n"
"ML 105,105,113\nML 106,106,114\nML 107,107,115\nMvav 100,244,208\nML 108,108,116\nML 109,109,117\n"
"ML 110,110,118\nML 111,111,119\nMvav 96,240,216\nMvav 100,244,216\ns_nop 7\n"
"v_pk_fma_f32 v[96:97], v[96:97], v[104:105], v[120:121]\n"
"v_pk_fma_f32 v[98:99], v[98:99], v[106:107], v[122:123]\n"
"v_pk_fma_f32 v[100:101], v[100:101], v[108:109], v[124:125]\n"
"v_pk_fma_f32 v[102:103], v[102:103], v[110:111], v[126:127]\nCV 128,96,97\nCV 129,98,99\nCV 130,100,101\n"
"CV 131,102,103\ns_cmp_eq_u32 s74, 0\ns_cbranch_scc1 Lrsx1_%=\ns_bitcmp1_b64 s[6:7], s91\n"
"s_cbranch_scc1 Lrsx1_%=\ns_mov_b32 s90, 262144\ns_bfm_b64 exec, 1, s91\n"
"Lrwbx1_%=:\nglobal_load_dword v14, v13, s[98:99] sc1\ns_waitcnt vmcnt(0)\nv_sub_u32 v14, v14, v10\n"
"v_cmp_eq_u32 vcc, 16, v14\ns_sub_u32 s90, s90, 1\ns_cbranch_scc1 Lrgbx1_%=\ns_sleep 1\n"
"s_cbranch_vccz Lrwbx1_%=\n"
"Lrgbx1_%=:\ns_mov_b64 exec, -1\ns_add_u32 s92, s82, 0\n"
"buffer_load_dwordx4 a[68:71], v13, s[40:43], s92 offen offset:1024 sc1\n"
"buffer_load_dwordx4 a[76:79], v13, s[40:43], s92 offen offset:3072 sc1\ns_add_u32 s92, s82, 4096\n"
"buffer_load_dwordx4 a[84:87], v13, s[40:43], s92 offen offset:1024 sc1\n"
"buffer_load_dwordx4 a[92:95], v13, s[40:43], s92 offen offset:3072 sc1\ns_add_u32 s92, s82, 16384\n"
"buffer_load_dwordx4 a[100:103], v13, s[40:43], s92 offen offset:1024 sc1\n"
"buffer_load_dwordx4 a[108:111], v13, s[40:43], s92 offen offset:3072 sc1\ns_add_u32 s92, s82, 20480\n"
"buffer_load_dwordx4 a[116:119], v13, s[40:43], s92 offen offset:1024 sc1\n"
"buffer_load_dwordx4 a[124:127], v13, s[40:43], s92 offen offset:3072 sc1\ns_add_u32 s92, s82, 0\n"
"buffer_load_dwordx4 a[64:67], v13, s[40:43], s92 offen offset:0 sc1\n"
"buffer_load_dwordx4 a[72:75], v13, s[40:43], s92 offen offset:2048 sc1\ns_add_u32 s92, s82, 4096\n"
"buffer_load_dwordx4 a[80:83], v13, s[40:43], s92 offen offset:0 sc1\n"
"buffer_load_dwordx4 a[88:91], v13, s[40:43], s92 offen offset:2048 sc1\ns_add_u32 s92, s82, 16384\n"
"buffer_load_dwordx4 a[96:99], v13, s[40:43], s92 offen offset:0 sc1\n"
"buffer_load_dwordx4 a[104:107], v13, s[40:43], s92 offen offset:2048 sc1\ns_add_u32 s92, s82, 20480\n"
"buffer_load_dwordx4 a[112:115], v13, s[40:43], s92 offen offset:0 sc1\n"
"buffer_load_dwordx4 a[120:123], v13, s[40:43], s92 offen offset:2048 sc1\n"
"Lrsx1_%=:\nbuffer_store_dwordx4 v[128:131], v11, s[44:47], s4 offen sc1\ns_cmp_eq_u32 s74, 0\n"
"s_cbranch_scc1 Lfinx1_%=\ns_cmp_eq_u32 s35, 0\ns_cbranch_scc1 Lrqx1_%=\n"
"Lrpx1_%=:\ns_bfm_b64 exec, 1, 63\nglobal_atomic_add v16, v13, v8, s[98:99] sc0\ns_mov_b32 exec_lo, 0x11110000\n"
"s_mov_b32 exec_hi, 0x11111\nglobal_load_dword v14, v13, s[98:99] sc1\ns_mov_b64 exec, -1\n"
"Lrqx1_%=:\ns_waitcnt vmcnt(16)\nMvaaz 104,240,68\nv_lshlrev_b32 v120, 16, v232\n"
"v_and_b32 v121, 0xffff0000, v232\nMvaaz 108,244,68\nv_lshlrev_b32 v122, 16, v233\n"
"v_and_b32 v123, 0xffff0000, v233\ns_waitcnt vmcnt(15)\nMvaa 104,240,76\nv_lshlrev_b32 v124, 16, v234\n"
"v_and_b32 v125, 0xffff0000, v234\nMvaa 108,244,76\nv_lshlrev_b32 v126, 16, v235\n"
"v_and_b32 v127, 0xffff0000, v235\ns_waitcnt vmcnt(14)\nMvaa 104,240,84\nMvaa 108,244,84\ns_waitcnt vmcnt(13)\n"
"Mvaa 104,240,92\nMvaa 108,244,92\ns_waitcnt vmcnt(12)\nMvaa 104,240,100\nMvaa 108,244,100\ns_waitcnt vmcnt(11)\n"
"Mvaa 104,240,108\nMvaa 108,244,108\ns_waitcnt vmcnt(10)\nMvaa 104,240,116\nMvaa 108,244,116\n"
"s_waitcnt vmcnt(9)\nMvaa 104,240,124\nMvaa 108,244,124\ns_waitcnt vmcnt(8)\nMvaaz 96,240,64\ns_nop 1\n"
"Mvaaz 100,244,64\ns_nop 1\ns_waitcnt vmcnt(7)\nMvaa 96,240,72\nMvaa 100,244,72\ns_waitcnt vmcnt(6)\n"
"Mvaa 96,240,80\nEX 112,104\nEX 113,105\nEX 114,106\nEX 115,107\nMvaa 100,244,80\nEX 116,108\nEX 117,109\n"
"EX 118,110\nEX 119,111\ns_waitcnt vmcnt(5)\nMvaa 96,240,88\nv_mul_f32 v104, s89, v104\n"
"v_mul_f32 v105, s89, v105\nv_mul_f32 v106, s89, v106\nv_mul_f32 v107, s89, v107\nMvaa 100,244,88\n"
"v_mul_f32 v108, s89, v108\nv_mul_f32 v109, s89, v109\nv_mul_f32 v110, s89, v110\nv_mul_f32 v111, s89, v111\n"
"s_waitcnt vmcnt(4)\nMvaa 96,240,96\nv_add_f32 v112, 1.0, v112\nv_add_f32 v113, 1.0, v113\n"
"v_add_f32 v114, 1.0, v114\nv_add_f32 v115, 1.0, v115\nMvaa 100,244,96\nv_add_f32 v116, 1.0, v116\n"
"v_add_f32 v117, 1.0, v117\nv_add_f32 v118, 1.0, v118\nv_add_f32 v119, 1.0, v119\ns_waitcnt vmcnt(3)\n"
"Mvaa 96,240,104\nRC 112,112\nRC 113,113\nRC 114,114\nRC 115,115\nMvaa 100,244,104\nRC 116,116\nRC 117,117\n"
"RC 118,118\nRC 119,119\ns_waitcnt vmcnt(2)\nMvaa 96,240,112\nML 104,104,112\nML 105,105,113\nML 106,106,114\n"
"ML 107,107,115\nMvaa 100,244,112\nML 108,108,116\nML 109,109,117\nML 110,110,118\nML 111,111,119\n"
"s_waitcnt vmcnt(1)\nMvaa 96,240,120\nMvaa 100,244,120\ns_nop 7\n"
"v_pk_fma_f32 v[96:97], v[96:97], v[104:105], v[120:121]\n"
"v_pk_fma_f32 v[98:99], v[98:99], v[106:107], v[122:123]\n"
"v_pk_fma_f32 v[100:101], v[100:101], v[108:109], v[124:125]\n"
"v_pk_fma_f32 v[102:103], v[102:103], v[110:111], v[126:127]\nCV 136,96,97\nCV 137,98,99\nCV 138,100,101\n"
"CV 139,102,103\nbuffer_store_dwordx4 v[136:139], v11, s[44:47], s2 offen sc1\ns_cmp_eq_u32 s35, 0\n"
"s_cbranch_scc1 Lfinx1_%=\ns_waitcnt vmcnt(1)\nv_sub_u32 v16, v16, v10\nv_sub_u32 v14, v14, v10\n"
"v_readlane_b32 s91, v16, 63\nv_cmp_eq_u32 s[6:7], 16, v14\ns_cmp_ge_u32 s91, 16\ns_cbranch_scc1 Lfinx1_%=\n"
"s_add_u32 s91, s91, 16\n"
"Lryx1_%=:\ns_lshl_b32 s82, s91, 15\ns_and_b32 s2, s91, 1\ns_mul_i32 s2, s2, 18400\ns_lshl_b32 s92, s91, 5\n"
"s_add_u32 s2, s2, s92\ns_and_b32 s91, s91, 60\ns_add_u32 s91, s91, 16\n"
"buffer_load_dwordx4 v[232:235], v11, s[36:39], s2 offen\ns_bitcmp1_b64 s[6:7], s91\ns_cbranch_scc1 Lrgcx1_%=\n"
"s_mov_b32 s90, 262144\ns_bfm_b64 exec, 1, s91\n"
"Lrwcx1_%=:\nglobal_load_dword v14, v13, s[98:99] sc1\ns_waitcnt vmcnt(0)\nv_sub_u32 v14, v14, v10\n"
"v_cmp_eq_u32 vcc, 16, v14\ns_sub_u32 s90, s90, 1\ns_cbranch_scc1 Lrgcx1_%=\ns_sleep 1\n"
"s_cbranch_vccz Lrwcx1_%=\n"
"Lrgcx1_%=:\ns_mov_b64 exec, -1\ns_add_u32 s92, s82, 0\n"
"buffer_load_dwordx4 a[68:71], v13, s[40:43], s92 offen offset:1024 sc1\n"
"buffer_load_dwordx4 a[76:79], v13, s[40:43], s92 offen offset:3072 sc1\ns_add_u32 s92, s82, 4096\n"
"buffer_load_dwordx4 a[84:87], v13, s[40:43], s92 offen offset:1024 sc1\n"
"buffer_load_dwordx4 a[92:95], v13, s[40:43], s92 offen offset:3072 sc1\ns_add_u32 s92, s82, 16384\n"
"buffer_load_dwordx4 a[100:103], v13, s[40:43], s92 offen offset:1024 sc1\n"
"buffer_load_dwordx4 a[108:111], v13, s[40:43], s92 offen offset:3072 sc1\ns_add_u32 s92, s82, 20480\n"
"buffer_load_dwordx4 a[116:119], v13, s[40:43], s92 offen offset:1024 sc1\n"
"buffer_load_dwordx4 a[124:127], v13, s[40:43], s92 offen offset:3072 sc1\ns_add_u32 s92, s82, 0\n"
"buffer_load_dwordx4 a[64:67], v13, s[40:43], s92 offen offset:0 sc1\n"
"buffer_load_dwordx4 a[72:75], v13, s[40:43], s92 offen offset:2048 sc1\ns_add_u32 s92, s82, 4096\n"
"buffer_load_dwordx4 a[80:83], v13, s[40:43], s92 offen offset:0 sc1\n"
"buffer_load_dwordx4 a[88:91], v13, s[40:43], s92 offen offset:2048 sc1\ns_add_u32 s92, s82, 16384\n"
"buffer_load_dwordx4 a[96:99], v13, s[40:43], s92 offen offset:0 sc1\n"
"buffer_load_dwordx4 a[104:107], v13, s[40:43], s92 offen offset:2048 sc1\ns_add_u32 s92, s82, 20480\n"
"buffer_load_dwordx4 a[112:115], v13, s[40:43], s92 offen offset:0 sc1\n"
"buffer_load_dwordx4 a[120:123], v13, s[40:43], s92 offen offset:2048 sc1\ns_branch Lrpx1_%=\n"
"Lfinx1_%=:\ns_endpgm\n"
"Lrslowx1_%=:\ns_waitcnt vmcnt(0)\ns_bfm_b64 exec, 1, 48\nglobal_atomic_add v13, v9, s[98:99]\n"
"s_mov_b64 exec, -1\ns_cmp_lg_u32 s84, 0\ns_cbranch_scc1 Lrf0x1_%=\ns_lshl_b32 s82, s91, 15\ns_mov_b32 s91, s78\n"
"s_bitcmp1_b64 s[6:7], s91\ns_cbranch_scc1 Lrg0x1_%=\ns_mov_b32 s90, 262144\ns_bfm_b64 exec, 1, s91\n"
"Lrw0x1_%=:\nglobal_load_dword v14, v13, s[98:99] sc1\ns_waitcnt vmcnt(0)\nv_sub_u32 v14, v14, v10\n"
"v_cmp_eq_u32 vcc, 16, v14\ns_sub_u32 s90, s90, 1\ns_cbranch_scc1 Lrg0x1_%=\ns_sleep 1\n"
"s_cbranch_vccz Lrw0x1_%=\n"
"Lrg0x1_%=:\ns_mov_b64 exec, -1\nbuffer_load_dwordx4 v[224:227], v11, s[36:39], s4 offen\ns_add_u32 s92, s82, 0\n"
"buffer_load_dwordx4 v[164:167], v13, s[40:43], s92 offen offset:1024 sc1\n"
"buffer_load_dwordx4 v[172:175], v13, s[40:43], s92 offen offset:3072 sc1\ns_add_u32 s92, s82, 4096\n"
"buffer_load_dwordx4 v[180:183], v13, s[40:43], s92 offen offset:1024 sc1\n"
"buffer_load_dwordx4 v[188:191], v13, s[40:43], s92 offen offset:3072 sc1\ns_add_u32 s92, s82, 16384\n"
"buffer_load_dwordx4 v[196:199], v13, s[40:43], s92 offen offset:1024 sc1\n"
"buffer_load_dwordx4 v[204:207], v13, s[40:43], s92 offen offset:3072 sc1\ns_add_u32 s92, s82, 20480\n"
"buffer_load_dwordx4 v[212:215], v13, s[40:43], s92 offen offset:1024 sc1\n"
"buffer_load_dwordx4 v[220:223], v13, s[40:43], s92 offen offset:3072 sc1\ns_add_u32 s92, s82, 0\n"
"buffer_load_dwordx4 v[160:163], v13, s[40:43], s92 offen offset:0 sc1\n"
"buffer_load_dwordx4 v[168:171], v13, s[40:43], s92 offen offset:2048 sc1\ns_add_u32 s92, s82, 4096\n"
"buffer_load_dwordx4 v[176:179], v13, s[40:43], s92 offen offset:0 sc1\n"
"buffer_load_dwordx4 v[184:187], v13, s[40:43], s92 offen offset:2048 sc1\ns_add_u32 s92, s82, 16384\n"
"buffer_load_dwordx4 v[192:195], v13, s[40:43], s92 offen offset:0 sc1\n"
"buffer_load_dwordx4 v[200:203], v13, s[40:43], s92 offen offset:2048 sc1\ns_add_u32 s92, s82, 20480\n"
"buffer_load_dwordx4 v[208:211], v13, s[40:43], s92 offen offset:0 sc1\n"
"buffer_load_dwordx4 v[216:219], v13, s[40:43], s92 offen offset:2048 sc1\n"
"Lrf0x1_%=:\ns_branch Lrhx1_%=\n"
"Lrslow2x1_%=:\ns_cmp_eq_u32 s74, 0\ns_cbranch_scc1 Lre1x1_%=\n"
"buffer_load_dwordx4 v[232:235], v11, s[36:39], s2 offen\ns_bitcmp1_b64 s[6:7], s91\ns_cbranch_scc0 Lre1x1_%=\n"
"s_add_u32 s92, s82, 0\nbuffer_load_dwordx4 a[68:71], v13, s[40:43], s92 offen offset:1024 sc1\n"
"buffer_load_dwordx4 a[76:79], v13, s[40:43], s92 offen offset:3072 sc1\ns_add_u32 s92, s82, 4096\n"
"buffer_load_dwordx4 a[84:87], v13, s[40:43], s92 offen offset:1024 sc1\n"
"buffer_load_dwordx4 a[92:95], v13, s[40:43], s92 offen offset:3072 sc1\ns_add_u32 s92, s82, 16384\n"
"buffer_load_dwordx4 a[100:103], v13, s[40:43], s92 offen offset:1024 sc1\n"
"buffer_load_dwordx4 a[108:111], v13, s[40:43], s92 offen offset:3072 sc1\ns_add_u32 s92, s82, 20480\n"
"buffer_load_dwordx4 a[116:119], v13, s[40:43], s92 offen offset:1024 sc1\n"
"buffer_load_dwordx4 a[124:127], v13, s[40:43], s92 offen offset:3072 sc1\ns_add_u32 s92, s82, 0\n"
"buffer_load_dwordx4 a[64:67], v13, s[40:43], s92 offen offset:0 sc1\n"
"buffer_load_dwordx4 a[72:75], v13, s[40:43], s92 offen offset:2048 sc1\ns_add_u32 s92, s82, 4096\n"
"buffer_load_dwordx4 a[80:83], v13, s[40:43], s92 offen offset:0 sc1\n"
"buffer_load_dwordx4 a[88:91], v13, s[40:43], s92 offen offset:2048 sc1\ns_add_u32 s92, s82, 16384\n"
"buffer_load_dwordx4 a[96:99], v13, s[40:43], s92 offen offset:0 sc1\n"
"buffer_load_dwordx4 a[104:107], v13, s[40:43], s92 offen offset:2048 sc1\ns_add_u32 s92, s82, 20480\n"
"buffer_load_dwordx4 a[112:115], v13, s[40:43], s92 offen offset:0 sc1\n"
"buffer_load_dwordx4 a[120:123], v13, s[40:43], s92 offen offset:2048 sc1\ns_waitcnt vmcnt(17)\n"
"s_branch Lrmx1_%=\n"
"Lre1x1_%=:\ns_waitcnt vmcnt(0)\ns_branch Lrmx1_%=\n"
"Lpr1s_%=:\ns_mov_b32 s90, 262144\n"
"Lpr1l_%=:\ns_mov_b64 exec, s[0:1]\nglobal_atomic_add v13, v174, s[98:99]\ns_mov_b64 exec, -1\ns_sleep 16\n"
"s_bfm_b64 exec, 16, 0\nglobal_load_dword v175, v13, s[98:99] sc1\ns_mov_b64 exec, -1\ns_waitcnt vmcnt(0)\n"
"v_cmp_eq_u32 vcc, v172, v175\ns_sub_u32 s90, s90, 1\ns_cbranch_scc1 Lpr1r_%=\ns_and_b32 s92, vcc_lo, 0xffff\n"
"s_cbranch_scc1 Lpr1l_%=\ns_branch Lpr1r_%=\n"
"Lpas_%=:\ns_mov_b32 s90, 262144\ns_mov_b64 exec, 1\n"
"Lpal_%=:\nglobal_load_dword v10, v13, s[96:97] sc1\ns_waitcnt vmcnt(0)\nv_cmp_eq_u32 vcc, s80, v10\n"
"s_sub_u32 s90, s90, 1\ns_cbranch_scc1 Lpax_%=\ns_cbranch_vccz Lpax_%=\ns_sleep 2\ns_branch Lpal_%=\n"
"Lpax_%=:\ns_mov_b64 exec, -1\ns_branch Lpar_%=\n"
"Lb01s_%=:\ns_mov_b32 s90, 6\ns_bfm_b64 exec, 1, s78\n"
"Lb01l_%=:\nglobal_load_dword v14, v13, s[98:99] sc1\ns_waitcnt vmcnt(0)\nv_sub_u32 v14, v14, v10\n"
"v_cmp_eq_u32 vcc, 16, v14\ns_sub_u32 s90, s90, 1\ns_cbranch_scc1 Lb01x_%=\ns_cbranch_vccz Lb01l_%=\n"
"Lb01x_%=:\ns_mov_b64 exec, -1\ns_branch Lb01r_%=\n"
"Lend_%=:\ns_nop 0\n"
"Lfb_%=:\ns_waitcnt vmcnt(0) lgkmcnt(0)\ns_nop 15\nv_bfe_u32 v20, v13, 4, 4\nv_lshrrev_b32 v21, 8, v13\n"
"v_lshlrev_b32 v22, 1, v20\nv_lshl_add_u32 v22, v21, 10, v22\nv_add_u32 v23, 0x1000, v22\n"
"v_lshlrev_b32 v20, 8, v20\nv_lshl_add_u32 v20, v21, 3, v20\nv_mov_b32 v24, 0xff800000\n"
"v_mov_b32 v25, 0xff800000\nv_mov_b32 v26, 0xff800000\nv_mov_b32 v27, 0xff800000\ns_mov_b32 s90, 0\n"
"Lfb1_%=:\nbuffer_load_dwordx2 v[32:33], v20, s[52:55], s90 offen offset:0\n"
"buffer_load_dwordx2 v[34:35], v20, s[52:55], s90 offen offset:32\n"
"buffer_load_dwordx2 v[36:37], v20, s[52:55], s90 offen offset:64\n"
"buffer_load_dwordx2 v[38:39], v20, s[52:55], s90 offen offset:96\n"
"buffer_load_dwordx2 v[40:41], v20, s[52:55], s90 offen offset:128\n"
"buffer_load_dwordx2 v[42:43], v20, s[52:55], s90 offen offset:160\n"
"buffer_load_dwordx2 v[44:45], v20, s[52:55], s90 offen offset:192\n"
"buffer_load_dwordx2 v[46:47], v20, s[52:55], s90 offen offset:224\ns_waitcnt vmcnt(0)\nMvvaz 48,32,0\n"
"Mvva 48,36,4\nMvva 48,40,8\nMvva 48,44,12\nMvvaz 52,32,16\nMvva 52,36,20\nMvva 52,40,24\nMvva 52,44,28\n"
"Mvvaz 56,32,32\nMvva 56,36,36\nMvva 56,40,40\nMvva 56,44,44\nMvvaz 60,32,48\nMvva 60,36,52\nMvva 60,40,56\n"
"Mvva 60,44,60\ns_nop 7\ns_add_u32 s90, s90, 4096\nv_max3_f32 v24, v24, v48, v49\nv_max3_f32 v24, v24, v50, v51\n"
"v_max3_f32 v25, v25, v52, v53\nv_max3_f32 v25, v25, v54, v55\nv_max3_f32 v26, v26, v56, v57\n"
"v_max3_f32 v26, v26, v58, v59\nv_max3_f32 v27, v27, v60, v61\nv_max3_f32 v27, v27, v62, v63\n"
"s_cmp_lt_u32 s90, 131072\ns_cbranch_scc1 Lfb1_%=\nv_mov_b32 v28, v24\ns_nop 1\nv_permlane32_swap_b32 v24, v28\n"
"v_max_f32 v24, v24, v28\nv_mov_b32 v28, v24\ns_nop 1\nv_permlane16_swap_b32 v24, v28\nv_max_f32 v24, v24, v28\n"
"v_sub_f32 v80, 0, v24\nv_sub_f32 v81, 0, v24\nv_sub_f32 v82, 0, v24\nv_sub_f32 v83, 0, v24\nv_mov_b32 v28, v25\n"
"s_nop 1\nv_permlane32_swap_b32 v25, v28\nv_max_f32 v25, v25, v28\nv_mov_b32 v28, v25\ns_nop 1\n"
"v_permlane16_swap_b32 v25, v28\nv_max_f32 v25, v25, v28\nv_sub_f32 v84, 0, v25\nv_sub_f32 v85, 0, v25\n"
"v_sub_f32 v86, 0, v25\nv_sub_f32 v87, 0, v25\nv_mov_b32 v28, v26\ns_nop 1\nv_permlane32_swap_b32 v26, v28\n"
"v_max_f32 v26, v26, v28\nv_mov_b32 v28, v26\ns_nop 1\nv_permlane16_swap_b32 v26, v28\nv_max_f32 v26, v26, v28\n"
"v_sub_f32 v88, 0, v26\nv_sub_f32 v89, 0, v26\nv_sub_f32 v90, 0, v26\nv_sub_f32 v91, 0, v26\nv_mov_b32 v28, v27\n"
"s_nop 1\nv_permlane32_swap_b32 v27, v28\nv_max_f32 v27, v27, v28\nv_mov_b32 v28, v27\ns_nop 1\n"
"v_permlane16_swap_b32 v27, v28\nv_max_f32 v27, v27, v28\nv_sub_f32 v92, 0, v27\nv_sub_f32 v93, 0, v27\n"
"v_sub_f32 v94, 0, v27\nv_sub_f32 v95, 0, v27\nv_mov_b32 v128, 0\nv_mov_b32 v129, 0\nv_mov_b32 v130, 0\n"
"v_mov_b32 v131, 0\nv_mov_b32 v132, 0\nv_mov_b32 v133, 0\nv_mov_b32 v134, 0\nv_mov_b32 v135, 0\n"
"v_mov_b32 v136, 0\nv_mov_b32 v137, 0\nv_mov_b32 v138, 0\nv_mov_b32 v139, 0\nv_mov_b32 v140, 0\n"
"v_mov_b32 v141, 0\nv_mov_b32 v142, 0\nv_mov_b32 v143, 0\nv_mov_b32 v144, 0\nv_mov_b32 v145, 0\n"
"v_mov_b32 v146, 0\nv_mov_b32 v147, 0\nv_mov_b32 v148, 0\nv_mov_b32 v149, 0\nv_mov_b32 v150, 0\n"
"v_mov_b32 v151, 0\nv_mov_b32 v152, 0\nv_mov_b32 v153, 0\nv_mov_b32 v154, 0\nv_mov_b32 v155, 0\n"
"v_mov_b32 v156, 0\nv_mov_b32 v157, 0\nv_mov_b32 v158, 0\nv_mov_b32 v159, 0\nv_mov_b32 v160, 0\n"
"v_mov_b32 v161, 0\nv_mov_b32 v162, 0\nv_mov_b32 v163, 0\nv_mov_b32 v164, 0\nv_mov_b32 v165, 0\n"
"v_mov_b32 v166, 0\nv_mov_b32 v167, 0\nv_mov_b32 v168, 0\nv_mov_b32 v169, 0\nv_mov_b32 v170, 0\n"
"v_mov_b32 v171, 0\nv_mov_b32 v172, 0\nv_mov_b32 v173, 0\nv_mov_b32 v174, 0\nv_mov_b32 v175, 0\n"
"v_mov_b32 v176, 0\nv_mov_b32 v177, 0\nv_mov_b32 v178, 0\nv_mov_b32 v179, 0\nv_mov_b32 v180, 0\n"
"v_mov_b32 v181, 0\nv_mov_b32 v182, 0\nv_mov_b32 v183, 0\nv_mov_b32 v184, 0\nv_mov_b32 v185, 0\n"
"v_mov_b32 v186, 0\nv_mov_b32 v187, 0\nv_mov_b32 v188, 0\nv_mov_b32 v189, 0\nv_mov_b32 v190, 0\n"
"v_mov_b32 v191, 0\nv_mov_b32 v192, 0\nv_mov_b32 v193, 0\nv_mov_b32 v194, 0\nv_mov_b32 v195, 0\n"
"v_mov_b32 v196, 0\nv_mov_b32 v197, 0\nv_mov_b32 v198, 0\nv_mov_b32 v199, 0\nv_mov_b32 v200, 0\n"
"v_mov_b32 v201, 0\nv_mov_b32 v202, 0\nv_mov_b32 v203, 0\nv_mov_b32 v204, 0\nv_mov_b32 v205, 0\n"
"v_mov_b32 v206, 0\nv_mov_b32 v207, 0\nv_mov_b32 v208, 0\nv_mov_b32 v209, 0\nv_mov_b32 v210, 0\n"
"v_mov_b32 v211, 0\nv_mov_b32 v212, 0\nv_mov_b32 v213, 0\nv_mov_b32 v214, 0\nv_mov_b32 v215, 0\n"
"v_mov_b32 v216, 0\nv_mov_b32 v217, 0\nv_mov_b32 v218, 0\nv_mov_b32 v219, 0\nv_mov_b32 v220, 0\n"
"v_mov_b32 v221, 0\nv_mov_b32 v222, 0\nv_mov_b32 v223, 0\nv_mov_b32 v224, 0\nv_mov_b32 v225, 0\n"
"v_mov_b32 v226, 0\nv_mov_b32 v227, 0\nv_mov_b32 v228, 0\nv_mov_b32 v229, 0\nv_mov_b32 v230, 0\n"
"v_mov_b32 v231, 0\nv_mov_b32 v232, 0\nv_mov_b32 v233, 0\nv_mov_b32 v234, 0\nv_mov_b32 v235, 0\n"
"v_mov_b32 v236, 0\nv_mov_b32 v237, 0\nv_mov_b32 v238, 0\nv_mov_b32 v239, 0\nv_mov_b32 v240, 0\n"
"v_mov_b32 v241, 0\nv_mov_b32 v242, 0\nv_mov_b32 v243, 0\nv_mov_b32 v244, 0\nv_mov_b32 v245, 0\n"
"v_mov_b32 v246, 0\nv_mov_b32 v247, 0\nv_mov_b32 v248, 0\nv_mov_b32 v249, 0\nv_mov_b32 v250, 0\n"
"v_mov_b32 v251, 0\nv_mov_b32 v252, 0\nv_mov_b32 v253, 0\nv_mov_b32 v254, 0\nv_mov_b32 v255, 0\n"
"v_accvgpr_write_b32 a240, 0\nv_accvgpr_write_b32 a241, 0\nv_accvgpr_write_b32 a242, 0\n"
"v_accvgpr_write_b32 a243, 0\nv_accvgpr_write_b32 a244, 0\nv_accvgpr_write_b32 a245, 0\n"
"v_accvgpr_write_b32 a246, 0\nv_accvgpr_write_b32 a247, 0\nv_accvgpr_write_b32 a248, 0\n"
"v_accvgpr_write_b32 a249, 0\nv_accvgpr_write_b32 a250, 0\nv_accvgpr_write_b32 a251, 0\n"
"v_accvgpr_write_b32 a252, 0\nv_accvgpr_write_b32 a253, 0\nv_accvgpr_write_b32 a254, 0\n"
"v_accvgpr_write_b32 a255, 0\ns_mov_b32 s90, 0\n"
"Lfb2_%=:\ns_mov_b32 s92, s90\nbuffer_load_dwordx2 v[32:33], v20, s[52:55], s90 offen offset:0\n"
"buffer_load_dwordx2 v[34:35], v20, s[52:55], s90 offen offset:32\n"
"buffer_load_dwordx2 v[36:37], v20, s[52:55], s90 offen offset:64\n"
"buffer_load_dwordx2 v[38:39], v20, s[52:55], s90 offen offset:96\n"
"buffer_load_dwordx2 v[40:41], v20, s[52:55], s90 offen offset:128\n"
"buffer_load_dwordx2 v[42:43], v20, s[52:55], s90 offen offset:160\n"
"buffer_load_dwordx2 v[44:45], v20, s[52:55], s90 offen offset:192\n"
"buffer_load_dwordx2 v[46:47], v20, s[52:55], s90 offen offset:224\ns_waitcnt vmcnt(0)\nMvvac 48,32,0,80\n"
"Mvva 48,36,4\nMvva 48,40,8\nMvva 48,44,12\nMvvac 52,32,16,84\nMvva 52,36,20\nMvva 52,40,24\nMvva 52,44,28\n"
"Mvvac 56,32,32,88\nMvva 56,36,36\nMvva 56,40,40\nMvva 56,44,44\nMvvac 60,32,48,92\nMvva 60,36,52\n"
"Mvva 60,40,56\nMvva 60,44,60\ns_nop 7\ns_add_u32 s90, s90, 4096\nEX 48,48\nEX 49,49\nEX 50,50\nEX 51,51\n"
"EX 52,52\nEX 53,53\nEX 54,54\nEX 55,55\nEX 56,56\nEX 57,57\nEX 58,58\nEX 59,59\nEX 60,60\nEX 61,61\nEX 62,62\n"
"EX 63,63\ns_nop 0\nCV 96,48,49\nCV 97,50,51\nCV 100,52,53\nCV 101,54,55\nCV 104,56,57\nCV 105,58,59\n"
"CV 108,60,61\nCV 109,62,63\nbuffer_load_dwordx2 v[32:33], v20, s[52:55], s90 offen offset:0\n"
"buffer_load_dwordx2 v[34:35], v20, s[52:55], s90 offen offset:32\n"
"buffer_load_dwordx2 v[36:37], v20, s[52:55], s90 offen offset:64\n"
"buffer_load_dwordx2 v[38:39], v20, s[52:55], s90 offen offset:96\n"
"buffer_load_dwordx2 v[40:41], v20, s[52:55], s90 offen offset:128\n"
"buffer_load_dwordx2 v[42:43], v20, s[52:55], s90 offen offset:160\n"
"buffer_load_dwordx2 v[44:45], v20, s[52:55], s90 offen offset:192\n"
"buffer_load_dwordx2 v[46:47], v20, s[52:55], s90 offen offset:224\ns_waitcnt vmcnt(0)\nMvvac 48,32,0,80\n"
"Mvva 48,36,4\nMvva 48,40,8\nMvva 48,44,12\nMvvac 52,32,16,84\nMvva 52,36,20\nMvva 52,40,24\nMvva 52,44,28\n"
"Mvvac 56,32,32,88\nMvva 56,36,36\nMvva 56,40,40\nMvva 56,44,44\nMvvac 60,32,48,92\nMvva 60,36,52\n"
"Mvva 60,40,56\nMvva 60,44,60\ns_nop 7\ns_add_u32 s90, s90, 4096\nEX 48,48\nEX 49,49\nEX 50,50\nEX 51,51\n"
"EX 52,52\nEX 53,53\nEX 54,54\nEX 55,55\nEX 56,56\nEX 57,57\nEX 58,58\nEX 59,59\nEX 60,60\nEX 61,61\nEX 62,62\n"
"EX 63,63\ns_nop 0\nCV 98,48,49\nCV 99,50,51\nCV 102,52,53\nCV 103,54,55\nCV 106,56,57\nCV 107,58,59\n"
"CV 110,60,61\nCV 111,62,63\ns_nop 1\nMaav 240,236,96\nMaav 244,236,100\nMaav 248,236,104\nMaav 252,236,108\n"
"buffer_load_ushort v116, v22, s[56:59], s92 offen offset:0\n"
"buffer_load_ushort v117, v22, s[56:59], s92 offen offset:256\n"
"buffer_load_ushort v118, v22, s[56:59], s92 offen offset:512\n"
"buffer_load_ushort v119, v22, s[56:59], s92 offen offset:768\n"
"buffer_load_ushort v120, v23, s[56:59], s92 offen offset:0\n"
"buffer_load_ushort v121, v23, s[56:59], s92 offen offset:256\n"
"buffer_load_ushort v122, v23, s[56:59], s92 offen offset:512\n"
"buffer_load_ushort v123, v23, s[56:59], s92 offen offset:768\ns_waitcnt vmcnt(0)\n"
"v_lshl_or_b32 v112, v117, 16, v116\nv_lshl_or_b32 v113, v119, 16, v118\nv_lshl_or_b32 v114, v121, 16, v120\n"
"v_lshl_or_b32 v115, v123, 16, v122\ns_nop 1\nMvvv 128,112,96\nMvvv 132,112,100\nMvvv 136,112,104\n"
"Mvvv 140,112,108\nbuffer_load_ushort v116, v22, s[56:59], s92 offen offset:32\n"
"buffer_load_ushort v117, v22, s[56:59], s92 offen offset:288\n"
"buffer_load_ushort v118, v22, s[56:59], s92 offen offset:544\n"
"buffer_load_ushort v119, v22, s[56:59], s92 offen offset:800\n"
"buffer_load_ushort v120, v23, s[56:59], s92 offen offset:32\n"
"buffer_load_ushort v121, v23, s[56:59], s92 offen offset:288\n"
"buffer_load_ushort v122, v23, s[56:59], s92 offen offset:544\n"
"buffer_load_ushort v123, v23, s[56:59], s92 offen offset:800\ns_waitcnt vmcnt(0)\n"
"v_lshl_or_b32 v112, v117, 16, v116\nv_lshl_or_b32 v113, v119, 16, v118\nv_lshl_or_b32 v114, v121, 16, v120\n"
"v_lshl_or_b32 v115, v123, 16, v122\ns_nop 1\nMvvv 144,112,96\nMvvv 148,112,100\nMvvv 152,112,104\n"
"Mvvv 156,112,108\nbuffer_load_ushort v116, v22, s[56:59], s92 offen offset:64\n"
"buffer_load_ushort v117, v22, s[56:59], s92 offen offset:320\n"
"buffer_load_ushort v118, v22, s[56:59], s92 offen offset:576\n"
"buffer_load_ushort v119, v22, s[56:59], s92 offen offset:832\n"
"buffer_load_ushort v120, v23, s[56:59], s92 offen offset:64\n"
"buffer_load_ushort v121, v23, s[56:59], s92 offen offset:320\n"
"buffer_load_ushort v122, v23, s[56:59], s92 offen offset:576\n"
"buffer_load_ushort v123, v23, s[56:59], s92 offen offset:832\ns_waitcnt vmcnt(0)\n"
"v_lshl_or_b32 v112, v117, 16, v116\nv_lshl_or_b32 v113, v119, 16, v118\nv_lshl_or_b32 v114, v121, 16, v120\n"
"v_lshl_or_b32 v115, v123, 16, v122\ns_nop 1\nMvvv 160,112,96\nMvvv 164,112,100\nMvvv 168,112,104\n"
"Mvvv 172,112,108\nbuffer_load_ushort v116, v22, s[56:59], s92 offen offset:96\n"
"buffer_load_ushort v117, v22, s[56:59], s92 offen offset:352\n"
"buffer_load_ushort v118, v22, s[56:59], s92 offen offset:608\n"
"buffer_load_ushort v119, v22, s[56:59], s92 offen offset:864\n"
"buffer_load_ushort v120, v23, s[56:59], s92 offen offset:96\n"
"buffer_load_ushort v121, v23, s[56:59], s92 offen offset:352\n"
"buffer_load_ushort v122, v23, s[56:59], s92 offen offset:608\n"
"buffer_load_ushort v123, v23, s[56:59], s92 offen offset:864\ns_waitcnt vmcnt(0)\n"
"v_lshl_or_b32 v112, v117, 16, v116\nv_lshl_or_b32 v113, v119, 16, v118\nv_lshl_or_b32 v114, v121, 16, v120\n"
"v_lshl_or_b32 v115, v123, 16, v122\ns_nop 1\nMvvv 176,112,96\nMvvv 180,112,100\nMvvv 184,112,104\n"
"Mvvv 188,112,108\nbuffer_load_ushort v116, v22, s[56:59], s92 offen offset:128\n"
"buffer_load_ushort v117, v22, s[56:59], s92 offen offset:384\n"
"buffer_load_ushort v118, v22, s[56:59], s92 offen offset:640\n"
"buffer_load_ushort v119, v22, s[56:59], s92 offen offset:896\n"
"buffer_load_ushort v120, v23, s[56:59], s92 offen offset:128\n"
"buffer_load_ushort v121, v23, s[56:59], s92 offen offset:384\n"
"buffer_load_ushort v122, v23, s[56:59], s92 offen offset:640\n"
"buffer_load_ushort v123, v23, s[56:59], s92 offen offset:896\ns_waitcnt vmcnt(0)\n"
"v_lshl_or_b32 v112, v117, 16, v116\nv_lshl_or_b32 v113, v119, 16, v118\nv_lshl_or_b32 v114, v121, 16, v120\n"
"v_lshl_or_b32 v115, v123, 16, v122\ns_nop 1\nMvvv 192,112,96\nMvvv 196,112,100\nMvvv 200,112,104\n"
"Mvvv 204,112,108\nbuffer_load_ushort v116, v22, s[56:59], s92 offen offset:160\n"
"buffer_load_ushort v117, v22, s[56:59], s92 offen offset:416\n"
"buffer_load_ushort v118, v22, s[56:59], s92 offen offset:672\n"
"buffer_load_ushort v119, v22, s[56:59], s92 offen offset:928\n"
"buffer_load_ushort v120, v23, s[56:59], s92 offen offset:160\n"
"buffer_load_ushort v121, v23, s[56:59], s92 offen offset:416\n"
"buffer_load_ushort v122, v23, s[56:59], s92 offen offset:672\n"
"buffer_load_ushort v123, v23, s[56:59], s92 offen offset:928\ns_waitcnt vmcnt(0)\n"
"v_lshl_or_b32 v112, v117, 16, v116\nv_lshl_or_b32 v113, v119, 16, v118\nv_lshl_or_b32 v114, v121, 16, v120\n"
"v_lshl_or_b32 v115, v123, 16, v122\ns_nop 1\nMvvv 208,112,96\nMvvv 212,112,100\nMvvv 216,112,104\n"
"Mvvv 220,112,108\nbuffer_load_ushort v116, v22, s[56:59], s92 offen offset:192\n"
"buffer_load_ushort v117, v22, s[56:59], s92 offen offset:448\n"
"buffer_load_ushort v118, v22, s[56:59], s92 offen offset:704\n"
"buffer_load_ushort v119, v22, s[56:59], s92 offen offset:960\n"
"buffer_load_ushort v120, v23, s[56:59], s92 offen offset:192\n"
"buffer_load_ushort v121, v23, s[56:59], s92 offen offset:448\n"
"buffer_load_ushort v122, v23, s[56:59], s92 offen offset:704\n"
"buffer_load_ushort v123, v23, s[56:59], s92 offen offset:960\ns_waitcnt vmcnt(0)\n"
"v_lshl_or_b32 v112, v117, 16, v116\nv_lshl_or_b32 v113, v119, 16, v118\nv_lshl_or_b32 v114, v121, 16, v120\n"
"v_lshl_or_b32 v115, v123, 16, v122\ns_nop 1\nMvvv 224,112,96\nMvvv 228,112,100\nMvvv 232,112,104\n"
"Mvvv 236,112,108\nbuffer_load_ushort v116, v22, s[56:59], s92 offen offset:224\n"
"buffer_load_ushort v117, v22, s[56:59], s92 offen offset:480\n"
"buffer_load_ushort v118, v22, s[56:59], s92 offen offset:736\n"
"buffer_load_ushort v119, v22, s[56:59], s92 offen offset:992\n"
"buffer_load_ushort v120, v23, s[56:59], s92 offen offset:224\n"
"buffer_load_ushort v121, v23, s[56:59], s92 offen offset:480\n"
"buffer_load_ushort v122, v23, s[56:59], s92 offen offset:736\n"
"buffer_load_ushort v123, v23, s[56:59], s92 offen offset:992\ns_waitcnt vmcnt(0)\n"
"v_lshl_or_b32 v112, v117, 16, v116\nv_lshl_or_b32 v113, v119, 16, v118\nv_lshl_or_b32 v114, v121, 16, v120\n"
"v_lshl_or_b32 v115, v123, 16, v122\ns_nop 1\nMvvv 240,112,96\nMvvv 244,112,100\nMvvv 248,112,104\n"
"Mvvv 252,112,108\ns_cmp_lt_u32 s90, 131072\ns_cbranch_scc1 Lfb2_%=\ns_nop 15\nv_accvgpr_read_b32 v16, a240\n"
"v_accvgpr_read_b32 v18, a244\nv_accvgpr_read_b32 v20, a248\nv_accvgpr_read_b32 v22, a252\nv_mov_b32 v96, 0\n"
"v_mov_b32 v97, 0\nv_mov_b32 v98, 0\nv_mov_b32 v99, 0\nv_mov_b32 v100, 0\nv_mov_b32 v101, 0\nv_mov_b32 v102, 0\n"
"v_mov_b32 v103, 0\nv_mov_b32 v104, 0\nv_mov_b32 v105, 0\nv_mov_b32 v106, 0\nv_mov_b32 v107, 0\n"
"v_mov_b32 v108, 0\nv_mov_b32 v109, 0\nv_mov_b32 v110, 0\nv_mov_b32 v111, 0\nv_mov_b32 v112, 0\n"
"v_mov_b32 v113, 0\nv_mov_b32 v114, 0\nv_mov_b32 v115, 0\nv_mov_b32 v116, 0\nv_mov_b32 v117, 0\n"
"v_mov_b32 v118, 0\nv_mov_b32 v119, 0\nv_mov_b32 v120, 0\nv_mov_b32 v121, 0\nv_mov_b32 v122, 0\n"
"v_mov_b32 v123, 0\nv_mov_b32 v124, 0\nv_mov_b32 v125, 0\nv_mov_b32 v126, 0\nv_mov_b32 v127, 0\n"
"s_cmp_eq_u32 s79, 1\ns_cbranch_scc1 Lfbr1_%=\ns_branch Lfbr0_%=\n.if Lw1_%=-Lw1_%= != 0\n"
".error \"page stub misplaced\"\n.endif\n.if Lw2_%=-Lw1_%= != 4096\n.error \"page stub misplaced\"\n.endif\n"
".if Lw3_%=-Lw1_%= != 8192\n.error \"page stub misplaced\"\n.endif\n.if Lw4_%=-Lw1_%= != 12288\n"
".error \"page stub misplaced\"\n.endif\n.if Lw5_%=-Lw1_%= != 16384\n.error \"page stub misplaced\"\n.endif\n"
".if Lw6_%=-Lw1_%= != 20480\n.error \"page stub misplaced\"\n.endif\n.if Lw7_%=-Lw1_%= != 24576\n"
".error \"page stub misplaced\"\n.endif\n.if Lw8_%=-Lw1_%= != 28672\n.error \"page stub misplaced\"\n.endif\n"
".if Lw9_%=-Lw1_%= != 32768\n.error \"page stub misplaced\"\n.endif\n.if Lw10_%=-Lw1_%= != 36864\n"
".error \"page stub misplaced\"\n.endif\n.if Lw11_%=-Lw1_%= != 40960\n.error \"page stub misplaced\"\n.endif\n"
".if Lw12_%=-Lw1_%= != 45056\n.error \"page stub misplaced\"\n.endif\n.if Lw13_%=-Lw1_%= != 49152\n"
".error \"page stub misplaced\"\n.endif\n.if Lw14_%=-Lw1_%= != 53248\n.error \"page stub misplaced\"\n.endif\n"
".if Lw15_%=-Lw1_%= != 57344\n.error \"page stub misplaced\"\n.endif\n.if Lw16_%=-Lw1_%= != 61440\n"
".error \"page stub misplaced\"\n.endif\n.if Lw17_%=-Lw1_%= != 65536\n.error \"page stub misplaced\"\n.endif\n"
".if Lw18_%=-Lw1_%= != 69632\n.error \"page stub misplaced\"\n.endif\n.if Lw19_%=-Lw1_%= != 73728\n"
".error \"page stub misplaced\"\n.endif\n.if Lw21_%=-Lw21_%= != 0\n.error \"page stub misplaced\"\n.endif\n"
".if Lw22_%=-Lw21_%= != 4096\n.error \"page stub misplaced\"\n.endif\n.if Lw23_%=-Lw21_%= != 8192\n"
".error \"page stub misplaced\"\n.endif\n.if Lw24_%=-Lw21_%= != 12288\n.error \"page stub misplaced\"\n.endif\n"
".if Lw25_%=-Lw21_%= != 16384\n.error \"page stub misplaced\"\n.endif\n.if Lw26_%=-Lw21_%= != 20480\n"
".error \"page stub misplaced\"\n.endif\n.if Lw27_%=-Lw21_%= != 24576\n.error \"page stub misplaced\"\n.endif\n"
".if Lw28_%=-Lw21_%= != 28672\n.error \"page stub misplaced\"\n.endif\n.if Lw29_%=-Lw21_%= != 32768\n"
".error \"page stub misplaced\"\n.endif\n.if Lw30_%=-Lw21_%= != 36864\n.error \"page stub misplaced\"\n.endif\n"
".if Lw31_%=-Lw21_%= != 40960\n.error \"page stub misplaced\"\n.endif\n.if Lw32_%=-Lw21_%= != 45056\n"
".error \"page stub misplaced\"\n.endif\n.if Lw33_%=-Lw21_%= != 49152\n.error \"page stub misplaced\"\n.endif\n"
".if Lw34_%=-Lw21_%= != 53248\n.error \"page stub misplaced\"\n.endif\n.if Lw35_%=-Lw21_%= != 57344\n"
".error \"page stub misplaced\"\n.endif\n.if Lw36_%=-Lw21_%= != 61440\n.error \"page stub misplaced\"\n.endif\n"
".if Lw37_%=-Lw21_%= != 65536\n.error \"page stub misplaced\"\n.endif\n.if Lw38_%=-Lw21_%= != 69632\n"
".error \"page stub misplaced\"\n.endif\n.if Lw39_%=-Lw21_%= != 73728\n.error \"page stub misplaced\"\n.endif\n"
  :
  : [ka] "s"(__builtin_amdgcn_kernarg_segment_ptr()), [bid] "s"(blockIdx.x), [wv] "s"(wv)
  : "memory", "vcc", "scc", "v0","v1","v2","v3","v4","v5","v6","v7","v8","v9","v10","v11","v12","v13","v14","v15","v16","v17","v18","v19","v20","v21","v22","v23","v24","v25","v26","v27","v28","v29","v30","v31","v32","v33","v34","v35","v36","v37","v38","v39","v40","v41","v42","v43","v44","v45","v46","v47","v48","v49","v50","v51","v52","v53","v54","v55","v56","v57","v58","v59","v60","v61","v62","v63","v64","v65","v66","v67","v68","v69","v70","v71","v72","v73","v74","v75","v76","v77","v78","v79","v80","v81","v82","v83","v84","v85","v86","v87","v88","v89","v90","v91","v92","v93","v94","v95","v96","v97","v98","v99","v100","v101","v102","v103","v104","v105","v106","v107","v108","v109","v110","v111","v112","v113","v114","v115","v116","v117","v118","v119","v120","v121","v122","v123","v124","v125","v126","v127","v128","v129","v130","v131","v132","v133","v134","v135","v136","v137","v138","v139","v140","v141","v142","v143","v144","v145","v146","v147","v148","v149","v150","v151","v152","v153","v154","v155","v156","v157","v158","v159","v160","v161","v162","v163","v164","v165","v166","v167","v168","v169","v170","v171","v172","v173","v174","v175","v176","v177","v178","v179","v180","v181","v182","v183","v184","v185","v186","v187","v188","v189","v190","v191","v192","v193","v194","v195","v196","v197","v198","v199","v200","v201","v202","v203","v204","v205","v206","v207","v208","v209","v210","v211","v212","v213","v214","v215","v216","v217","v218","v219","v220","v221","v222","v223","v224","v225","v226","v227","v228","v229","v230","v231","v232","v233","v234","v235","v236","v237","v238","v239","v240","v241","v242","v243","v244","v245","v246","v247","v248","v249","v250","v251","v252","v253","v254","v255","a0","a1","a2","a3","a4","a5","a6","a7","a8","a9","a10","a11","a12","a13","a14","a15","a16","a17","a18","a19","a20","a21","a22","a23","a24","a25","a26","a27","a28","a29","a30","a31","a32","a33","a34","a35","a36","a37","a38","a39","a40","a41","a42","a43","a44","a45","a46","a47","a48","a49","a50","a51","a52","a53","a54","a55","a56","a57","a58","a59","a60","a61","a62","a63","a64","a65","a66","a67","a68","a69","a70","a71","a72","a73","a74","a75","a76","a77","a78","a79","a80","a81","a82","a83","a84","a85","a86","a87","a88","a89","a90","a91","a92","a93","a94","a95","a96","a97","a98","a99","a100","a101","a102","a103","a104","a105","a106","a107","a108","a109","a110","a111","a112","a113","a114","a115","a116","a117","a118","a119","a120","a121","a122","a123","a124","a125","a126","a127","a128","a129","a130","a131","a132","a133","a134","a135","a136","a137","a138","a139","a140","a141","a142","a143","a144","a145","a146","a147","a148","a149","a150","a151","a152","a153","a154","a155","a156","a157","a158","a159","a160","a161","a162","a163","a164","a165","a166","a167","a168","a169","a170","a171","a172","a173","a174","a175","a176","a177","a178","a179","a180","a181","a182","a183","a184","a185","a186","a187","a188","a189","a190","a191","a192","a193","a194","a195","a196","a197","a198","a199","a200","a201","a202","a203","a204","a205","a206","a207","a208","a209","a210","a211","a212","a213","a214","a215","a216","a217","a218","a219","a220","a221","a222","a223","a224","a225","a226","a227","a228","a229","a230","a231","a232","a233","a234","a235","a236","a237","a238","a239","a240","a241","a242","a243","a244","a245","a246","a247","a248","a249","a250","a251","a252","a253","a254","a255","s8","s9","s10","s11","s12","s13","s14","s15","s16","s17","s18","s19","s20","s21","s22","s23","s24","s25","s26","s27","s28","s29","s30","s31","s34","s35","s36","s37","s38","s39","s40","s41","s42","s43","s44","s45","s46","s47","s48","s49","s50","s51","s52","s53","s54","s55","s56","s57","s58","s59","s60","s61","s62","s63","s64","s65","s66","s67","s68","s69","s70","s71","s72","s73","s74","s75","s76","s77","s78","s79","s80","s81","s82","s83","s84","s85","s86","s87","s88","s89","s90","s91","s92","s93","s94","s95","s96","s97","s98","s99");
}

extern "C" void kernel_launch(void* const* d_in, const int* in_sizes, int n_in, void* d_out, int out_size, void* d_ws,
                              size_t ws_size, hipStream_t stream) {
  main_kernel<<<256, 256, 0, stream>>>(d_in[0], d_in[1], d_in[2], d_in[3], d_in[4], d_in[6], d_in[7], d_ws, d_out);
}
```
